```python
import torch

def _softmax(x, axis=-1):
    return torch.softmax(x, dim=axis)

def _gelu(x):
    return torch.nn.functional.gelu(x, approximate='tanh')

def _silu(x):
    return torch.nn.functional.silu(x)

def _sigmoid(x):
    return torch.sigmoid(x)

def _layernorm(x, axis=-1, eps=1e-05):
    m = x.mean(dim=axis, keepdim=True)
    v = x.var(dim=axis, keepdim=True, unbiased=False)
    return (x - m) / torch.sqrt(v + eps)

def _scatter_add(tgt, idx, src, axis=0):
    return tgt.clone().index_add_(axis, idx, src)

def _segment_sum(src, seg, n):
    out = torch.zeros((n,) + src.shape[1:], dtype=src.dtype)
    return out.index_add_(0, seg, src)

def _pad(x, pw):
    flat = tuple((v for pair in reversed(pw) for v in pair))
    return torch.nn.functional.pad(x, flat)
kernel_name = 'adaLN_cond__gqa_block_cached__glu_gated_swiglu__prompt'

def setup_inputs():

    _w = lambda a: (lambda t: t.to(torch.bfloat16) if t.is_floating_point() else t)(torch.as_tensor(a))

    def _A_setup():
        B, Sq, D = (1, 2048, 576)
        Hq, Hkv, Sk, Dh = (32, 16, 2048, 128)
        assert Hq % Hkv == 0, (Hq, Hkv)
        return {'x': _w((lambda *s: torch.randn(*s, dtype=torch.bfloat16) * 0.05)(B, Sq, D)), 'k_cache': _w((lambda *s: torch.randn(*s, dtype=torch.bfloat16) * 3.2)(B, Hkv, Sk, Dh)), 'v_cache': _w((lambda *s: torch.randn(*s, dtype=torch.bfloat16) * 1.6)(B, Hkv, Sk, Dh)), 'wq': _w((lambda *s: torch.randn(*s, dtype=torch.bfloat16) * 3.2)(D, Hq * Dh) * D ** (-0.5)), 'wo': _w((lambda *s: torch.randn(*s, dtype=torch.bfloat16) * 1.6)(Hq * Dh, D) * (Hq * Dh) ** (-0.5))}
    _A_in = _A_setup()

    def _B_setup():
        B, S, D, F = (1, 2048, 576, 4608)
        return {'x': _w((lambda *s: torch.randn(*s, dtype=torch.bfloat16) * 0.05)(B, S, D)), 'w1': _w((lambda *s: torch.randn(*s, dtype=torch.bfloat16) * 0.8)(D, F) * (2.0 / D) ** 0.5), 'w3': _w((lambda *s: torch.randn(*s, dtype=torch.bfloat16) * 0.8)(D, F) * (2.0 / D) ** 0.5), 'w2': _w((lambda *s: torch.randn(*s, dtype=torch.bfloat16) * 0.8)(F, D) * (2.0 / F) ** 0.5)}
    _B_in = _B_setup()
    return {'A_x': _A_in['x'], 'A_k_cache': _A_in['k_cache'], 'A_v_cache': _A_in['v_cache'], 'A_wq': _A_in['wq'], 'A_wo': _A_in['wo'], 'B_x': _B_in['x'], 'B_w1': _B_in['w1'], 'B_w3': _B_in['w3'], 'B_w2': _B_in['w2'], 'c': _w((lambda *s: torch.randn(*s, dtype=torch.bfloat16) * 0.2)(1, 576) * 8.0), 'w_mod': _w((lambda *s: torch.randn(*s, dtype=torch.bfloat16) * 0.2)(576, 6 * 576) * 576 ** (-0.5) * 8.0)}

def reference(A_x, A_k_cache, A_v_cache, A_wq, A_wo, B_x, B_w1, B_w3, B_w2, c, w_mod):

    def _A_ref(x, k_cache, v_cache, wq, wo):
        B, Sq, D = x.shape
        Hq, Dh = (32, 128)
        Hkv = k_cache.shape[1]
        rep = Hq // Hkv
        q = (lambda x, *d: x.permute(*d))((x @ wq).reshape(B, Sq, Hq, Dh), 0, 2, 1, 3)
        k = (lambda x, n, axis: torch.repeat_interleave(x, n, dim=axis))(k_cache, rep, axis=1)
        v = (lambda x, n, axis: torch.repeat_interleave(x, n, dim=axis))(v_cache, rep, axis=1)
        scores = torch.einsum('bhqd,bhkd->bhqk', q, k) * Dh ** (-0.5)
        p = _softmax(scores, axis=-1)
        o = torch.einsum('bhqk,bhkd->bhqd', p, v)
        return (lambda x, *d: x.permute(*d))(o, 0, 2, 1, 3).reshape(B, Sq, Hq * Dh) @ wo

    def _B_ref(x, w1, w3, w2):
        gate = torch.einsum('bsd,df->bsf', x, w1)
        up = torch.einsum('bsd,df->bsf', x, w3)
        h = _silu(gate) * up
        out = torch.einsum('bsf,fd->bsd', h, w2)
        return out
    x = A_x
    mod = (_silu(c) @ w_mod).reshape(c.shape[0], 6, 1, -1)
    sa, ca, ga, sb, cb, gb = (mod[:, 0].clone(), mod[:, 1].clone(), mod[:, 2].clone(), mod[:, 3].clone(), mod[:, 4].clone(), mod[:, 5].clone())
    h = _layernorm(x, axis=-1) * (1 + ca) + sa
    x = x + ga * _A_ref(h, A_k_cache, A_v_cache, A_wq, A_wo)
    h = _layernorm(x, axis=-1) * (1 + cb) + sb
    x = x + gb * _B_ref(h, B_w1, B_w3, B_w2)
    return x
```

```cpp
#include <hip/hip_runtime.h>
#include <hip/hip_bf16.h>
#include <stdint.h>
#define MIMIC 1
typedef unsigned short u16;
__device__ __forceinline__ float bf2f(u16 v){ return __uint_as_float(((unsigned)v)<<16); }
__device__ __forceinline__ u16 f2bf(float f){ __bf16 b=(__bf16)f; return __builtin_bit_cast(u16,b); }
__device__ __forceinline__ float rb(float f){ return bf2f(f2bf(f)); }
constexpr int S=2048, DM=576, HQ=32, HKV=16, DH=128, F=4608;

__global__ void k_mod(const u16* c, const u16* w, float* mod){
  int n = blockIdx.x*blockDim.x+threadIdx.x; if(n>=6*DM) return;
  float acc=0;
  for(int k=0;k<DM;k++){ float x=bf2f(c[k]); float s=rb(x/(1.f+__expf(-x))); acc+=s*bf2f(w[k*6*DM+n]); }
  mod[n]=rb(acc);
}
__global__ void k_ln(const float* x, const float* mod, int shift_idx, int scale_idx, u16* h){
  int r=blockIdx.x; int t=threadIdx.x;
  float v[9]; float s=0;
  for(int i=0;i<9;i++){ v[i]=x[r*DM+t+64*i]; s+=v[i]; }
  for(int o=32;o;o>>=1) s+=__shfl_xor(s,o);
  float m=s/DM; float q=0;
  for(int i=0;i<9;i++){ float d=v[i]-m; q+=d*d; }
  for(int o=32;o;o>>=1) q+=__shfl_xor(q,o);
  float rs=rsqrtf(q/DM+1e-5f);
  for(int i=0;i<9;i++){ int cidx=t+64*i; float y=(v[i]-m)*rs; y=y*(1.f+mod[scale_idx*DM+cidx])+mod[shift_idx*DM+cidx]; h[r*DM+cidx]=f2bf(y);}
}
__global__ void k_cvt(const u16* x, float* o, int n){ int i=blockIdx.x*blockDim.x+threadIdx.x; if(i<n) o[i]=bf2f(x[i]); }
__global__ __launch_bounds__(256) void k_gemm(const u16* A, const u16* B, float* C, int M, int N, int K){
  __shared__ float As[16][65], Bs[16][65];
  int tx=threadIdx.x%16, ty=threadIdx.x/16; int m0=blockIdx.y*64, n0=blockIdx.x*64;
  float acc[4][4]={};
  for(int k0=0;k0<K;k0+=16){
    for(int i=threadIdx.x;i<64*16;i+=256){ int r=i/16,c=i%16; As[c][r]=bf2f(A[(size_t)(m0+r)*K+k0+c]); }
    for(int i=threadIdx.x;i<64*16;i+=256){ int r=i/64,c=i%64; Bs[r][c]=bf2f(B[(size_t)(k0+r)*N+n0+c]); }
    __syncthreads();
    for(int k=0;k<16;k++){ float a[4],b[4]; for(int i=0;i<4;i++){a[i]=As[k][ty*4+i]; b[i]=Bs[k][tx*4+i];}
      for(int i=0;i<4;i++)for(int j=0;j<4;j++) acc[i][j]+=a[i]*b[j]; }
    __syncthreads();
  }
  for(int i=0;i<4;i++)for(int j=0;j<4;j++) C[(size_t)(m0+ty*4+i)*N+n0+tx*4+j]=acc[i][j];
}
__global__ __launch_bounds__(256) void k_attn(const float* q, const u16* K, const u16* V, u16* o){
  __shared__ float sc[4][S]; __shared__ float qs[4][DH];
  int w=threadIdx.x/64, l=threadIdx.x%64; int row=blockIdx.x*4+w; int h=blockIdx.y; int kvh=h/2;
  for(int d=l;d<DH;d+=64) qs[w][d]=rb(q[(size_t)row*4096+h*DH+d]);
  __syncthreads();
  const u16* Kh=K+(size_t)kvh*S*DH; const u16* Vh=V+(size_t)kvh*S*DH;
  float mx=-1e30f;
  for(int j=l;j<S;j+=64){ float a=0; const uint4* kp=(const uint4*)(Kh+(size_t)j*DH);
    _Pragma("unroll 2") for(int d8=0;d8<16;d8++){ uint4 u=kp[d8]; unsigned uu[4]={u.x,u.y,u.z,u.w};
      for(int e=0;e<4;e++){ a+=qs[w][d8*8+2*e]*__uint_as_float(uu[e]<<16); a+=qs[w][d8*8+2*e+1]*__uint_as_float(uu[e]&0xffff0000u);} }
    a=rb(rb(a)*rb(0.08838834764831845f));
    sc[w][j]=a; mx=fmaxf(mx,a);}
  for(int of=32;of;of>>=1) mx=fmaxf(mx,__shfl_xor(mx,of));
  float sum=0;
  for(int j=l;j<S;j+=64){ float p=__expf(sc[w][j]-mx); sc[w][j]=p; sum+=p; }
  for(int of=32;of;of>>=1) sum+=__shfl_xor(sum,of);
  float inv=1.f/sum;
  __syncthreads();
  float a0=0,a1=0;
  for(int j=0;j<S;j++){ float p=sc[w][j]*inv;
    p=rb(p);
    unsigned u=*(const unsigned*)(Vh+(size_t)j*DH+2*l); a0+=p*__uint_as_float(u<<16); a1+=p*__uint_as_float(u&0xffff0000u);}
  o[(size_t)row*4096+h*DH+2*l]=f2bf(a0); o[(size_t)row*4096+h*DH+2*l+1]=f2bf(a1);
}
__global__ void k_res(float* x, const float* a, const float* mod, int gidx, u16* out, int n){ int i=blockIdx.x*blockDim.x+threadIdx.x; if(i<n){ float v=x[i]+mod[gidx*DM+i%DM]*a[i]; x[i]=rb(v); if(out) out[i]=f2bf(v);} }
__global__ void k_swiglu(const float* g, const float* u, u16* o, int n){ int i=blockIdx.x*blockDim.x+threadIdx.x; if(i<n){ float x=g[i]; o[i]=f2bf(x/(1.f+__expf(-x))*u[i]); } }

extern "C" void kernel_launch(void* const* d_in, const int* in_sizes, int n_in, void* d_out, int out_size, void* d_ws, size_t ws_size, hipStream_t stream){
  const u16* x=(const u16*)d_in[0]; const u16* K=(const u16*)d_in[1]; const u16* V=(const u16*)d_in[2];
  const u16* wq=(const u16*)d_in[3]; const u16* wo=(const u16*)d_in[4];
  const u16* w1=(const u16*)d_in[6]; const u16* w3=(const u16*)d_in[7]; const u16* w2=(const u16*)d_in[8];
  const u16* c=(const u16*)d_in[9]; const u16* wm=(const u16*)d_in[10];
  char* ws=(char*)d_ws;
  float* mod=(float*)ws; ws+=16384;
  float* xf=(float*)ws; ws+=S*DM*4;
  u16* h=(u16*)ws; ws+=S*DM*2;
  float* q=(float*)ws; ws+=(size_t)S*4096*4;
  u16* o=(u16*)ws; ws+=(size_t)S*4096*2;
  float* a=(float*)ws; ws+=S*DM*4;
  float* g=(float*)ws; ws+=(size_t)S*F*4;
  float* u=(float*)ws; ws+=(size_t)S*F*4;
  u16* act=(u16*)ws; ws+=(size_t)S*F*2;
  k_mod<<<(6*DM+63)/64,64,0,stream>>>(c,wm,mod);
  k_cvt<<<S*DM/256,256,0,stream>>>(x,xf,S*DM);
  k_ln<<<S,64,0,stream>>>(xf,mod,0,1,h);
  k_gemm<<<dim3(4096/64,S/64),256,0,stream>>>(h,wq,q,S,4096,DM);
  k_attn<<<dim3(S/4,HQ),256,0,stream>>>(q,K,V,o);
  k_gemm<<<dim3(DM/64,S/64),256,0,stream>>>(o,wo,a,S,DM,4096);
  k_res<<<S*DM/256,256,0,stream>>>(xf,a,mod,2,nullptr,S*DM);
  k_ln<<<S,64,0,stream>>>(xf,mod,3,4,h);
  k_gemm<<<dim3(F/64,S/64),256,0,stream>>>(h,w1,g,S,F,DM);
  k_gemm<<<dim3(F/64,S/64),256,0,stream>>>(h,w3,u,S,F,DM);
  k_swiglu<<<S*F/256,256,0,stream>>>(g,u,act,S*F);
  k_gemm<<<dim3(DM/64,S/64),256,0,stream>>>(act,w2,a,S,DM,F);
  k_res<<<S*DM/256,256,0,stream>>>(xf,a,mod,5,(u16*)d_out,S*DM);
}
```

```cpp
#include <hip/hip_runtime.h>
__global__ __launch_bounds__(256, 1) __attribute__((aligned(4096))) void k_main(const void* x, const void* kc, const void* vc, const void* wq, const void* wo, const void* w1, const void* w3,
    const void* w2, const void* c, const void* wm, void* out, void* ws) {
  __shared__ __attribute__((aligned(16))) char lds[163840];
  const unsigned wid = __builtin_amdgcn_readfirstlane(threadIdx.x >> 6);
  const unsigned bid = blockIdx.x;
  asm volatile(
".macro xM a:vararg\nv_mfma_f32_16x16x32_bf16 \\a\n.endm\n"
".macro xT a:vararg\nds_read_b64_tr_b16 \\a\n.endm\n"
".macro xR a:vararg\nds_read_b128 \\a\n.endm\n"
".macro xL a:vararg\nbuffer_load_dwordx4 \\a\n.endm\n"
".macro xC a:vararg\nv_cvt_pk_bf16_f32 \\a\n.endm\n"
".macro xF a:vararg\nv_fma_mix_f32 \\a\n.endm\n"
".macro xW a:vararg\ns_waitcnt \\a\n.endm\n"
".macro xE a:vararg\nv_exp_f32 \\a\n.endm\n"
".macro xA a:vararg\ns_add_u32 \\a\n.endm\n"
".macro xP a:vararg\nv_accvgpr_write_b32 \\a\n.endm\n"
".macro xS a:vararg\nbuffer_store_dwordx4 \\a\n.endm\n"
".macro xU a:vararg\nv_mul_f32 \\a\n.endm\n"
".macro xX a:vararg\nv_max3_f32 \\a\n.endm\n"
"s_nop 0\n.p2align 6\nCSTART_%=:\ns_load_dwordx8 s[72:79], %[ka], 0\ns_load_dwordx8 s[80:87], %[ka], 32\ns_load_dwordx8 s[88:95], %[ka], 64\n"
"v_mbcnt_lo_u32_b32 v255, -1, 0\nv_mbcnt_hi_u32_b32 v255, -1, v255\ns_getpc_b64 s[28:29]\nPCPSTART_%=:\nxA s28,s28,PSTART_%=-PCPSTART_%=\ns_addc_u32 s29, s29, 0\n"
"s_and_b32 s29, s29, 0xffff\ns_mov_b32 s30, OSTART_%=-PSTART_%=\ns_mov_b32 s31, 0x20000\nv_lshlrev_b32 v252, 7, v255\ns_mul_i32 s38, %[wid], 16384\nbuffer_load_dword a255, v252, s[28:31], s38 offen\n"
"xA s38,s38,8192\nbuffer_load_dword a255, v252, s[28:31], s38 offen\nxA s38,s38,8192\nPSTART_%=:\ns_cmp_eq_u32 %[wid], 0\ns_cselect_b64 s[100:101], -1, 0\n"
"s_and_b32 s70, %[bid], 7\ns_lshr_b32 s71, %[bid], 3\ns_and_b32 s39, s70, 7\ns_lshl_b32 s39, s39, 1\ns_lshr_b32 s70, s70, 3\ns_mul_i32 s70, s70, 16\n"
"s_lshr_b32 s38, s71, 1\nxA s70,s70,s38\ns_and_b32 s71, s71, 1\nxA s71,s71,s39\ns_lshl_b32 s36, %[wid], 12\ns_mov_b32 s40, 0x0\n"
"s_mov_b32 s41, 0x42c80000\nv_lshrrev_b32 v237, 4, v255\nv_and_b32 v238, 15, v255\nv_bfe_u32 v239, v255, 2, 2\nv_and_b32 v240, 3, v255\nv_bfe_u32 v241, v255, 2, 3\n"
"v_lshlrev_b32 v251, 4, v255\nv_lshrrev_b32 v242, 5, v255\nv_lshlrev_b32 v242, 6, v242\nv_lshl_add_u32 v242, v240, 4, v242\nv_lshrrev_b32 v247, 2, v241\nv_lshl_add_u32 v247, v247, 4, v239\n"
"v_lshl_add_u32 v247, v247, 13, v242\ns_lshl_b32 s47, s70, 2\nxA s47,s47,%[wid]\ns_mul_i32 s47, s47, 36864\nxA s47,s47,3145728\ns_and_b32 s39, %[wid], 1\n"
"s_mul_i32 s42, s39, 49160\ns_lshl_b32 s43, s39, 16\ns_lshr_b32 s39, %[wid], 1\ns_lshl_b32 s39, s39, 12\nxA s42,s42,s39\nxA s42,s42,49152\n"
"s_lshl_b32 s39, s39, 3\nxA s43,s43,s39\nxW lgkmcnt(0)\nxA s50,s94,1277952\ns_addc_u32 s51, s95, 0\ns_load_dwordx2 s[68:69], s[50:51], 0\n"
"xA s96,s94,0\ns_addc_u32 s97, s95, 0\ns_and_b32 s97, s97, 0xffff\ns_mov_b32 s98, 110624768\ns_mov_b32 s99, 0x20000\ns_lshl_b32 s39, s71, 9\n"
"xA s20,s78,s39\ns_addc_u32 s21, s79, 0\ns_and_b32 s21, s21, 0xffff\ns_mov_b32 s22, 4713600\ns_mov_b32 s23, 0x20000\ns_sub_u32 s20, s20, 2688\n"
"s_subb_u32 s21, s21, 0\nv_add_u32 v235, 2688, v247\nv_add_u32 v236, 1792, v247\nv_add_u32 v252, 896, v247\nv_lshrrev_b32 v177, 3, v255\nv_and_b32 v178, 7, v255\n"
"s_lshl_b32 s49, s70, 4\nxA s49,s49,s71\ns_mul_i32 s39, s49, 9216\nxA s28,s72,s39\ns_addc_u32 s29, s73, 0\ns_and_b32 s29, s29, 0xffff\n"
"s_mov_b32 s30, 9216\ns_mov_b32 s31, 0x20000\nv_mul_u32_u24 v176, 1152, v178\nv_lshl_add_u32 v176, v177, 4, v176\ns_mul_i32 s38, %[wid], 288\nxL v[0:3],v176,s[28:31],s38 offen offset:0\n"
"xL v[4:7],v176,s[28:31],s38 offen offset:128\nxL v[8:11],v176,s[28:31],s38 offen offset:256\ns_lshl_b32 s39, %[wid], 3\nv_add_u32 v189, s39, v177\nv_mul_u32_u24 v188, 6912, v189\nv_lshl_add_u32 v188, v178, 4, v188\n"
"v_lshlrev_b32 v189, 1, v189\ns_and_b32 s37, %[bid], 3\ns_mul_i32 s39, s37, 288\nxA s56,s88,s39\ns_addc_u32 s57, s89, 0\ns_and_b32 s57, s57, 0xffff\n"
"s_mov_b32 s58, 288\ns_mov_b32 s59, 0x20000\nbuffer_load_ushort v40, v189, s[56:59], 0 offen offset:0\nbuffer_load_ushort v41, v189, s[56:59], 0 offen offset:64\nbuffer_load_ushort v42, v189, s[56:59], 0 offen offset:128\nbuffer_load_ushort v43, v189, s[56:59], 0 offen offset:192\n"
"buffer_load_ushort v44, v189, s[56:59], 0 offen offset:256\ns_mul_i32 s39, s37, 995328\ns_lshr_b32 s38, %[bid], 2\ns_lshl_b32 s38, s38, 7\nxA s39,s39,s38\nxA s56,s90,s39\n"
"s_addc_u32 s57, s91, 0\ns_and_b32 s57, s57, 0xffff\ns_mov_b32 s58, 988544\ns_mov_b32 s59, 0x20000\ns_cmp_lt_u32 %[bid], 216\ns_cselect_b32 s58, s58, 0\n"
"s_mov_b32 s38, 0\nxL v[16:19],v188,s[56:59],s38 offen\ns_mov_b32 s38, 221184\nxL v[20:23],v188,s[56:59],s38 offen\ns_mov_b32 s38, 442368\nxL v[24:27],v188,s[56:59],s38 offen\n"
"s_mov_b32 s38, 663552\nxL v[28:31],v188,s[56:59],s38 offen\ns_mov_b32 s38, 884736\nxL v[32:35],v188,s[56:59],s38 offen\ns_and_b32 s35, s70, 15\ns_lshl_b32 s35, s35, 2\n"
"xA s35,s35,%[wid]\nv_lshrrev_b32 v202, 2, v255\nv_lshlrev_b32 v202, 13, v202\nv_and_b32 v183, 3, v255\nv_lshl_add_u32 v202, v183, 7, v202\nv_lshlrev_b32 v203, 7, v255\n"
"s_lshl_b32 s39, %[bid], 2\nxA s39,s39,%[wid]\ns_lshl_b32 s39, s39, 8\nxA s39,s39,1130496\ns_mov_b64 exec, 3\nbuffer_load_dword a255, v203, s[96:99], s39 offen sc1\n"
"s_mov_b64 exec, -1\ns_mul_i32 s39, s35, 24576\nxA s39,s39,2688\ns_mov_b64 exec, 0xfff\nbuffer_load_dword a255, v202, s[20:23], s39 offen\ns_mov_b64 exec, -1\n"
"v_cmp_gt_u32 vcc, 16, v255\nv_lshlrev_b32 v187, 3, v178\nv_add_u32 v187, 160256, v187\nv_cndmask_b32 v179, 0, 1.0, vcc\nv_lshlrev_b32 v186, 8, v177\nv_lshl_add_u32 v186, v178, 4, v186\n"
"s_lshl_b32 s39, %[wid], 6\nv_add_u32 v200, s39, v187\nv_lshrrev_b32 v201, 4, v255\nv_lshlrev_b32 v201, 3, v201\nv_lshl_add_u32 v201, v178, 5, v201\ns_lshl_b32 s39, %[wid], 8\n"
"xA s39,s39,160512\nv_add_u32 v201, s39, v201\nv_lshlrev_b32 v204, 2, v255\ns_lshr_b32 s37, s49, 1\ns_mul_i32 s37, s37, 18432\ns_mul_i32 s39, %[wid], 4608\n"
"xA s37,s37,s39\ns_and_b32 s39, s71, 1\ns_lshl_b32 s39, s39, 7\nxA s37,s37,s39\nxA s37,s37,3145728\nxW lgkmcnt(0)\n"
"xA s68,s68,1\nv_mov_b32 v198, s68\nv_mov_b32 v199, s69\ns_lshl_b32 s51, s70, 9\nxA s51,s51,1204224\ns_lshl_b32 s39, s71, 2\n"
"s_lshl_b64 s[54:55], 15, s39\nxA s39,s39,%[wid]\ns_lshl_b32 s39, s39, 3\nxA s50,s51,s39\nv_lshlrev_b32 v196, 3, v255\nv_and_b32 v197, 7, v255\n"
"v_add_u32 v197, 64, v197\ns_sub_u32 s39, %[bid], 216\ns_cmp_lt_u32 s39, 36\ns_cselect_b32 s61, 1, 0\ns_lshl_b32 s39, s39, 2\nxA s39,s39,72\n"
"s_cmp_eq_u32 s61, 1\ns_cselect_b32 s39, s39, 64\ns_mov_b32 exec_lo, 0\nv_add_u32 v197, s39, v183\ns_mov_b32 exec_lo, -1\nv_lshlrev_b32 v197, 3, v197\n"
"s_lshl_b32 s53, s70, 11\nxA s53,s53,1212416\ns_and_b32 s62, %[bid], 3\ns_mul_i32 s62, s62, 13824\ns_lshr_b32 s39, %[bid], 2\ns_lshl_b32 s39, s39, 8\n"
"xA s62,s62,s39\nxA s62,s62,1064960\ns_lshl_b32 s63, %[bid], 3\nxA s63,s63,1212416\ns_lshl_b32 s39, s71, 19\nxA s12,s74,s39\n"
"s_addc_u32 s13, s75, 0\ns_and_b32 s13, s13, 0xffff\ns_mov_b32 s14, 524288\ns_mov_b32 s15, 0x20000\nxA s16,s76,s39\ns_addc_u32 s17, s77, 0\n"
"s_and_b32 s17, s17, 0xffff\ns_mov_b32 s18, 524288\ns_mov_b32 s19, 0x20000\ns_mul_i32 s39, s71, 294912\nxA s24,s80,s39\ns_addc_u32 s25, s81, 0\n"
"s_and_b32 s25, s25, 0xffff\ns_mov_b32 s26, 294912\ns_mov_b32 s27, 0x20000\ns_lshl_b32 s46, s70, 2\nxA s46,s46,%[wid]\ns_mul_i32 s46, s46, 589824\n"
"s_mul_i32 s38, s71, 9216\nxA s46,s46,s38\nxA s46,s46,7864320\nv_add_u32 v242, 0, v237\nv_xor_b32 v242, v242, v238\nv_lshlrev_b32 v242, 4, v242\n"
"v_lshl_add_u32 v224, v238, 8, v242\nv_lshl_add_u32 v230, v237, 8, v242\nv_add_u32 v242, 4, v237\nv_xor_b32 v242, v242, v238\nv_lshlrev_b32 v242, 4, v242\nv_lshl_add_u32 v225, v238, 8, v242\n"
"v_lshl_add_u32 v231, v237, 8, v242\nv_add_u32 v242, 8, v237\nv_xor_b32 v242, v242, v238\nv_lshlrev_b32 v242, 4, v242\nv_lshl_add_u32 v226, v238, 8, v242\nv_lshl_add_u32 v232, v237, 8, v242\n"
"v_add_u32 v242, 12, v237\nv_xor_b32 v242, v242, v238\nv_lshlrev_b32 v242, 4, v242\nv_lshl_add_u32 v227, v238, 8, v242\nv_lshl_add_u32 v233, v237, 8, v242\nv_and_b32 v242, 1, v237\n"
"v_lshrrev_b32 v243, 1, v237\nv_lshl_add_u32 v244, v242, 2, v239\nv_lshlrev_b32 v244, 6, v244\nv_lshl_add_u32 v244, v240, 3, v244\nv_lshl_add_u32 v244, v242, 5, v244\nv_lshl_add_u32 v228, v243, 11, v244\n"
"v_add_u32 v228, 32768, v228\nv_xor_b32 v229, 32, v228\nv_lshl_add_u32 v246, v243, 2, v239\nv_lshlrev_b32 v246, 6, v246\nv_lshl_add_u32 v246, v240, 4, v246\nv_mul_u32_u24 v242, 49160, v242\n"
"s_mov_b32 s39, 49152\nv_add3_u32 v246, v246, v242, s39\nv_lshrrev_b32 v242, 2, v241\nv_lshlrev_b32 v242, 1, v242\nv_xor_b32 v242, v242, v240\nv_lshlrev_b32 v242, 4, v242\n"
"v_lshrrev_b32 v243, 5, v255\nv_lshl_add_u32 v242, v243, 6, v242\nv_lshl_add_u32 v234, v241, 8, v242\nv_mul_u32_u24 v248, 1152, v241\nv_add_u32 v248, v248, v242\nv_lshrrev_b32 v242, 3, v238\n"
"v_mul_u32_u24 v242, 147456, v242\nv_lshl_add_u32 v242, v237, 7, v242\nv_and_b32 v243, 7, v255\nv_lshl_add_u32 v249, v243, 4, v242\nv_add_u32 v250, 294912, v249\nv_lshlrev_b32 v245, 5, v237\n"
"v_add_u32 v245, 148480, v245\nv_mov_b32 v242, 0x3f803f80\nxP a192,v242\nxP a193,v242\nxP a194,v242\nxP a195,v242\n"
"v_and_b32 v242, 1, v255\nv_lshlrev_b32 v242, 4, v242\nv_mov_b32 v243, 0x3f80\nv_lshlrev_b32 v242, v242, v243\nv_lshrrev_b32 v243, 1, v238\nv_cmp_eq_u32 vcc, 0, v243\n"
"s_nop 1\nv_cndmask_b32 v244, 0, v242, vcc\nxP a196,v244\nv_cmp_eq_u32 vcc, 1, v243\ns_nop 1\nv_cndmask_b32 v244, 0, v242, vcc\n"
"xP a197,v244\nv_cmp_eq_u32 vcc, 2, v243\ns_nop 1\nv_cndmask_b32 v244, 0, v242, vcc\nxP a198,v244\nv_cmp_eq_u32 vcc, 3, v243\n"
"s_nop 1\nv_cndmask_b32 v244, 0, v242, vcc\nxP a199,v244\nv_cmp_eq_u32 vcc, 4, v243\ns_nop 1\nv_cndmask_b32 v244, 0, v242, vcc\n"
"xP a200,v244\nv_cmp_eq_u32 vcc, 5, v243\ns_nop 1\nv_cndmask_b32 v244, 0, v242, vcc\nxP a201,v244\nv_cmp_eq_u32 vcc, 6, v243\n"
"s_nop 1\nv_cndmask_b32 v244, 0, v242, vcc\nxP a202,v244\nv_cmp_eq_u32 vcc, 7, v243\ns_nop 1\nv_cndmask_b32 v244, 0, v242, vcc\n"
"xP a203,v244\ns_mov_b32 s32, s36\ns_mov_b32 s33, s36\nxA s34,s36,128\ns_lshl_b32 s44, %[wid], 13\ns_mul_i32 s45, %[wid], 73728\n"
"s_mov_b32 s56, s80\ns_mov_b32 s57, s81\ns_cmp_eq_u32 %[wid], 1\ns_cselect_b32 s56, s82, s56\ns_cselect_b32 s57, s83, s57\ns_cmp_eq_u32 %[wid], 2\n"
"s_cselect_b32 s56, s84, s56\ns_cselect_b32 s57, s85, s57\ns_cmp_eq_u32 %[wid], 3\ns_cselect_b32 s56, s86, s56\ns_cselect_b32 s57, s87, s57\ns_and_b32 s57, s57, 0xffff\n"
"s_cmp_eq_u32 %[wid], 0\ns_mov_b32 s58, 5308416\ns_cselect_b32 s58, 4718592, s58\ns_mov_b32 s59, 0x20000\ns_mul_i32 s48, %[bid], 21760\ns_mov_b32 s46, 2176\n"
"v_lshlrev_b32 v253, 7, v255\ns_lshl_b32 s39, s70, 1\ns_and_b32 s38, s71, 1\nxA s39,s39,s38\ns_lshr_b32 s38, s71, 1\ns_sub_u32 s52, %[wid], 1\n"
"s_cmp_lt_u32 s52, 2\ns_cbranch_scc1 LPA1_%=\ns_lshl_b32 s38, s38, 5\nxA s39,s39,s38\ns_cmp_eq_u32 %[wid], 0\ns_cselect_b32 s38, 16, 18\n"
"s_mul_i32 s39, s39, s38\ns_mul_i32 s48, s39, 1152\ns_mov_b32 s46, 2304\ns_branch LPB1_%=\nLPA1_%=:\ns_mul_i32 s39, s39, 165888\n"
"s_mul_i32 s38, s38, 1152\nxA s48,s39,s38\ns_mov_b32 s46, 18432\nv_cmp_lt_u32 vcc, 8, v255\nv_add_u32 v244, 8064, v253\ns_nop 0\n"
"v_cndmask_b32 v253, v253, v244, vcc\nLPB1_%=:\nv_mov_b32 v244, 0x40000000\nv_cmp_gt_u32 vcc, 18, v255\ns_nop 1\nv_cndmask_b32 v253, v244, v253, vcc\n"
"v_lshrrev_b32 v182, 4, v255\nv_mul_u32_u24 v183, 9216, v182\nv_and_b32 v181, 15, v255\nv_lshl_add_u32 v183, v181, 4, v183\ns_lshl_b32 s39, %[wid], 8\nv_add_u32 v183, s39, v183\n"
"v_lshrrev_b32 v181, 5, v255\nv_lshlrev_b32 v181, 6, v181\nv_and_b32 v182, 1, v182\nv_lshl_add_u32 v181, v182, 4, v181\nv_bfe_u32 v182, v255, 3, 1\nv_lshl_add_u32 v181, v182, 2, v181\n"
"s_lshr_b32 s39, %[wid], 1\ns_lshl_b32 s39, s39, 5\ns_and_b32 s38, %[wid], 1\ns_lshl_b32 s38, s38, 3\nxA s39,s39,s38\nv_add_u32 v181, s39, v181\n"
"v_lshlrev_b32 v183, 2, v181\nv_and_b32 v182, 7, v255\nv_mul_u32_u24 v182, 1152, v182\nv_lshl_add_u32 v176, v181, 1, v182\nxP a218,v176\nxW vmcnt(12)\n"
"v_lshlrev_b32 v64, 16, v0\nv_and_b32 v65, 0xffff0000, v0\nv_lshlrev_b32 v66, 16, v1\nv_and_b32 v67, 0xffff0000, v1\nv_lshlrev_b32 v68, 16, v2\nv_and_b32 v69, 0xffff0000, v2\n"
"v_lshlrev_b32 v70, 16, v3\nv_and_b32 v71, 0xffff0000, v3\nv_lshlrev_b32 v72, 16, v4\nxA m0,s42,0\nxA s39,s43,0\nxL v235,s[20:23],s39 offen offset:0 lds\n"
"v_and_b32 v73, 0xffff0000, v4\nv_lshlrev_b32 v74, 16, v5\nv_and_b32 v75, 0xffff0000, v5\nv_lshlrev_b32 v76, 16, v6\nv_and_b32 v77, 0xffff0000, v6\nv_lshlrev_b32 v78, 16, v7\n"
"v_and_b32 v79, 0xffff0000, v7\nv_lshlrev_b32 v80, 16, v8\nv_and_b32 v81, 0xffff0000, v8\nxL v236,s[20:23],s39 offen offset:1024 lds\nv_lshlrev_b32 v82, 16, v9\nv_and_b32 v83, 0xffff0000, v9\n"
"v_lshlrev_b32 v84, 16, v10\nv_and_b32 v85, 0xffff0000, v10\nv_lshlrev_b32 v86, 16, v11\nv_and_b32 v87, 0xffff0000, v11\nv_pk_mul_f32 v[80:81], v[80:81], v[178:179] op_sel:[0,1] op_sel_hi:[1,1]\nv_pk_mul_f32 v[82:83], v[82:83], v[178:179] op_sel:[0,1] op_sel_hi:[1,1]\n"
"v_pk_mul_f32 v[84:85], v[84:85], v[178:179] op_sel:[0,1] op_sel_hi:[1,1]\nxL v252,s[20:23],s39 offen offset:2048 lds\nv_pk_mul_f32 v[86:87], v[86:87], v[178:179] op_sel:[0,1] op_sel_hi:[1,1]\nv_pk_add_f32 v[152:153], v[64:65], v[66:67]\nv_pk_add_f32 v[152:153], v[152:153], v[68:69]\nv_pk_add_f32 v[152:153], v[152:153], v[70:71]\n"
"v_pk_add_f32 v[152:153], v[152:153], v[72:73]\nv_pk_add_f32 v[152:153], v[152:153], v[74:75]\nv_pk_add_f32 v[152:153], v[152:153], v[76:77]\nv_pk_add_f32 v[152:153], v[152:153], v[78:79]\nv_pk_add_f32 v[152:153], v[152:153], v[80:81]\nxL v247,s[20:23],s39 offen offset:3072 lds\n"
"v_pk_add_f32 v[152:153], v[152:153], v[82:83]\nv_pk_add_f32 v[152:153], v[152:153], v[84:85]\nv_pk_add_f32 v[152:153], v[152:153], v[86:87]\nv_add_f32 v180, v152, v153\ns_nop 1\nv_add_f32_dpp v180, v180, v180 row_ror:8 row_mask:0xf bank_mask:0xf\n"
"v_mov_b32 v182, v180\ns_nop 1\nv_permlane16_swap_b32 v180, v182\nv_add_f32 v180, v180, v182\nv_mov_b32 v182, v180\nxA m0,s42,8192\n"
"xA s39,s43,262144\nxL v235,s[20:23],s39 offen offset:0 lds\ns_nop 1\nv_permlane32_swap_b32 v180, v182\nv_add_f32 v180, v180, v182\nxU v180,0x3be38e39,v180\n"
"v_pk_add_f32 v[128:129], v[64:65], v[180:181] op_sel:[0,0] op_sel_hi:[1,0] neg_lo:[0,1] neg_hi:[0,1]\nv_pk_add_f32 v[130:131], v[66:67], v[180:181] op_sel:[0,0] op_sel_hi:[1,0] neg_lo:[0,1] neg_hi:[0,1]\nv_pk_add_f32 v[132:133], v[68:69], v[180:181] op_sel:[0,0] op_sel_hi:[1,0] neg_lo:[0,1] neg_hi:[0,1]\nv_pk_add_f32 v[134:135], v[70:71], v[180:181] op_sel:[0,0] op_sel_hi:[1,0] neg_lo:[0,1] neg_hi:[0,1]\nv_pk_add_f32 v[136:137], v[72:73], v[180:181] op_sel:[0,0] op_sel_hi:[1,0] neg_lo:[0,1] neg_hi:[0,1]\nv_pk_add_f32 v[138:139], v[74:75], v[180:181] op_sel:[0,0] op_sel_hi:[1,0] neg_lo:[0,1] neg_hi:[0,1]\n"
"xL v236,s[20:23],s39 offen offset:1024 lds\nv_pk_add_f32 v[140:141], v[76:77], v[180:181] op_sel:[0,0] op_sel_hi:[1,0] neg_lo:[0,1] neg_hi:[0,1]\nv_pk_add_f32 v[142:143], v[78:79], v[180:181] op_sel:[0,0] op_sel_hi:[1,0] neg_lo:[0,1] neg_hi:[0,1]\nv_pk_add_f32 v[144:145], v[80:81], v[180:181] op_sel:[0,0] op_sel_hi:[1,0] neg_lo:[0,1] neg_hi:[0,1]\nv_pk_add_f32 v[146:147], v[82:83], v[180:181] op_sel:[0,0] op_sel_hi:[1,0] neg_lo:[0,1] neg_hi:[0,1]\nv_pk_add_f32 v[148:149], v[84:85], v[180:181] op_sel:[0,0] op_sel_hi:[1,0] neg_lo:[0,1] neg_hi:[0,1]\n"
"v_pk_add_f32 v[150:151], v[86:87], v[180:181] op_sel:[0,0] op_sel_hi:[1,0] neg_lo:[0,1] neg_hi:[0,1]\nv_pk_mul_f32 v[144:145], v[144:145], v[178:179] op_sel:[0,1] op_sel_hi:[1,1]\nv_pk_mul_f32 v[146:147], v[146:147], v[178:179] op_sel:[0,1] op_sel_hi:[1,1]\nv_pk_mul_f32 v[148:149], v[148:149], v[178:179] op_sel:[0,1] op_sel_hi:[1,1]\nxL v252,s[20:23],s39 offen offset:2048 lds\nv_pk_mul_f32 v[150:151], v[150:151], v[178:179] op_sel:[0,1] op_sel_hi:[1,1]\n"
"v_pk_mul_f32 v[152:153], v[128:129], v[128:129]\nv_pk_fma_f32 v[152:153], v[130:131], v[130:131], v[152:153]\nv_pk_fma_f32 v[152:153], v[132:133], v[132:133], v[152:153]\nv_pk_fma_f32 v[152:153], v[134:135], v[134:135], v[152:153]\nv_pk_fma_f32 v[152:153], v[136:137], v[136:137], v[152:153]\nv_pk_fma_f32 v[152:153], v[138:139], v[138:139], v[152:153]\n"
"v_pk_fma_f32 v[152:153], v[140:141], v[140:141], v[152:153]\nv_pk_fma_f32 v[152:153], v[142:143], v[142:143], v[152:153]\nxL v247,s[20:23],s39 offen offset:3072 lds\nv_pk_fma_f32 v[152:153], v[144:145], v[144:145], v[152:153]\nv_pk_fma_f32 v[152:153], v[146:147], v[146:147], v[152:153]\nv_pk_fma_f32 v[152:153], v[148:149], v[148:149], v[152:153]\n"
"v_pk_fma_f32 v[152:153], v[150:151], v[150:151], v[152:153]\nv_add_f32 v181, v152, v153\ns_nop 1\nv_add_f32_dpp v181, v181, v181 row_ror:8 row_mask:0xf bank_mask:0xf\nv_mov_b32 v182, v181\ns_nop 1\n"
"v_permlane16_swap_b32 v181, v182\nv_add_f32 v181, v181, v182\nv_mov_b32 v182, v181\ns_nop 1\nv_permlane32_swap_b32 v181, v182\nv_add_f32 v181, v181, v182\n"
"xW vmcnt(15)\nv_lshlrev_b32 v40, 16, v40\nxU v96,0xbfb8aa3b,v40\nv_lshlrev_b32 v41, 16, v41\nxU v97,0xbfb8aa3b,v41\nv_lshlrev_b32 v42, 16, v42\n"
"xU v98,0xbfb8aa3b,v42\nv_lshlrev_b32 v43, 16, v43\nxU v99,0xbfb8aa3b,v43\nv_lshlrev_b32 v44, 16, v44\nxU v100,0xbfb8aa3b,v44\nxE v96,v96\n"
"xE v97,v97\nxE v98,v98\nxE v99,v99\nxE v100,v100\nv_add_f32 v96, 1.0, v96\nv_add_f32 v97, 1.0, v97\n"
"v_add_f32 v98, 1.0, v98\nv_add_f32 v99, 1.0, v99\nv_add_f32 v100, 1.0, v100\nv_rcp_f32 v96, v96\nv_rcp_f32 v97, v97\nv_rcp_f32 v98, v98\n"
"v_rcp_f32 v99, v99\nv_rcp_f32 v100, v100\nxU v40,v40,v96\nxU v41,v41,v97\nxU v42,v42,v98\nxU v43,v43,v99\n"
"xU v44,v44,v100\nxC v48,v40,0\nxC v49,0,v40\nxC v50,v41,0\nxC v51,0,v41\nxC v52,v42,0\n"
"xC v53,0,v42\nxC v54,v43,0\nxC v55,0,v43\nxC v56,v44,0\nxC v57,0,v44\nxW vmcnt(10)\n"
"v_dot2_f32_bf16 v88, v16, v48, 0\nv_dot2_f32_bf16 v89, v16, v49, 0\nv_dot2_f32_bf16 v90, v17, v48, 0\nv_dot2_f32_bf16 v91, v17, v49, 0\nv_dot2_f32_bf16 v92, v18, v48, 0\nv_dot2_f32_bf16 v93, v18, v49, 0\n"
"v_dot2_f32_bf16 v94, v19, v48, 0\nv_dot2_f32_bf16 v95, v19, v49, 0\nv_dot2_f32_bf16 v88, v20, v50, v88\nv_dot2_f32_bf16 v89, v20, v51, v89\nv_dot2_f32_bf16 v90, v21, v50, v90\nv_dot2_f32_bf16 v91, v21, v51, v91\n"
"v_dot2_f32_bf16 v92, v22, v50, v92\nv_dot2_f32_bf16 v93, v22, v51, v93\nv_dot2_f32_bf16 v94, v23, v50, v94\nv_dot2_f32_bf16 v95, v23, v51, v95\nv_dot2_f32_bf16 v88, v24, v52, v88\nv_dot2_f32_bf16 v89, v24, v53, v89\n"
"v_dot2_f32_bf16 v90, v25, v52, v90\nv_dot2_f32_bf16 v91, v25, v53, v91\nv_dot2_f32_bf16 v92, v26, v52, v92\nv_dot2_f32_bf16 v93, v26, v53, v93\nv_dot2_f32_bf16 v94, v27, v52, v94\nv_dot2_f32_bf16 v95, v27, v53, v95\n"
"v_dot2_f32_bf16 v88, v28, v54, v88\nv_dot2_f32_bf16 v89, v28, v55, v89\nv_dot2_f32_bf16 v90, v29, v54, v90\nv_dot2_f32_bf16 v91, v29, v55, v91\nv_dot2_f32_bf16 v92, v30, v54, v92\nv_dot2_f32_bf16 v93, v30, v55, v93\n"
"v_dot2_f32_bf16 v94, v31, v54, v94\nv_dot2_f32_bf16 v95, v31, v55, v95\nv_dot2_f32_bf16 v88, v32, v56, v88\nv_dot2_f32_bf16 v89, v32, v57, v89\nv_dot2_f32_bf16 v90, v33, v56, v90\nv_dot2_f32_bf16 v91, v33, v57, v91\n"
"v_dot2_f32_bf16 v92, v34, v56, v92\nv_dot2_f32_bf16 v93, v34, v57, v93\nv_dot2_f32_bf16 v94, v35, v56, v94\nv_dot2_f32_bf16 v95, v35, v57, v95\ns_nop 3\nv_permlane32_swap_b32 v88, v92\n"
"v_permlane32_swap_b32 v89, v93\nv_permlane32_swap_b32 v90, v94\nv_permlane32_swap_b32 v91, v95\nv_add_f32 v88, v88, v92\nv_add_f32 v89, v89, v93\nv_add_f32 v90, v90, v94\n"
"v_add_f32 v91, v91, v95\ns_nop 1\nv_permlane16_swap_b32 v88, v90\nv_permlane16_swap_b32 v89, v91\nv_add_f32 v88, v88, v90\nv_add_f32 v89, v89, v91\n"
"s_nop 1\nv_add_f32_dpp v88, v88, v88 row_ror:8 row_mask:0xf bank_mask:0xf\nv_add_f32_dpp v89, v89, v89 row_ror:8 row_mask:0xf bank_mask:0xf\nds_write_b64 v201, v[88:89]\nds_write_b64 v200, v[180:181]\nv_add_u32 v182, 160512, v204\n"
"xW lgkmcnt(0)\ns_barrier\nds_read_b32 v88, v182 offset:0\nds_read_b32 v89, v182 offset:256\nds_read_b32 v90, v182 offset:512\nds_read_b32 v91, v182 offset:768\n"
"ds_read_b64 v[0:1], v187 offset:0\nds_read_b64 v[2:3], v187 offset:64\nds_read_b64 v[4:5], v187 offset:128\nds_read_b64 v[6:7], v187 offset:192\nxW lgkmcnt(4)\nv_add_f32 v88, v88, v89\n"
"v_add_f32 v90, v90, v91\nv_add_f32 v88, v88, v90\ns_cmp_ge_u32 %[bid], 216\ns_cselect_b32 s60, 1, %[wid]\ns_cmp_lg_u32 s60, 0\ns_cbranch_scc1 P0D_%=\n"
"buffer_store_dword v88, v204, s[96:99], s62 offen sc1\nP0D_%=:\nxW lgkmcnt(0)\nv_add_f32 v183, v0, v2\nv_add_f32 v182, v4, v6\nv_add_f32 v183, v183, v182\n"
"xU v183,0x3e800000,v183\nv_add_f32 v181, v1, v3\nv_add_f32 v182, v5, v7\nv_add_f32 v181, v181, v182\nv_mov_b32 v180, 0\nv_sub_f32 v182, v0, v183\n"
"v_fmac_f32 v180, v182, v182\nv_sub_f32 v182, v2, v183\nv_fmac_f32 v180, v182, v182\nv_sub_f32 v182, v4, v183\nv_fmac_f32 v180, v182, v182\nv_sub_f32 v182, v6, v183\n"
"v_fmac_f32 v180, v182, v182\nv_fmac_f32 v181, 0x43100000, v180\nv_mov_b32 v182, 0x3727c5ac\nv_fmac_f32 v182, 0x3ae38e39, v181\nv_rsq_f32 v184, v182\ns_nop 0\n"
"xU v185,v183,v184\nv_pk_fma_f32 v[64:65], v[64:65], v[184:185], v[184:185] op_sel:[0,0,1] op_sel_hi:[1,0,1] neg_lo:[0,0,1] neg_hi:[0,0,1]\nv_pk_fma_f32 v[66:67], v[66:67], v[184:185], v[184:185] op_sel:[0,0,1] op_sel_hi:[1,0,1] neg_lo:[0,0,1] neg_hi:[0,0,1]\nv_pk_fma_f32 v[68:69], v[68:69], v[184:185], v[184:185] op_sel:[0,0,1] op_sel_hi:[1,0,1] neg_lo:[0,0,1] neg_hi:[0,0,1]\nv_pk_fma_f32 v[70:71], v[70:71], v[184:185], v[184:185] op_sel:[0,0,1] op_sel_hi:[1,0,1] neg_lo:[0,0,1] neg_hi:[0,0,1]\nv_cvt_pk_f16_f32 v0, v64, v65\n"
"v_cvt_pk_f16_f32 v1, v66, v67\nv_cvt_pk_f16_f32 v2, v68, v69\nv_cvt_pk_f16_f32 v3, v70, v71\nxS v[0:3],v186,s[96:99],s37 offen offset:0 sc1\nv_pk_fma_f32 v[72:73], v[72:73], v[184:185], v[184:185] op_sel:[0,0,1] op_sel_hi:[1,0,1] neg_lo:[0,0,1] neg_hi:[0,0,1]\nv_pk_fma_f32 v[74:75], v[74:75], v[184:185], v[184:185] op_sel:[0,0,1] op_sel_hi:[1,0,1] neg_lo:[0,0,1] neg_hi:[0,0,1]\n"
"v_pk_fma_f32 v[76:77], v[76:77], v[184:185], v[184:185] op_sel:[0,0,1] op_sel_hi:[1,0,1] neg_lo:[0,0,1] neg_hi:[0,0,1]\nv_pk_fma_f32 v[78:79], v[78:79], v[184:185], v[184:185] op_sel:[0,0,1] op_sel_hi:[1,0,1] neg_lo:[0,0,1] neg_hi:[0,0,1]\nv_cvt_pk_f16_f32 v4, v72, v73\nv_cvt_pk_f16_f32 v5, v74, v75\nv_cvt_pk_f16_f32 v6, v76, v77\nv_cvt_pk_f16_f32 v7, v78, v79\n"
"xS v[4:7],v186,s[96:99],s37 offen offset:2048 sc1\nv_pk_fma_f32 v[80:81], v[80:81], v[184:185], v[184:185] op_sel:[0,0,1] op_sel_hi:[1,0,1] neg_lo:[0,0,1] neg_hi:[0,0,1]\nv_pk_fma_f32 v[82:83], v[82:83], v[184:185], v[184:185] op_sel:[0,0,1] op_sel_hi:[1,0,1] neg_lo:[0,0,1] neg_hi:[0,0,1]\nv_pk_fma_f32 v[84:85], v[84:85], v[184:185], v[184:185] op_sel:[0,0,1] op_sel_hi:[1,0,1] neg_lo:[0,0,1] neg_hi:[0,0,1]\nv_pk_fma_f32 v[86:87], v[86:87], v[184:185], v[184:185] op_sel:[0,0,1] op_sel_hi:[1,0,1] neg_lo:[0,0,1] neg_hi:[0,0,1]\nv_cvt_pk_f16_f32 v8, v80, v81\n"
"v_cvt_pk_f16_f32 v9, v82, v83\nv_cvt_pk_f16_f32 v10, v84, v85\nv_cvt_pk_f16_f32 v11, v86, v87\ns_nop 0\nxA s37,s37,4096\ns_mov_b64 exec, 0xffff\n"
"xS v[8:11],v186,s[96:99],s37 offen offset:0 sc1\ns_mov_b64 exec, -1\nxA m0,s42,16384\nxA s39,s43,524288\nxL v235,s[20:23],s39 offen offset:0 lds\nxL v236,s[20:23],s39 offen offset:1024 lds\n"
"xL v252,s[20:23],s39 offen offset:2048 lds\nxL v247,s[20:23],s39 offen offset:3072 lds\nv_lshlrev_b32 v183, 11, v255\nxW vmcnt(4)\ns_barrier\ns_cmp_lg_u32 %[wid], 1\n"
"s_cbranch_scc1 P0E_%=\ns_sub_u32 s50, s50, 8\ns_mov_b64 exec, 15\nbuffer_store_dwordx2 v[198:199], v196, s[96:99], s50 offen sc1\ns_cmp_ge_u32 %[bid], 216\ns_cbranch_scc1 P0E2_%=\n"
"s_mov_b64 exec, 0xffff\nbuffer_store_dwordx2 v[198:199], v183, s[96:99], s63 offen sc1\nP0E2_%=:\ns_mov_b64 exec, -1\nP0E_%=:\nxA m0,s42,24576\n"
"xA s39,s43,786432\nxL v235,s[20:23],s39 offen offset:0 lds\nxL v236,s[20:23],s39 offen offset:1024 lds\nxL v252,s[20:23],s39 offen offset:2048 lds\nxL v247,s[20:23],s39 offen offset:3072 lds\nxA m0,s42,32768\n"
"xA s39,s43,1048576\nxL v235,s[20:23],s39 offen offset:0 lds\nxL v236,s[20:23],s39 offen offset:1024 lds\nxL v252,s[20:23],s39 offen offset:2048 lds\nxL v247,s[20:23],s39 offen offset:3072 lds\nxA m0,s42,40960\n"
"xA s39,s43,1310720\nxL v235,s[20:23],s39 offen offset:0 lds\nxL v236,s[20:23],s39 offen offset:1024 lds\nxL v252,s[20:23],s39 offen offset:2048 lds\nxL v247,s[20:23],s39 offen offset:3072 lds\ns_cmp_eq_u32 %[wid], 0\n"
"s_cbranch_scc1 P0P_%=\ns_cmp_lt_u32 s70, 8\ns_cbranch_scc1 P0P_%=\ns_and_b32 s35, s70, 7\ns_mul_i32 s35, s35, 3\nxA s35,s35,%[wid]\n"
"s_sub_u32 s35, s35, 1\ns_mul_i32 s39, s35, 131072\nxA s39,s39,1575552\ns_mov_b64 exec, -1\nbuffer_load_dword a255, v202, s[20:23], s39 offen\ns_mov_b64 exec, -1\n"
"s_mul_i32 s39, s35, 1408\nxA s39,s39,0\ns_mov_b64 exec, 0x7ff\nbuffer_load_dword a255, v203, s[12:15], s39 offen\nbuffer_load_dword a255, v203, s[16:19], s39 offen\ns_mov_b64 exec, -1\n"
"P0P_%=:\ns_mov_b64 exec, 0\ns_cmp_eq_u32 %[wid], 1\ns_cbranch_scc1 TCH0_1_%=\ns_cmp_eq_u32 %[wid], 2\ns_cbranch_scc1 TCH0_2_%=\n"
"s_cmp_eq_u32 %[wid], 3\ns_cbranch_scc1 TCH0_3_%=\ns_cmp_eq_u32 %[wid], 0\ns_cbranch_scc1 TCH0_0_%=\nTDONE0_%=:\ns_mov_b64 exec, -1\n"
"s_cmp_lg_u32 %[wid], 0\ns_cbranch_scc1 P0T_%=\nbuffer_load_dwordx2 v[190:191], v196, s[96:99], s51 offen sc1\nbuffer_load_dwordx2 v[192:193], v196, s[96:99], s53 offen sc1\nbuffer_load_dwordx2 v[194:195], v197, s[96:99], s53 offen sc1\nP0T_%=:\n"
"s_and_b32 s39, s71, 1\ns_mul_i32 s39, s39, 18432\nxA s45,s47,18432\ns_sub_u32 s45, s45, s39\nxA s47,s47,s39\ns_mul_i32 s37, %[wid], 576\n"
"xA s39,s37,148480\nv_add_u32 v182, s39, v251\nxA s37,s37,1064960\nv_mov_b32 v184, 0x3e0293ee\ns_cmp_lg_u32 %[wid], 0\ns_cbranch_scc1 P0G_%=\n"
"s_mov_b32 s52, 200000\nP0L_%=:\nxW vmcnt(0)\nv_cmp_ne_u64 vcc, s[68:69], v[190:191]\ns_andn2_b64 s[62:63], vcc, s[54:55]\nv_cmp_ne_u64 vcc, s[68:69], v[192:193]\n"
"s_or_b64 s[62:63], s[62:63], vcc\nv_cmp_ne_u64 vcc, s[68:69], v[194:195]\ns_or_b64 s[62:63], s[62:63], vcc\ns_cbranch_scc0 P0G_%=\ns_sleep 4\nbuffer_load_dwordx2 v[190:191], v196, s[96:99], s51 offen sc1\n"
"buffer_load_dwordx2 v[192:193], v196, s[96:99], s53 offen sc1\nbuffer_load_dwordx2 v[194:195], v197, s[96:99], s53 offen sc1\ns_sub_u32 s52, s52, 1\ns_cmp_lg_u32 s52, 0\ns_cbranch_scc1 P0L_%=\nxW vmcnt(0)\n"
"P0G_%=:\ns_barrier\ns_mov_b32 exec_hi, 15\nxL v[96:99],v251,s[96:99],s37 offen sc1\nxL v[100:103],v251,s[96:99],s37 offen offset:2304 sc1\nxA s37,s37,13824\n"
"xL v[104:107],v251,s[96:99],s37 offen sc1\nxL v[108:111],v251,s[96:99],s37 offen offset:2304 sc1\nxA s37,s37,13824\nxL v[112:115],v251,s[96:99],s37 offen sc1\nxL v[116:119],v251,s[96:99],s37 offen offset:2304 sc1\nxA s37,s37,13824\n"
"xL v[120:123],v251,s[96:99],s37 offen sc1\nxL v[124:127],v251,s[96:99],s37 offen offset:2304 sc1\nxA s37,s37,13824\ns_mov_b32 exec_hi, -1\nxA s38,s47,0\nxL v[128:131],v251,s[96:99],s38 offen sc1\n"
"xA s38,s45,0\nxL v[132:135],v251,s[96:99],s38 offen sc1\nxA s38,s47,1024\nxL v[136:139],v251,s[96:99],s38 offen sc1\nxA s38,s45,1024\nxL v[140:143],v251,s[96:99],s38 offen sc1\n"
"xA s38,s47,2048\nxL v[144:147],v251,s[96:99],s38 offen sc1\nxA s38,s45,2048\nxL v[148:151],v251,s[96:99],s38 offen sc1\nxA s38,s47,3072\nxL v[152:155],v251,s[96:99],s38 offen sc1\n"
"xA s38,s45,3072\nxL v[156:159],v251,s[96:99],s38 offen sc1\nxA s38,s47,4096\nxL v[160:163],v251,s[96:99],s38 offen sc1\nxA s38,s45,4096\nxL v[164:167],v251,s[96:99],s38 offen sc1\n"
"xW vmcnt(10)\nv_add_f32 v96, v96, v104\nv_add_f32 v112, v112, v120\nv_add_f32 v96, v96, v112\nxC v96,0,v96\nv_fma_f32 v96, v96, v184, 0\n"
"v_add_f32 v97, v97, v105\nv_add_f32 v113, v113, v121\nv_add_f32 v97, v97, v113\nxC v97,0,v97\nv_fma_f32 v97, v97, v184, 0\nv_add_f32 v98, v98, v106\n"
"v_add_f32 v114, v114, v122\nv_add_f32 v98, v98, v114\nxC v98,0,v98\nv_fma_f32 v98, v98, v184, 0\nv_add_f32 v99, v99, v107\nv_add_f32 v115, v115, v123\n"
"v_add_f32 v99, v99, v115\nxC v99,0,v99\nv_fma_f32 v99, v99, v184, 0\nv_add_f32 v100, v100, v108\nv_add_f32 v116, v116, v124\nv_add_f32 v100, v100, v116\n"
"xC v100,0,v100\nv_fma_f32 v100, v100, v184, v184\nv_add_f32 v101, v101, v109\nv_add_f32 v117, v117, v125\nv_add_f32 v101, v101, v117\nxC v101,0,v101\n"
"v_fma_f32 v101, v101, v184, v184\nv_add_f32 v102, v102, v110\nv_add_f32 v118, v118, v126\nv_add_f32 v102, v102, v118\nxC v102,0,v102\nv_fma_f32 v102, v102, v184, v184\n"
"v_add_f32 v103, v103, v111\nv_add_f32 v119, v119, v127\nv_add_f32 v103, v103, v119\nxC v103,0,v103\nv_fma_f32 v103, v103, v184, v184\nv_cvt_pk_f16_f32 v96, v100, v96\n"
"v_cvt_pk_f16_f32 v97, v101, v97\nv_cvt_pk_f16_f32 v98, v102, v98\nv_cvt_pk_f16_f32 v99, v103, v99\ns_mov_b32 exec_hi, 15\nds_write_b128 v182, v[96:99]\ns_mov_b32 exec_hi, -1\n"
"s_lshl_b32 s61, s61, 2\ns_xor_b32 s61, s61, 4\ns_or_b32 s61, s61, %[wid]\ns_cbranch_scc1 P0F_%=\ns_sub_u32 s37, %[bid], 198\ns_lshl_b32 s37, s37, 8\n"
"xA s37,s37,1064960\nbuffer_load_dword a204, v204, s[96:99], s37 offen sc1\nxA s37,s37,13824\nbuffer_load_dword a205, v204, s[96:99], s37 offen sc1\nxA s37,s37,13824\nbuffer_load_dword a206, v204, s[96:99], s37 offen sc1\n"
"xA s37,s37,13824\nbuffer_load_dword a207, v204, s[96:99], s37 offen sc1\nxA s37,s37,13824\nP0F_%=:\nxW lgkmcnt(0)\ns_cmp_lt_u32 s70, 8\n"
"s_cbranch_scc1 NODELAY_%=\ns_sleep 36\nNODELAY_%=:\nxW vmcnt(6)\ns_barrier\nQSTART_%=:\n"
"xR v[208:211],v245 offset:0\nxR v[216:219],v245 offset:16\nxT a[128:129],v246 offset:0\nxT a[130:131],v246 offset:4096\nxT a[132:133],v246 offset:8\nxT a[134:135],v246 offset:4104\n"
"xT a[136:137],v246 offset:512\nxT a[138:139],v246 offset:4608\nxT a[140:141],v246 offset:520\nxT a[142:143],v246 offset:4616\nxT a[144:145],v246 offset:1024\nxT a[146:147],v246 offset:5120\n"
"xW lgkmcnt(11)\nxF v237,v128,v208,v208 op_sel:[0,0,1] op_sel_hi:[1,1,1]\nxF v238,v128,v209,v209 op_sel:[1,0,1] op_sel_hi:[1,1,1]\nxC v176,v237,v238\nxF v239,v129,v210,v210 op_sel:[0,0,1] op_sel_hi:[1,1,1]\nxF v240,v129,v211,v211 op_sel:[1,0,1] op_sel_hi:[1,1,1]\n"
"xC v177,v239,v240\nxF v237,v132,v208,v208 op_sel:[0,0,1] op_sel_hi:[1,1,1]\nxF v238,v132,v209,v209 op_sel:[1,0,1] op_sel_hi:[1,1,1]\nxC v180,v237,v238\nxF v239,v133,v210,v210 op_sel:[0,0,1] op_sel_hi:[1,1,1]\nxF v240,v133,v211,v211 op_sel:[1,0,1] op_sel_hi:[1,1,1]\n"
"xC v181,v239,v240\nxR v[208:211],v245 offset:128\nxW lgkmcnt(11)\nxF v237,v130,v216,v216 op_sel:[0,0,1] op_sel_hi:[1,1,1]\nxF v238,v130,v217,v217 op_sel:[1,0,1] op_sel_hi:[1,1,1]\nxC v178,v237,v238\n"
"xF v239,v131,v218,v218 op_sel:[0,0,1] op_sel_hi:[1,1,1]\nxF v240,v131,v219,v219 op_sel:[1,0,1] op_sel_hi:[1,1,1]\nxC v179,v239,v240\nxF v237,v134,v216,v216 op_sel:[0,0,1] op_sel_hi:[1,1,1]\nxF v238,v134,v217,v217 op_sel:[1,0,1] op_sel_hi:[1,1,1]\nxC v182,v237,v238\n"
"xF v239,v135,v218,v218 op_sel:[0,0,1] op_sel_hi:[1,1,1]\nxF v240,v135,v219,v219 op_sel:[1,0,1] op_sel_hi:[1,1,1]\nxC v183,v239,v240\ns_nop 1\nxW lgkmcnt(9)\n"
"xM v[0:3],a[128:131],v[176:179],0\nxW lgkmcnt(7)\nxR v[216:219],v245 offset:144\nxW lgkmcnt(1)\nxF v237,v136,v208,v208 op_sel:[0,0,1] op_sel_hi:[1,1,1]\n"
"xM v[32:35],a[128:131],v[180:183],0\nxA s38,s47,5120\nxT a[148:149],v246 offset:1032\nxF v238,v136,v209,v209 op_sel:[1,0,1] op_sel_hi:[1,1,1]\n"
"xM v[4:7],a[132:135],v[176:179],0\nxL v[168:171],v251,s[96:99],s38 offen sc1\nxM v[36:39],a[132:135],v[180:183],0\nxT a[150:151],v246 offset:5128\nxT a[152:153],v246 offset:1536\n"
"xM v[8:11],a[136:139],v[176:179],0\nxT a[154:155],v246 offset:5632\nxC v184,v237,v238\n"
"xM v[40:43],a[136:139],v[180:183],0\nxA s38,s45,5120\nxT a[156:157],v246 offset:1544\nxF v239,v137,v210,v210 op_sel:[0,0,1] op_sel_hi:[1,1,1]\n"
"xM v[12:15],a[140:143],v[176:179],0\nxL v[172:175],v251,s[96:99],s38 offen sc1\nxM v[44:47],a[140:143],v[180:183],0\nxT a[158:159],v246 offset:5640\nxT a[160:161],v246 offset:2048\n"
"xM v[16:19],a[144:147],v[176:179],0\nxW lgkmcnt(5)\nxT a[162:163],v246 offset:6144\nxF v240,v137,v211,v211 op_sel:[1,0,1] op_sel_hi:[1,1,1]\n"
"xM v[48:51],a[144:147],v[180:183],0\nxA m0,s36,0\nxT a[164:165],v246 offset:2056\nxC v185,v239,v240\n"
"xM v[20:23],a[148:151],v[176:179],0\nxL v230,s[12:15],s32 offen offset:0 lds\nxM v[52:55],a[148:151],v[180:183],0\nxW lgkmcnt(5)\nxT a[166:167],v246 offset:6152\nxT a[168:169],v246 offset:2560\n"
"xM v[24:27],a[152:155],v[176:179],0\nxT a[170:171],v246 offset:6656\nxF v237,v140,v208,v208 op_sel:[0,0,1] op_sel_hi:[1,1,1]\n"
"xM v[56:59],a[152:155],v[180:183],0\nxW lgkmcnt(6)\nxT a[172:173],v246 offset:2568\nxF v238,v140,v209,v209 op_sel:[1,0,1] op_sel_hi:[1,1,1]\n"
"xM v[28:31],a[156:159],v[176:179],0\nxL v231,s[12:15],s32 offen offset:1024 lds\nxM v[60:63],a[156:159],v[180:183],0\nxW lgkmcnt(5)\nxT a[174:175],v246 offset:6664\nxT a[176:177],v246 offset:3072\n"
"xC v188,v237,v238\nxM v[64:67],a[160:163],v[176:179],0\nxT a[178:179],v246 offset:7168\nxF v239,v141,v210,v210 op_sel:[0,0,1] op_sel_hi:[1,1,1]\n"
"xM v[96:99],a[160:163],v[180:183],0\nxW lgkmcnt(6)\nxT a[180:181],v246 offset:3080\nxF v240,v141,v211,v211 op_sel:[1,0,1] op_sel_hi:[1,1,1]\n"
"xM v[68:71],a[164:167],v[176:179],0\nxW lgkmcnt(5)\nxL v232,s[12:15],s32 offen offset:2048 lds\nxC v189,v239,v240\n"
"xM v[100:103],a[164:167],v[180:183],0\nxW vmcnt(9)\ns_barrier\n"
"xM v[72:75],a[168:171],v[176:179],0\nxT a[182:183],v246 offset:7176\nxT a[184:185],v246 offset:3584\nxR v[208:211],v245 offset:256\nxF v237,v138,v216,v216 op_sel:[0,0,1] op_sel_hi:[1,1,1]\n"
"xM v[104:107],a[168:171],v[180:183],0\nxW lgkmcnt(6)\nxT a[186:187],v246 offset:7680\nxT a[188:189],v246 offset:3592\nxF v238,v138,v217,v217 op_sel:[1,0,1] op_sel_hi:[1,1,1]\n"
"xM v[76:79],a[172:175],v[176:179],0\nxW lgkmcnt(6)\nxL v233,s[12:15],s32 offen offset:3072 lds\nxC v186,v237,v238\n"
"xM v[108:111],a[172:175],v[180:183],0\nxA s32,s32,16384\nxT a[190:191],v246 offset:7688\nxT a[128:129],v246 offset:8192\nxF v239,v139,v218,v218 op_sel:[0,0,1] op_sel_hi:[1,1,1]\n"
"xM v[80:83],a[176:179],v[176:179],0\nxW lgkmcnt(6)\nxT a[130:131],v246 offset:12288\nxF v240,v139,v219,v219 op_sel:[1,0,1] op_sel_hi:[1,1,1]\n"
"xM v[112:115],a[176:179],v[180:183],0\nxA m0,s36,16384\nxT a[132:133],v246 offset:8200\nxC v187,v239,v240\n"
"xM v[84:87],a[180:183],v[176:179],0\nxL v230,s[12:15],s32 offen offset:0 lds\nxF v237,v142,v216,v216 op_sel:[0,0,1] op_sel_hi:[1,1,1]\n"
"xM v[116:119],a[180:183],v[180:183],0\nxW lgkmcnt(5)\nxT a[134:135],v246 offset:12296\nxT a[136:137],v246 offset:8704\nxF v238,v142,v217,v217 op_sel:[1,0,1] op_sel_hi:[1,1,1]\n"
"xM v[88:91],a[184:187],v[176:179],0\nxT a[138:139],v246 offset:12800\nxC v190,v237,v238\n"
"xM v[120:123],a[184:187],v[180:183],0\nxW lgkmcnt(6)\nxT a[140:141],v246 offset:8712\nxF v239,v143,v218,v218 op_sel:[0,0,1] op_sel_hi:[1,1,1]\n"
"xM v[92:95],a[188:191],v[176:179],0\nxL v231,s[12:15],s32 offen offset:1024 lds\nxF v240,v143,v219,v219 op_sel:[1,0,1] op_sel_hi:[1,1,1]\nxC v191,v239,v240\n"
"xM v[124:127],a[188:191],v[180:183],0\nxW lgkmcnt(5)\nxT a[142:143],v246 offset:12808\nxT a[144:145],v246 offset:9216\n"
"xM v[0:3],a[128:131],v[184:187],v[0:3]\nxT a[146:147],v246 offset:13312\nxR v[216:219],v245 offset:272\n"
"xM v[32:35],a[128:131],v[188:191],v[32:35]\nxW lgkmcnt(7)\nxT a[148:149],v246 offset:9224\nxF v237,v144,v208,v208 op_sel:[0,0,1] op_sel_hi:[1,1,1]\n"
"xM v[4:7],a[132:135],v[184:187],v[4:7]\nxL v232,s[12:15],s32 offen offset:2048 lds\nxM v[36:39],a[132:135],v[188:191],v[36:39]\nxW lgkmcnt(6)\nxT a[150:151],v246 offset:13320\nxT a[152:153],v246 offset:9728\n"
"xM v[8:11],a[136:139],v[184:187],v[8:11]\nxT a[154:155],v246 offset:13824\nxF v238,v144,v209,v209 op_sel:[1,0,1] op_sel_hi:[1,1,1]\n"
"xM v[40:43],a[136:139],v[188:191],v[40:43]\nxW lgkmcnt(7)\nxT a[156:157],v246 offset:9736\nxC v192,v237,v238\n"
"xM v[12:15],a[140:143],v[184:187],v[12:15]\nxW lgkmcnt(6)\nxL v233,s[12:15],s32 offen offset:3072 lds\n"
"xM v[44:47],a[140:143],v[188:191],v[44:47]\nxA s32,s32,16384\nxT a[158:159],v246 offset:13832\nxT a[160:161],v246 offset:10240\n"
"xM v[16:19],a[144:147],v[184:187],v[16:19]\nxW lgkmcnt(5)\nxT a[162:163],v246 offset:14336\nxF v239,v145,v210,v210 op_sel:[0,0,1] op_sel_hi:[1,1,1]\n"
"xM v[48:51],a[144:147],v[188:191],v[48:51]\nxA s38,s47,6144\nxT a[164:165],v246 offset:10248\nxF v240,v145,v211,v211 op_sel:[1,0,1] op_sel_hi:[1,1,1]\n"
"xM v[20:23],a[148:151],v[184:187],v[20:23]\nxL v[128:131],v251,s[96:99],s38 offen sc1\nxM v[52:55],a[148:151],v[188:191],v[52:55]\nxW lgkmcnt(5)\nxT a[166:167],v246 offset:14344\nxT a[168:169],v246 offset:10752\n"
"xM v[24:27],a[152:155],v[184:187],v[24:27]\nxW lgkmcnt(5)\nxT a[170:171],v246 offset:14848\nxC v193,v239,v240\n"
"xM v[56:59],a[152:155],v[188:191],v[56:59]\nxA s38,s45,6144\nxT a[172:173],v246 offset:10760\nxF v237,v148,v208,v208 op_sel:[0,0,1] op_sel_hi:[1,1,1]\n"
"xM v[28:31],a[156:159],v[184:187],v[28:31]\nxL v[132:135],v251,s[96:99],s38 offen sc1\nxF v238,v148,v209,v209 op_sel:[1,0,1] op_sel_hi:[1,1,1]\n"
"xM v[60:63],a[156:159],v[188:191],v[60:63]\nxW lgkmcnt(5)\nxT a[174:175],v246 offset:14856\nxT a[176:177],v246 offset:11264\nxC v196,v237,v238\n"
"xM v[64:67],a[160:163],v[184:187],v[64:67]\nxT a[178:179],v246 offset:15360\nxF v239,v149,v210,v210 op_sel:[0,0,1] op_sel_hi:[1,1,1]\n"
"xM v[96:99],a[160:163],v[188:191],v[96:99]\nxW lgkmcnt(6)\nxT a[180:181],v246 offset:11272\nxF v240,v149,v211,v211 op_sel:[1,0,1] op_sel_hi:[1,1,1]\n"
"xM v[68:71],a[164:167],v[184:187],v[68:71]\nxW lgkmcnt(5)\nxT a[182:183],v246 offset:15368\nxC v197,v239,v240\n"
"xM v[100:103],a[164:167],v[188:191],v[100:103]\nxW vmcnt(14)\ns_barrier\n"
"xM v[72:75],a[168:171],v[184:187],v[72:75]\nxA m0,s42,0\nxT a[184:185],v246 offset:11776\nxT a[186:187],v246 offset:15872\nxR v[208:211],v245 offset:384\nxF v237,v146,v216,v216 op_sel:[0,0,1] op_sel_hi:[1,1,1]\n"
"xM v[104:107],a[168:171],v[188:191],v[104:107]\nxA s39,s43,1572864\nxT a[188:189],v246 offset:11784\nxF v238,v146,v217,v217 op_sel:[1,0,1] op_sel_hi:[1,1,1]\nxW lgkmcnt(8)\n"
"xM v[76:79],a[172:175],v[184:187],v[76:79]\nxL v235,s[20:23],s39 offen offset:0 lds\nxC v194,v237,v238\n"
"xM v[108:111],a[172:175],v[188:191],v[108:111]\nxW lgkmcnt(6)\nxT a[190:191],v246 offset:15880\nxT a[128:129],v246 offset:16384\nxF v239,v147,v218,v218 op_sel:[0,0,1] op_sel_hi:[1,1,1]\n"
"xM v[80:83],a[176:179],v[184:187],v[80:83]\nxT a[130:131],v246 offset:20480\nxF v240,v147,v219,v219 op_sel:[1,0,1] op_sel_hi:[1,1,1]\n"
"xM v[112:115],a[176:179],v[188:191],v[112:115]\nxW lgkmcnt(7)\nxT a[132:133],v246 offset:16392\nxC v195,v239,v240\n"
"xM v[84:87],a[180:183],v[184:187],v[84:87]\nxL v236,s[20:23],s39 offen offset:1024 lds\nxF v237,v150,v216,v216 op_sel:[0,0,1] op_sel_hi:[1,1,1]\n"
"xM v[116:119],a[180:183],v[188:191],v[116:119]\nxW lgkmcnt(6)\nxT a[134:135],v246 offset:20488\nxT a[136:137],v246 offset:16896\nxF v238,v150,v217,v217 op_sel:[1,0,1] op_sel_hi:[1,1,1]\n"
"xM v[88:91],a[184:187],v[184:187],v[88:91]\nxT a[138:139],v246 offset:20992\nxC v198,v237,v238\n"
"xM v[120:123],a[184:187],v[188:191],v[120:123]\nxW lgkmcnt(6)\nxT a[140:141],v246 offset:16904\nxF v239,v151,v218,v218 op_sel:[0,0,1] op_sel_hi:[1,1,1]\n"
"xM v[92:95],a[188:191],v[184:187],v[92:95]\nxL v252,s[20:23],s39 offen offset:2048 lds\nxF v240,v151,v219,v219 op_sel:[1,0,1] op_sel_hi:[1,1,1]\nxC v199,v239,v240\n"
"xM v[124:127],a[188:191],v[188:191],v[124:127]\nxW lgkmcnt(5)\nxT a[142:143],v246 offset:21000\nxT a[144:145],v246 offset:17408\n"
"xM v[0:3],a[128:131],v[192:195],v[0:3]\nxT a[146:147],v246 offset:21504\nxR v[216:219],v245 offset:400\n"
"xM v[32:35],a[128:131],v[196:199],v[32:35]\nxW lgkmcnt(7)\nxT a[148:149],v246 offset:17416\nxF v237,v152,v208,v208 op_sel:[0,0,1] op_sel_hi:[1,1,1]\n"
"xM v[4:7],a[132:135],v[192:195],v[4:7]\nxL v247,s[20:23],s39 offen offset:3072 lds\nxM v[36:39],a[132:135],v[196:199],v[36:39]\nxW lgkmcnt(6)\nxT a[150:151],v246 offset:21512\nxT a[152:153],v246 offset:17920\n"
"xM v[8:11],a[136:139],v[192:195],v[8:11]\nxW lgkmcnt(6)\nxT a[154:155],v246 offset:22016\nxF v238,v152,v209,v209 op_sel:[1,0,1] op_sel_hi:[1,1,1]\n"
"xM v[40:43],a[136:139],v[196:199],v[40:43]\nxA s38,s47,7168\nxT a[156:157],v246 offset:17928\nxC v200,v237,v238\n"
"xM v[12:15],a[140:143],v[192:195],v[12:15]\nxL v[136:139],v251,s[96:99],s38 offen sc1\nxM v[44:47],a[140:143],v[196:199],v[44:47]\nxW lgkmcnt(6)\nxT a[158:159],v246 offset:22024\nxT a[160:161],v246 offset:18432\n"
"xM v[16:19],a[144:147],v[192:195],v[16:19]\nxW lgkmcnt(5)\nxT a[162:163],v246 offset:22528\nxF v239,v153,v210,v210 op_sel:[0,0,1] op_sel_hi:[1,1,1]\n"
"xM v[48:51],a[144:147],v[196:199],v[48:51]\nxA s38,s45,7168\nxT a[164:165],v246 offset:18440\nxF v240,v153,v211,v211 op_sel:[1,0,1] op_sel_hi:[1,1,1]\n"
"xM v[20:23],a[148:151],v[192:195],v[20:23]\nxL v[140:143],v251,s[96:99],s38 offen sc1\nxM v[52:55],a[148:151],v[196:199],v[52:55]\nxW lgkmcnt(5)\nxT a[166:167],v246 offset:22536\nxT a[168:169],v246 offset:18944\n"
"xM v[24:27],a[152:155],v[192:195],v[24:27]\nxT a[170:171],v246 offset:23040\nxC v201,v239,v240\n"
"xM v[56:59],a[152:155],v[196:199],v[56:59]\nxW lgkmcnt(6)\nxT a[172:173],v246 offset:18952\nxF v237,v156,v208,v208 op_sel:[0,0,1] op_sel_hi:[1,1,1]\n"
"xM v[28:31],a[156:159],v[192:195],v[28:31]\nxT a[174:175],v246 offset:23048\nxF v238,v156,v209,v209 op_sel:[1,0,1] op_sel_hi:[1,1,1]\n"
"xM v[60:63],a[156:159],v[196:199],v[60:63]\nxW lgkmcnt(6)\nxT a[176:177],v246 offset:19456\nxC v204,v237,v238\n"
"xM v[64:67],a[160:163],v[192:195],v[64:67]\nxT a[178:179],v246 offset:23552\nxF v239,v157,v210,v210 op_sel:[0,0,1] op_sel_hi:[1,1,1]\n"
"xM v[96:99],a[160:163],v[196:199],v[96:99]\nxW lgkmcnt(6)\nxT a[180:181],v246 offset:19464\nxF v240,v157,v211,v211 op_sel:[1,0,1] op_sel_hi:[1,1,1]\n"
"xM v[68:71],a[164:167],v[192:195],v[68:71]\nxW lgkmcnt(5)\nxT a[182:183],v246 offset:23560\nxC v205,v239,v240\n"
"xM v[100:103],a[164:167],v[196:199],v[100:103]\nxW vmcnt(18)\ns_barrier\n"
"xM v[72:75],a[168:171],v[192:195],v[72:75]\nxA m0,s42,8192\nxT a[184:185],v246 offset:19968\nxT a[186:187],v246 offset:24064\nxR v[208:211],v245 offset:512\nxF v237,v154,v216,v216 op_sel:[0,0,1] op_sel_hi:[1,1,1]\n"
"xM v[104:107],a[168:171],v[196:199],v[104:107]\nxA s39,s43,1835008\nxT a[188:189],v246 offset:19976\nxF v238,v154,v217,v217 op_sel:[1,0,1] op_sel_hi:[1,1,1]\nxW lgkmcnt(8)\n"
"xM v[76:79],a[172:175],v[192:195],v[76:79]\nxL v235,s[20:23],s39 offen offset:0 lds\nxC v202,v237,v238\n"
"xM v[108:111],a[172:175],v[196:199],v[108:111]\nxW lgkmcnt(6)\nxT a[190:191],v246 offset:24072\nxT a[128:129],v246 offset:24576\nxF v239,v155,v218,v218 op_sel:[0,0,1] op_sel_hi:[1,1,1]\n"
"xM v[80:83],a[176:179],v[192:195],v[80:83]\nxT a[130:131],v246 offset:28672\nxF v240,v155,v219,v219 op_sel:[1,0,1] op_sel_hi:[1,1,1]\n"
"xM v[112:115],a[176:179],v[196:199],v[112:115]\nxW lgkmcnt(7)\nxT a[132:133],v246 offset:24584\nxC v203,v239,v240\n"
"xM v[84:87],a[180:183],v[192:195],v[84:87]\nxL v236,s[20:23],s39 offen offset:1024 lds\nxF v237,v158,v216,v216 op_sel:[0,0,1] op_sel_hi:[1,1,1]\n"
"xM v[116:119],a[180:183],v[196:199],v[116:119]\nxW lgkmcnt(6)\nxT a[134:135],v246 offset:28680\nxT a[136:137],v246 offset:25088\nxF v238,v158,v217,v217 op_sel:[1,0,1] op_sel_hi:[1,1,1]\n"
"xM v[88:91],a[184:187],v[192:195],v[88:91]\nTCH0_1_%=:\ns_cbranch_execz TC0_1_1_%=\nxT a[138:139],v246 offset:29184\nxC v206,v237,v238\n"
"xM v[120:123],a[184:187],v[196:199],v[120:123]\nxW lgkmcnt(6)\nxT a[140:141],v246 offset:25096\nxF v239,v159,v218,v218 op_sel:[0,0,1] op_sel_hi:[1,1,1]\n"
"xM v[92:95],a[188:191],v[192:195],v[92:95]\nxL v252,s[20:23],s39 offen offset:2048 lds\nxF v240,v159,v219,v219 op_sel:[1,0,1] op_sel_hi:[1,1,1]\nxC v207,v239,v240\n"
"xM v[124:127],a[188:191],v[196:199],v[124:127]\nxW lgkmcnt(5)\nxT a[142:143],v246 offset:29192\nxT a[144:145],v246 offset:25600\n"
"xM v[0:3],a[128:131],v[200:203],v[0:3]\nxT a[146:147],v246 offset:29696\nxR v[216:219],v245 offset:528\n"
"xM v[32:35],a[128:131],v[204:207],v[32:35]\nxW lgkmcnt(7)\nxT a[148:149],v246 offset:25608\nxF v237,v160,v208,v208 op_sel:[0,0,1] op_sel_hi:[1,1,1]\n"
"xM v[4:7],a[132:135],v[200:203],v[4:7]\nxL v247,s[20:23],s39 offen offset:3072 lds\nxM v[36:39],a[132:135],v[204:207],v[36:39]\nxW lgkmcnt(6)\nxT a[150:151],v246 offset:29704\nxT a[152:153],v246 offset:26112\n"
"xM v[8:11],a[136:139],v[200:203],v[8:11]\nxW lgkmcnt(6)\nxT a[154:155],v246 offset:30208\nxF v238,v160,v209,v209 op_sel:[1,0,1] op_sel_hi:[1,1,1]\n"
"xM v[40:43],a[136:139],v[204:207],v[40:43]\nxA s38,s47,8192\nxT a[156:157],v246 offset:26120\nxC v176,v237,v238\n"
"xM v[12:15],a[140:143],v[200:203],v[12:15]\nxL v[144:147],v251,s[96:99],s38 offen sc1\nxM v[44:47],a[140:143],v[204:207],v[44:47]\nxW lgkmcnt(6)\nxT a[158:159],v246 offset:30216\nxT a[160:161],v246 offset:26624\n"
"xM v[16:19],a[144:147],v[200:203],v[16:19]\nxW lgkmcnt(5)\nxT a[162:163],v246 offset:30720\nxF v239,v161,v210,v210 op_sel:[0,0,1] op_sel_hi:[1,1,1]\n"
"xM v[48:51],a[144:147],v[204:207],v[48:51]\nxA s38,s45,8192\nxT a[164:165],v246 offset:26632\nxF v240,v161,v211,v211 op_sel:[1,0,1] op_sel_hi:[1,1,1]\n"
"xM v[20:23],a[148:151],v[200:203],v[20:23]\nxL v[148:151],v251,s[96:99],s38 offen sc1\nxM v[52:55],a[148:151],v[204:207],v[52:55]\nxW lgkmcnt(5)\nxT a[166:167],v246 offset:30728\nxT a[168:169],v246 offset:27136\n"
"xM v[24:27],a[152:155],v[200:203],v[24:27]\nxT a[170:171],v246 offset:31232\nxC v177,v239,v240\n"
"xM v[56:59],a[152:155],v[204:207],v[56:59]\nxW lgkmcnt(6)\nxT a[172:173],v246 offset:27144\nxF v237,v164,v208,v208 op_sel:[0,0,1] op_sel_hi:[1,1,1]\n"
"xM v[28:31],a[156:159],v[200:203],v[28:31]\nxT a[174:175],v246 offset:31240\nxF v238,v164,v209,v209 op_sel:[1,0,1] op_sel_hi:[1,1,1]\n"
"xM v[60:63],a[156:159],v[204:207],v[60:63]\nxW lgkmcnt(6)\nxT a[176:177],v246 offset:27648\nxC v180,v237,v238\n"
"xM v[64:67],a[160:163],v[200:203],v[64:67]\nxT a[178:179],v246 offset:31744\nxF v239,v165,v210,v210 op_sel:[0,0,1] op_sel_hi:[1,1,1]\n"
"xM v[96:99],a[160:163],v[204:207],v[96:99]\nxW lgkmcnt(6)\nxT a[180:181],v246 offset:27656\nxF v240,v165,v211,v211 op_sel:[1,0,1] op_sel_hi:[1,1,1]\n"
"xM v[68:71],a[164:167],v[200:203],v[68:71]\nxW lgkmcnt(5)\nxT a[182:183],v246 offset:31752\nxC v181,v239,v240\n"
"xM v[100:103],a[164:167],v[204:207],v[100:103]\nxW vmcnt(22)\ns_barrier\n"
"xM v[72:75],a[168:171],v[200:203],v[72:75]\nxA m0,s42,16384\nxT a[184:185],v246 offset:28160\nxT a[186:187],v246 offset:32256\nxR v[208:211],v245 offset:640\nxF v237,v162,v216,v216 op_sel:[0,0,1] op_sel_hi:[1,1,1]\n"
"xM v[104:107],a[168:171],v[204:207],v[104:107]\nxA s39,s43,2097152\nxT a[188:189],v246 offset:28168\nxF v238,v162,v217,v217 op_sel:[1,0,1] op_sel_hi:[1,1,1]\nxW lgkmcnt(8)\n"
"xM v[76:79],a[172:175],v[200:203],v[76:79]\nxL v235,s[20:23],s39 offen offset:0 lds\nxC v178,v237,v238\n"
"xM v[108:111],a[172:175],v[204:207],v[108:111]\nxW lgkmcnt(6)\nxT a[190:191],v246 offset:32264\nxT a[128:129],v246 offset:32768\nxF v239,v163,v218,v218 op_sel:[0,0,1] op_sel_hi:[1,1,1]\n"
"xM v[80:83],a[176:179],v[200:203],v[80:83]\nxT a[130:131],v246 offset:36864\nxF v240,v163,v219,v219 op_sel:[1,0,1] op_sel_hi:[1,1,1]\n"
"xM v[112:115],a[176:179],v[204:207],v[112:115]\nxW lgkmcnt(7)\nxT a[132:133],v246 offset:32776\nxC v179,v239,v240\n"
"xM v[84:87],a[180:183],v[200:203],v[84:87]\nxL v236,s[20:23],s39 offen offset:1024 lds\nxF v237,v166,v216,v216 op_sel:[0,0,1] op_sel_hi:[1,1,1]\n"
"xM v[116:119],a[180:183],v[204:207],v[116:119]\nxW lgkmcnt(6)\nxT a[134:135],v246 offset:36872\nxT a[136:137],v246 offset:33280\nxF v238,v166,v217,v217 op_sel:[1,0,1] op_sel_hi:[1,1,1]\n"
"xM v[88:91],a[184:187],v[200:203],v[88:91]\nxT a[138:139],v246 offset:37376\nxC v182,v237,v238\n"
"xM v[120:123],a[184:187],v[204:207],v[120:123]\nxW lgkmcnt(6)\nxT a[140:141],v246 offset:33288\nxF v239,v167,v218,v218 op_sel:[0,0,1] op_sel_hi:[1,1,1]\n"
"xM v[92:95],a[188:191],v[200:203],v[92:95]\nxL v252,s[20:23],s39 offen offset:2048 lds\nxF v240,v167,v219,v219 op_sel:[1,0,1] op_sel_hi:[1,1,1]\nxC v183,v239,v240\n"
"xM v[124:127],a[188:191],v[204:207],v[124:127]\nxW lgkmcnt(5)\nxT a[142:143],v246 offset:37384\nxT a[144:145],v246 offset:33792\n"
"xM v[0:3],a[128:131],v[176:179],v[0:3]\nxT a[146:147],v246 offset:37888\nxR v[216:219],v245 offset:656\n"
"xM v[32:35],a[128:131],v[180:183],v[32:35]\nxW lgkmcnt(7)\nxT a[148:149],v246 offset:33800\nxF v237,v168,v208,v208 op_sel:[0,0,1] op_sel_hi:[1,1,1]\n"
"xM v[4:7],a[132:135],v[176:179],v[4:7]\nxL v247,s[20:23],s39 offen offset:3072 lds\nxM v[36:39],a[132:135],v[180:183],v[36:39]\nxW lgkmcnt(6)\nxT a[150:151],v246 offset:37896\nxT a[152:153],v246 offset:34304\n"
"xM v[8:11],a[136:139],v[176:179],v[8:11]\nxW lgkmcnt(6)\nxT a[154:155],v246 offset:38400\nxF v238,v168,v209,v209 op_sel:[1,0,1] op_sel_hi:[1,1,1]\n"
"xM v[40:43],a[136:139],v[180:183],v[40:43]\nxA s38,s47,9216\nxT a[156:157],v246 offset:34312\nxC v184,v237,v238\n"
"xM v[12:15],a[140:143],v[176:179],v[12:15]\nxL v[152:155],v251,s[96:99],s38 offen sc1\nxM v[44:47],a[140:143],v[180:183],v[44:47]\nxW lgkmcnt(6)\nxT a[158:159],v246 offset:38408\nxT a[160:161],v246 offset:34816\n"
"xM v[16:19],a[144:147],v[176:179],v[16:19]\nxW lgkmcnt(5)\nxT a[162:163],v246 offset:38912\nxF v239,v169,v210,v210 op_sel:[0,0,1] op_sel_hi:[1,1,1]\n"
"xM v[48:51],a[144:147],v[180:183],v[48:51]\nxA s38,s45,9216\nxT a[164:165],v246 offset:34824\nxF v240,v169,v211,v211 op_sel:[1,0,1] op_sel_hi:[1,1,1]\n"
"xM v[20:23],a[148:151],v[176:179],v[20:23]\nxL v[156:159],v251,s[96:99],s38 offen sc1\nxM v[52:55],a[148:151],v[180:183],v[52:55]\nxW lgkmcnt(5)\nxT a[166:167],v246 offset:38920\nxT a[168:169],v246 offset:35328\n"
"xM v[24:27],a[152:155],v[176:179],v[24:27]\nxW lgkmcnt(5)\nxT a[170:171],v246 offset:39424\nxC v185,v239,v240\n"
"xM v[56:59],a[152:155],v[180:183],v[56:59]\nxA m0,s36,32768\nxT a[172:173],v246 offset:35336\nxF v237,v172,v208,v208 op_sel:[0,0,1] op_sel_hi:[1,1,1]\n"
"xM v[28:31],a[156:159],v[176:179],v[28:31]\nxL v234,s[16:19],s33 offen offset:0 lds\nxF v238,v172,v209,v209 op_sel:[1,0,1] op_sel_hi:[1,1,1]\n"
"xM v[60:63],a[156:159],v[180:183],v[60:63]\nxW lgkmcnt(5)\nxT a[174:175],v246 offset:39432\nxT a[176:177],v246 offset:35840\nxC v188,v237,v238\n"
"xM v[64:67],a[160:163],v[176:179],v[64:67]\nxT a[178:179],v246 offset:39936\nxF v239,v173,v210,v210 op_sel:[0,0,1] op_sel_hi:[1,1,1]\n"
"xM v[96:99],a[160:163],v[180:183],v[96:99]\nxW lgkmcnt(6)\nxT a[180:181],v246 offset:35848\nxF v240,v173,v211,v211 op_sel:[1,0,1] op_sel_hi:[1,1,1]\n"
"xM v[68:71],a[164:167],v[176:179],v[68:71]\nxW lgkmcnt(5)\nxT a[182:183],v246 offset:39944\nxC v189,v239,v240\n"
"xM v[100:103],a[164:167],v[180:183],v[100:103]\nxW vmcnt(19)\ns_barrier\n"
"xM v[72:75],a[168:171],v[176:179],v[72:75]\nxW lgkmcnt(4)\nxL v234,s[16:19],s33 offen offset:2048 lds\nxR v[208:211],v245 offset:768\nxF v237,v170,v216,v216 op_sel:[0,0,1] op_sel_hi:[1,1,1]\n"
"xM v[104:107],a[168:171],v[180:183],v[104:107]\nxA s33,s33,16384\nxT a[184:185],v246 offset:36352\nxT a[186:187],v246 offset:40448\nxF v238,v170,v217,v217 op_sel:[1,0,1] op_sel_hi:[1,1,1]\n"
"xM v[76:79],a[172:175],v[176:179],v[76:79]\nxW lgkmcnt(5)\nxT a[188:189],v246 offset:36360\nxT a[190:191],v246 offset:40456\nxC v186,v237,v238\n"
"xM v[108:111],a[172:175],v[180:183],v[108:111]\nxA m0,s36,33792\nxT a[128:129],v246 offset:40960\nxF v239,v171,v218,v218 op_sel:[0,0,1] op_sel_hi:[1,1,1]\n"
"xM v[80:83],a[176:179],v[176:179],v[80:83]\nxL v234,s[16:19],s34 offen offset:0 lds\nxF v240,v171,v219,v219 op_sel:[1,0,1] op_sel_hi:[1,1,1]\n"
"xM v[112:115],a[176:179],v[180:183],v[112:115]\nxW lgkmcnt(6)\nxT a[130:131],v246 offset:45056\nxT a[132:133],v246 offset:40968\nxC v187,v239,v240\n"
"xM v[84:87],a[180:183],v[176:179],v[84:87]\nxT a[134:135],v246 offset:45064\nxF v237,v174,v216,v216 op_sel:[0,0,1] op_sel_hi:[1,1,1]\n"
"xM v[116:119],a[180:183],v[180:183],v[116:119]\nxW lgkmcnt(6)\nxT a[136:137],v246 offset:41472\nxF v238,v174,v217,v217 op_sel:[1,0,1] op_sel_hi:[1,1,1]\n"
"xM v[88:91],a[184:187],v[176:179],v[88:91]\nxW lgkmcnt(5)\nxL v234,s[16:19],s34 offen offset:2048 lds\nxC v190,v237,v238\n"
"xM v[120:123],a[184:187],v[180:183],v[120:123]\nxA s34,s34,16384\nxT a[138:139],v246 offset:45568\nxT a[140:141],v246 offset:41480\nxF v239,v175,v218,v218 op_sel:[0,0,1] op_sel_hi:[1,1,1]\n"
"xM v[92:95],a[188:191],v[176:179],v[92:95]\nxA m0,s42,24576\nxT a[142:143],v246 offset:45576\nxF v240,v175,v219,v219 op_sel:[1,0,1] op_sel_hi:[1,1,1]\nxC v191,v239,v240\n"
"xM v[124:127],a[188:191],v[180:183],v[124:127]\nxA s39,s43,2359296\nxT a[144:145],v246 offset:41984\nxR v[216:219],v245 offset:784\nxW lgkmcnt(8)\n"
"xM v[0:3],a[128:131],v[184:187],v[0:3]\nxL v235,s[20:23],s39 offen offset:0 lds\nxM v[32:35],a[128:131],v[188:191],v[32:35]\nxW lgkmcnt(6)\nxT a[146:147],v246 offset:46080\nxT a[148:149],v246 offset:41992\n"
"xM v[4:7],a[132:135],v[184:187],v[4:7]\nxT a[150:151],v246 offset:46088\nxF v237,v128,v208,v208 op_sel:[0,0,1] op_sel_hi:[1,1,1]\n"
"xM v[36:39],a[132:135],v[188:191],v[36:39]\nxW lgkmcnt(7)\nxT a[152:153],v246 offset:42496\nxF v238,v128,v209,v209 op_sel:[1,0,1] op_sel_hi:[1,1,1]\n"
"xM v[8:11],a[136:139],v[184:187],v[8:11]\nxL v236,s[20:23],s39 offen offset:1024 lds\nxM v[40:43],a[136:139],v[188:191],v[40:43]\nxW lgkmcnt(6)\nxT a[154:155],v246 offset:46592\nxT a[156:157],v246 offset:42504\n"
"xM v[12:15],a[140:143],v[184:187],v[12:15]\nxT a[158:159],v246 offset:46600\nxC v192,v237,v238\n"
"xM v[44:47],a[140:143],v[188:191],v[44:47]\nxW lgkmcnt(6)\nxT a[160:161],v246 offset:43008\nxF v239,v129,v210,v210 op_sel:[0,0,1] op_sel_hi:[1,1,1]\n"
"xM v[16:19],a[144:147],v[184:187],v[16:19]\nxL v252,s[20:23],s39 offen offset:2048 lds\nxM v[48:51],a[144:147],v[188:191],v[48:51]\nxW lgkmcnt(5)\nxT a[162:163],v246 offset:47104\nxT a[164:165],v246 offset:43016\n"
"xM v[20:23],a[148:151],v[184:187],v[20:23]\nxT a[166:167],v246 offset:47112\nxF v240,v129,v211,v211 op_sel:[1,0,1] op_sel_hi:[1,1,1]\n"
"xM v[52:55],a[148:151],v[188:191],v[52:55]\nxW lgkmcnt(6)\nxT a[168:169],v246 offset:43520\nxC v193,v239,v240\n"
"xM v[24:27],a[152:155],v[184:187],v[24:27]\nxL v247,s[20:23],s39 offen offset:3072 lds\nxM v[56:59],a[152:155],v[188:191],v[56:59]\nxW lgkmcnt(5)\nxT a[170:171],v246 offset:47616\nxT a[172:173],v246 offset:43528\n"
"xF v237,v132,v208,v208 op_sel:[0,0,1] op_sel_hi:[1,1,1]\nxM v[28:31],a[156:159],v[184:187],v[28:31]\nxW lgkmcnt(5)\nxT a[174:175],v246 offset:47624\nxF v238,v132,v209,v209 op_sel:[1,0,1] op_sel_hi:[1,1,1]\n"
"xM v[60:63],a[156:159],v[188:191],v[60:63]\nxA s38,s47,10240\nxT a[176:177],v246 offset:44032\nxC v196,v237,v238\n"
"xM v[64:67],a[160:163],v[184:187],v[64:67]\nxL v[160:163],v251,s[96:99],s38 offen sc1\nxF v239,v133,v210,v210 op_sel:[0,0,1] op_sel_hi:[1,1,1]\n"
"xM v[96:99],a[160:163],v[188:191],v[96:99]\nxW lgkmcnt(5)\nxT a[178:179],v246 offset:48128\nxT a[180:181],v246 offset:44040\nxF v240,v133,v211,v211 op_sel:[1,0,1] op_sel_hi:[1,1,1]\n"
"xM v[68:71],a[164:167],v[184:187],v[68:71]\nxW lgkmcnt(5)\nxT a[182:183],v246 offset:48136\nxC v197,v239,v240\n"
"xM v[100:103],a[164:167],v[188:191],v[100:103]\nxW vmcnt(21)\ns_barrier\n"
"xM v[72:75],a[168:171],v[184:187],v[72:75]\nxA s38,s45,10240\nxT a[184:185],v246 offset:44544\nxT a[186:187],v246 offset:48640\nxR v[208:211],v245 offset:896\nxF v237,v130,v216,v216 op_sel:[0,0,1] op_sel_hi:[1,1,1]\n"
"xM v[104:107],a[168:171],v[188:191],v[104:107]\nxW lgkmcnt(7)\nxL v[164:167],v251,s[96:99],s38 offen sc1\nxF v238,v130,v217,v217 op_sel:[1,0,1] op_sel_hi:[1,1,1]\n"
"xM v[76:79],a[172:175],v[184:187],v[76:79]\nxW lgkmcnt(5)\nxT a[188:189],v246 offset:44552\nxT a[190:191],v246 offset:48648\nxC v194,v237,v238\n"
"xM v[108:111],a[172:175],v[188:191],v[108:111]\nxA m0,s42,32768\nxT a[128:129],v246 offset:0\nxF v239,v131,v218,v218 op_sel:[0,0,1] op_sel_hi:[1,1,1]\n"
"xM v[80:83],a[176:179],v[184:187],v[80:83]\nxA s39,s43,2621440\nxT a[130:131],v246 offset:4096\nxF v240,v131,v219,v219 op_sel:[1,0,1] op_sel_hi:[1,1,1]\n"
"xM v[112:115],a[176:179],v[188:191],v[112:115]\nxW lgkmcnt(7)\nxL v235,s[20:23],s39 offen offset:0 lds\nxC v195,v239,v240\n"
"xM v[84:87],a[180:183],v[184:187],v[84:87]\nxT a[132:133],v246 offset:8\nxT a[134:135],v246 offset:4104\nxF v237,v134,v216,v216 op_sel:[0,0,1] op_sel_hi:[1,1,1]\n"
"xM v[116:119],a[180:183],v[188:191],v[116:119]\nxW lgkmcnt(7)\nxT a[136:137],v246 offset:512\nxF v238,v134,v217,v217 op_sel:[1,0,1] op_sel_hi:[1,1,1]\n"
"xM v[88:91],a[184:187],v[184:187],v[88:91]\nxT a[138:139],v246 offset:4608\nxC v198,v237,v238\n"
"xM v[120:123],a[184:187],v[188:191],v[120:123]\nxW lgkmcnt(6)\nxL v236,s[20:23],s39 offen offset:1024 lds\nxF v239,v135,v218,v218 op_sel:[0,0,1] op_sel_hi:[1,1,1]\n"
"xM v[92:95],a[188:191],v[184:187],v[92:95]\nxT a[140:141],v246 offset:520\nxT a[142:143],v246 offset:4616\nxF v240,v135,v219,v219 op_sel:[1,0,1] op_sel_hi:[1,1,1]\nxC v199,v239,v240\n"
"xM v[124:127],a[188:191],v[188:191],v[124:127]\nxW lgkmcnt(6)\nxT a[144:145],v246 offset:1024\nxR v[216:219],v245 offset:912\n"
"xM v[0:3],a[128:131],v[192:195],v[0:3]\nxT a[146:147],v246 offset:5120\nxF v237,v136,v208,v208 op_sel:[0,0,1] op_sel_hi:[1,1,1]\n"
"xM v[32:35],a[128:131],v[196:199],v[32:35]\nxW lgkmcnt(7)\nxL v252,s[20:23],s39 offen offset:2048 lds\n"
"xM v[4:7],a[132:135],v[192:195],v[4:7]\nxT a[148:149],v246 offset:1032\nxT a[150:151],v246 offset:5128\n"
"xM v[36:39],a[132:135],v[196:199],v[36:39]\nxW lgkmcnt(7)\nxT a[152:153],v246 offset:1536\nxF v238,v136,v209,v209 op_sel:[1,0,1] op_sel_hi:[1,1,1]\n"
"xM v[8:11],a[136:139],v[192:195],v[8:11]\nxT a[154:155],v246 offset:5632\nxC v200,v237,v238\n"
"xM v[40:43],a[136:139],v[196:199],v[40:43]\nxW lgkmcnt(7)\nxL v247,s[20:23],s39 offen offset:3072 lds\n"
"xM v[12:15],a[140:143],v[192:195],v[12:15]\nxT a[156:157],v246 offset:1544\nxT a[158:159],v246 offset:5640\n"
"xM v[44:47],a[140:143],v[196:199],v[44:47]\nxW lgkmcnt(6)\nxT a[160:161],v246 offset:2048\nxF v239,v137,v210,v210 op_sel:[0,0,1] op_sel_hi:[1,1,1]\n"
"xM v[16:19],a[144:147],v[192:195],v[16:19]\nxA s38,s47,11264\nxT a[162:163],v246 offset:6144\nxF v240,v137,v211,v211 op_sel:[1,0,1] op_sel_hi:[1,1,1]\n"
"xM v[48:51],a[144:147],v[196:199],v[48:51]\nxW lgkmcnt(6)\nxL v[168:171],v251,s[96:99],s38 offen sc1\n"
"xM v[20:23],a[148:151],v[192:195],v[20:23]\nxT a[164:165],v246 offset:2056\nxT a[166:167],v246 offset:6152\n"
"xM v[52:55],a[148:151],v[196:199],v[52:55]\nxW lgkmcnt(6)\nxT a[168:169],v246 offset:2560\nxC v201,v239,v240\n"
"xM v[24:27],a[152:155],v[192:195],v[24:27]\nxA s38,s45,11264\nxT a[170:171],v246 offset:6656\nxF v237,v140,v208,v208 op_sel:[0,0,1] op_sel_hi:[1,1,1]\n"
"xM v[56:59],a[152:155],v[196:199],v[56:59]\nxW lgkmcnt(6)\nxL v[172:175],v251,s[96:99],s38 offen sc1\n"
"xM v[28:31],a[156:159],v[192:195],v[28:31]\nxT a[172:173],v246 offset:2568\nxT a[174:175],v246 offset:6664\nxF v238,v140,v209,v209 op_sel:[1,0,1] op_sel_hi:[1,1,1]\n"
"xM v[60:63],a[156:159],v[196:199],v[60:63]\nxW lgkmcnt(6)\nxT a[176:177],v246 offset:3072\nxC v204,v237,v238\n"
"xM v[64:67],a[160:163],v[192:195],v[64:67]\nxT a[178:179],v246 offset:7168\nxF v239,v141,v210,v210 op_sel:[0,0,1] op_sel_hi:[1,1,1]\n"
"xM v[96:99],a[160:163],v[196:199],v[96:99]\nxW lgkmcnt(6)\nxT a[180:181],v246 offset:3080\nxF v240,v141,v211,v211 op_sel:[1,0,1] op_sel_hi:[1,1,1]\n"
"xM v[68:71],a[164:167],v[192:195],v[68:71]\nxW lgkmcnt(5)\nxT a[182:183],v246 offset:7176\nxC v205,v239,v240\n"
"xM v[100:103],a[164:167],v[196:199],v[100:103]\nxW vmcnt(22)\ns_barrier\n"
"xM v[72:75],a[168:171],v[192:195],v[72:75]\nxA m0,s42,40960\nxT a[184:185],v246 offset:3584\nxT a[186:187],v246 offset:7680\nxR v[208:211],v245 offset:1024\nxF v237,v138,v216,v216 op_sel:[0,0,1] op_sel_hi:[1,1,1]\n"
"xM v[104:107],a[168:171],v[196:199],v[104:107]\nxA s39,s43,2883584\nxT a[188:189],v246 offset:3592\nxF v238,v138,v217,v217 op_sel:[1,0,1] op_sel_hi:[1,1,1]\nxW lgkmcnt(8)\n"
"xM v[76:79],a[172:175],v[192:195],v[76:79]\nxL v235,s[20:23],s39 offen offset:0 lds\nxC v202,v237,v238\n"
"xM v[108:111],a[172:175],v[196:199],v[108:111]\nxW lgkmcnt(6)\nxT a[190:191],v246 offset:7688\nxT a[128:129],v246 offset:8192\nxF v239,v139,v218,v218 op_sel:[0,0,1] op_sel_hi:[1,1,1]\n"
"xM v[80:83],a[176:179],v[192:195],v[80:83]\nxT a[130:131],v246 offset:12288\nxF v240,v139,v219,v219 op_sel:[1,0,1] op_sel_hi:[1,1,1]\n"
"xM v[112:115],a[176:179],v[196:199],v[112:115]\nxW lgkmcnt(7)\nxT a[132:133],v246 offset:8200\nxC v203,v239,v240\n"
"xM v[84:87],a[180:183],v[192:195],v[84:87]\nxL v236,s[20:23],s39 offen offset:1024 lds\nxF v237,v142,v216,v216 op_sel:[0,0,1] op_sel_hi:[1,1,1]\n"
"xM v[116:119],a[180:183],v[196:199],v[116:119]\nxW lgkmcnt(6)\nxT a[134:135],v246 offset:12296\nxT a[136:137],v246 offset:8704\nxF v238,v142,v217,v217 op_sel:[1,0,1] op_sel_hi:[1,1,1]\n"
"xM v[88:91],a[184:187],v[192:195],v[88:91]\nxT a[138:139],v246 offset:12800\nxC v206,v237,v238\n"
"xM v[120:123],a[184:187],v[196:199],v[120:123]\nxW lgkmcnt(6)\nxT a[140:141],v246 offset:8712\nxF v239,v143,v218,v218 op_sel:[0,0,1] op_sel_hi:[1,1,1]\n"
"xM v[92:95],a[188:191],v[192:195],v[92:95]\nxL v252,s[20:23],s39 offen offset:2048 lds\nxF v240,v143,v219,v219 op_sel:[1,0,1] op_sel_hi:[1,1,1]\nxC v207,v239,v240\n"
"xM v[124:127],a[188:191],v[196:199],v[124:127]\nxW lgkmcnt(5)\nxT a[142:143],v246 offset:12808\nxT a[144:145],v246 offset:9216\n"
"xM v[0:3],a[128:131],v[200:203],v[0:3]\nxT a[146:147],v246 offset:13312\nxR v[216:219],v245 offset:1040\n"
"xM v[32:35],a[128:131],v[204:207],v[32:35]\nxW lgkmcnt(7)\nxT a[148:149],v246 offset:9224\nxF v237,v144,v208,v208 op_sel:[0,0,1] op_sel_hi:[1,1,1]\n"
"xM v[4:7],a[132:135],v[200:203],v[4:7]\nxL v247,s[20:23],s39 offen offset:3072 lds\nxM v[36:39],a[132:135],v[204:207],v[36:39]\nxW lgkmcnt(6)\nxT a[150:151],v246 offset:13320\nxT a[152:153],v246 offset:9728\n"
"xM v[8:11],a[136:139],v[200:203],v[8:11]\nxW lgkmcnt(6)\nxT a[154:155],v246 offset:13824\nxF v238,v144,v209,v209 op_sel:[1,0,1] op_sel_hi:[1,1,1]\n"
"xM v[40:43],a[136:139],v[204:207],v[40:43]\nxA s38,s47,12288\nxT a[156:157],v246 offset:9736\nxC v176,v237,v238\n"
"xM v[12:15],a[140:143],v[200:203],v[12:15]\nxL v[128:131],v251,s[96:99],s38 offen sc1\nxM v[44:47],a[140:143],v[204:207],v[44:47]\nxW lgkmcnt(6)\nxT a[158:159],v246 offset:13832\nxT a[160:161],v246 offset:10240\n"
"xM v[16:19],a[144:147],v[200:203],v[16:19]\nxW lgkmcnt(5)\nxT a[162:163],v246 offset:14336\nxF v239,v145,v210,v210 op_sel:[0,0,1] op_sel_hi:[1,1,1]\n"
"xM v[48:51],a[144:147],v[204:207],v[48:51]\nxA s38,s45,12288\nxT a[164:165],v246 offset:10248\nxF v240,v145,v211,v211 op_sel:[1,0,1] op_sel_hi:[1,1,1]\n"
"xM v[20:23],a[148:151],v[200:203],v[20:23]\nxL v[132:135],v251,s[96:99],s38 offen sc1\nxM v[52:55],a[148:151],v[204:207],v[52:55]\nxW lgkmcnt(5)\nxT a[166:167],v246 offset:14344\nxT a[168:169],v246 offset:10752\n"
"xM v[24:27],a[152:155],v[200:203],v[24:27]\nxT a[170:171],v246 offset:14848\nxC v177,v239,v240\n"
"xM v[56:59],a[152:155],v[204:207],v[56:59]\nxW lgkmcnt(6)\nxT a[172:173],v246 offset:10760\nxF v237,v148,v208,v208 op_sel:[0,0,1] op_sel_hi:[1,1,1]\n"
"xM v[28:31],a[156:159],v[200:203],v[28:31]\nxT a[174:175],v246 offset:14856\nxF v238,v148,v209,v209 op_sel:[1,0,1] op_sel_hi:[1,1,1]\n"
"xM v[60:63],a[156:159],v[204:207],v[60:63]\nxW lgkmcnt(6)\nxT a[176:177],v246 offset:11264\nxC v180,v237,v238\n"
"xM v[64:67],a[160:163],v[200:203],v[64:67]\nTCH0_2_%=:\ns_cbranch_execz TC0_2_1_%=\nxT a[178:179],v246 offset:15360\nxF v239,v149,v210,v210 op_sel:[0,0,1] op_sel_hi:[1,1,1]\n"
"xM v[96:99],a[160:163],v[204:207],v[96:99]\nxW lgkmcnt(6)\nxT a[180:181],v246 offset:11272\nxF v240,v149,v211,v211 op_sel:[1,0,1] op_sel_hi:[1,1,1]\n"
"xM v[68:71],a[164:167],v[200:203],v[68:71]\nxW lgkmcnt(5)\nxT a[182:183],v246 offset:15368\nxC v181,v239,v240\n"
"xM v[100:103],a[164:167],v[204:207],v[100:103]\nxW vmcnt(22)\ns_barrier\n"
"xM v[72:75],a[168:171],v[200:203],v[72:75]\nxA m0,s42,0\nxT a[184:185],v246 offset:11776\nxT a[186:187],v246 offset:15872\nxR v[208:211],v245 offset:1152\nxF v237,v146,v216,v216 op_sel:[0,0,1] op_sel_hi:[1,1,1]\n"
"xM v[104:107],a[168:171],v[204:207],v[104:107]\nxA s39,s43,3145728\nxT a[188:189],v246 offset:11784\nxF v238,v146,v217,v217 op_sel:[1,0,1] op_sel_hi:[1,1,1]\nxW lgkmcnt(8)\n"
"xM v[76:79],a[172:175],v[200:203],v[76:79]\nxL v235,s[20:23],s39 offen offset:0 lds\nxC v178,v237,v238\n"
"xM v[108:111],a[172:175],v[204:207],v[108:111]\nxW lgkmcnt(6)\nxT a[190:191],v246 offset:15880\nxT a[128:129],v246 offset:16384\nxF v239,v147,v218,v218 op_sel:[0,0,1] op_sel_hi:[1,1,1]\n"
"xM v[80:83],a[176:179],v[200:203],v[80:83]\nxT a[130:131],v246 offset:20480\nxF v240,v147,v219,v219 op_sel:[1,0,1] op_sel_hi:[1,1,1]\n"
"xM v[112:115],a[176:179],v[204:207],v[112:115]\nxW lgkmcnt(7)\nxT a[132:133],v246 offset:16392\nxC v179,v239,v240\n"
"xM v[84:87],a[180:183],v[200:203],v[84:87]\nxL v236,s[20:23],s39 offen offset:1024 lds\nxF v237,v150,v216,v216 op_sel:[0,0,1] op_sel_hi:[1,1,1]\n"
"xM v[116:119],a[180:183],v[204:207],v[116:119]\nxW lgkmcnt(6)\nxT a[134:135],v246 offset:20488\nxT a[136:137],v246 offset:16896\nxF v238,v150,v217,v217 op_sel:[1,0,1] op_sel_hi:[1,1,1]\n"
"xM v[88:91],a[184:187],v[200:203],v[88:91]\nxT a[138:139],v246 offset:20992\nxC v182,v237,v238\n"
"xM v[120:123],a[184:187],v[204:207],v[120:123]\nxW lgkmcnt(6)\nxT a[140:141],v246 offset:16904\nxF v239,v151,v218,v218 op_sel:[0,0,1] op_sel_hi:[1,1,1]\n"
"xM v[92:95],a[188:191],v[200:203],v[92:95]\nxL v252,s[20:23],s39 offen offset:2048 lds\nxF v240,v151,v219,v219 op_sel:[1,0,1] op_sel_hi:[1,1,1]\nxC v183,v239,v240\n"
"xM v[124:127],a[188:191],v[204:207],v[124:127]\nxW lgkmcnt(5)\nxT a[142:143],v246 offset:21000\nxT a[144:145],v246 offset:17408\n"
"xM v[0:3],a[128:131],v[176:179],v[0:3]\nxT a[146:147],v246 offset:21504\nxR v[216:219],v245 offset:1168\n"
"xM v[32:35],a[128:131],v[180:183],v[32:35]\nxW lgkmcnt(7)\nxT a[148:149],v246 offset:17416\nxF v237,v152,v208,v208 op_sel:[0,0,1] op_sel_hi:[1,1,1]\n"
"xM v[4:7],a[132:135],v[176:179],v[4:7]\nxL v247,s[20:23],s39 offen offset:3072 lds\nxM v[36:39],a[132:135],v[180:183],v[36:39]\nxW lgkmcnt(6)\nxT a[150:151],v246 offset:21512\nxT a[152:153],v246 offset:17920\n"
"xM v[8:11],a[136:139],v[176:179],v[8:11]\nxW lgkmcnt(6)\nxT a[154:155],v246 offset:22016\nxF v238,v152,v209,v209 op_sel:[1,0,1] op_sel_hi:[1,1,1]\n"
"xM v[40:43],a[136:139],v[180:183],v[40:43]\nxA s38,s47,13312\nxT a[156:157],v246 offset:17928\nxC v184,v237,v238\n"
"xM v[12:15],a[140:143],v[176:179],v[12:15]\nxL v[136:139],v251,s[96:99],s38 offen sc1\nxM v[44:47],a[140:143],v[180:183],v[44:47]\nxW lgkmcnt(6)\nxT a[158:159],v246 offset:22024\nxT a[160:161],v246 offset:18432\n"
"xM v[16:19],a[144:147],v[176:179],v[16:19]\nxW lgkmcnt(5)\nxT a[162:163],v246 offset:22528\nxF v239,v153,v210,v210 op_sel:[0,0,1] op_sel_hi:[1,1,1]\n"
"xM v[48:51],a[144:147],v[180:183],v[48:51]\nxA s38,s45,13312\nxT a[164:165],v246 offset:18440\nxF v240,v153,v211,v211 op_sel:[1,0,1] op_sel_hi:[1,1,1]\n"
"xM v[20:23],a[148:151],v[176:179],v[20:23]\nxL v[140:143],v251,s[96:99],s38 offen sc1\nxM v[52:55],a[148:151],v[180:183],v[52:55]\nxW lgkmcnt(5)\nxT a[166:167],v246 offset:22536\nxT a[168:169],v246 offset:18944\n"
"xM v[24:27],a[152:155],v[176:179],v[24:27]\nxT a[170:171],v246 offset:23040\nxC v185,v239,v240\n"
"xM v[56:59],a[152:155],v[180:183],v[56:59]\nxW lgkmcnt(6)\nxT a[172:173],v246 offset:18952\nxF v237,v156,v208,v208 op_sel:[0,0,1] op_sel_hi:[1,1,1]\n"
"xM v[28:31],a[156:159],v[176:179],v[28:31]\nxT a[174:175],v246 offset:23048\nxF v238,v156,v209,v209 op_sel:[1,0,1] op_sel_hi:[1,1,1]\n"
"xM v[60:63],a[156:159],v[180:183],v[60:63]\nxW lgkmcnt(6)\nxT a[176:177],v246 offset:19456\nxC v188,v237,v238\n"
"xM v[64:67],a[160:163],v[176:179],v[64:67]\nxT a[178:179],v246 offset:23552\nxF v239,v157,v210,v210 op_sel:[0,0,1] op_sel_hi:[1,1,1]\n"
"xM v[96:99],a[160:163],v[180:183],v[96:99]\nxW lgkmcnt(6)\nxT a[180:181],v246 offset:19464\nxF v240,v157,v211,v211 op_sel:[1,0,1] op_sel_hi:[1,1,1]\n"
"xM v[68:71],a[164:167],v[176:179],v[68:71]\nxW lgkmcnt(5)\nxT a[182:183],v246 offset:23560\nxC v189,v239,v240\n"
"xM v[100:103],a[164:167],v[180:183],v[100:103]\nxW vmcnt(18)\ns_barrier\n"
"xM v[72:75],a[168:171],v[176:179],v[72:75]\nxA m0,s42,8192\nxT a[184:185],v246 offset:19968\nxT a[186:187],v246 offset:24064\nxR v[208:211],v245 offset:1280\nxF v237,v154,v216,v216 op_sel:[0,0,1] op_sel_hi:[1,1,1]\n"
"xM v[104:107],a[168:171],v[180:183],v[104:107]\nxA s39,s43,3407872\nxT a[188:189],v246 offset:19976\nxF v238,v154,v217,v217 op_sel:[1,0,1] op_sel_hi:[1,1,1]\nxW lgkmcnt(8)\n"
"xM v[76:79],a[172:175],v[176:179],v[76:79]\nxL v235,s[20:23],s39 offen offset:0 lds\nxC v186,v237,v238\n"
"xM v[108:111],a[172:175],v[180:183],v[108:111]\nxW lgkmcnt(6)\nxT a[190:191],v246 offset:24072\nxT a[128:129],v246 offset:24576\nxF v239,v155,v218,v218 op_sel:[0,0,1] op_sel_hi:[1,1,1]\n"
"xM v[80:83],a[176:179],v[176:179],v[80:83]\nxT a[130:131],v246 offset:28672\nxF v240,v155,v219,v219 op_sel:[1,0,1] op_sel_hi:[1,1,1]\n"
"xM v[112:115],a[176:179],v[180:183],v[112:115]\nxW lgkmcnt(7)\nxT a[132:133],v246 offset:24584\nxC v187,v239,v240\n"
"xM v[84:87],a[180:183],v[176:179],v[84:87]\nxL v236,s[20:23],s39 offen offset:1024 lds\nxF v237,v158,v216,v216 op_sel:[0,0,1] op_sel_hi:[1,1,1]\n"
"xM v[116:119],a[180:183],v[180:183],v[116:119]\nxW lgkmcnt(6)\nxT a[134:135],v246 offset:28680\nxT a[136:137],v246 offset:25088\nxF v238,v158,v217,v217 op_sel:[1,0,1] op_sel_hi:[1,1,1]\n"
"xM v[88:91],a[184:187],v[176:179],v[88:91]\nxT a[138:139],v246 offset:29184\nxC v190,v237,v238\n"
"xM v[120:123],a[184:187],v[180:183],v[120:123]\nxW lgkmcnt(6)\nxT a[140:141],v246 offset:25096\nxF v239,v159,v218,v218 op_sel:[0,0,1] op_sel_hi:[1,1,1]\n"
"xM v[92:95],a[188:191],v[176:179],v[92:95]\nxL v252,s[20:23],s39 offen offset:2048 lds\nxF v240,v159,v219,v219 op_sel:[1,0,1] op_sel_hi:[1,1,1]\nxC v191,v239,v240\n"
"xM v[124:127],a[188:191],v[180:183],v[124:127]\nxW lgkmcnt(5)\nxT a[142:143],v246 offset:29192\nxT a[144:145],v246 offset:25600\n"
"xM v[0:3],a[128:131],v[184:187],v[0:3]\nxT a[146:147],v246 offset:29696\nxR v[216:219],v245 offset:1296\n"
"xM v[32:35],a[128:131],v[188:191],v[32:35]\nxW lgkmcnt(7)\nxT a[148:149],v246 offset:25608\nxF v237,v160,v208,v208 op_sel:[0,0,1] op_sel_hi:[1,1,1]\n"
"xM v[4:7],a[132:135],v[184:187],v[4:7]\nxL v247,s[20:23],s39 offen offset:3072 lds\nxM v[36:39],a[132:135],v[188:191],v[36:39]\nxW lgkmcnt(6)\nxT a[150:151],v246 offset:29704\nxT a[152:153],v246 offset:26112\n"
"xM v[8:11],a[136:139],v[184:187],v[8:11]\nxW lgkmcnt(6)\nxT a[154:155],v246 offset:30208\nxF v238,v160,v209,v209 op_sel:[1,0,1] op_sel_hi:[1,1,1]\n"
"xM v[40:43],a[136:139],v[188:191],v[40:43]\nxA s38,s47,14336\nxT a[156:157],v246 offset:26120\nxC v192,v237,v238\n"
"xM v[12:15],a[140:143],v[184:187],v[12:15]\nxL v[144:147],v251,s[96:99],s38 offen sc1\nxM v[44:47],a[140:143],v[188:191],v[44:47]\nxW lgkmcnt(6)\nxT a[158:159],v246 offset:30216\nxT a[160:161],v246 offset:26624\n"
"xM v[16:19],a[144:147],v[184:187],v[16:19]\nxW lgkmcnt(5)\nxT a[162:163],v246 offset:30720\nxF v239,v161,v210,v210 op_sel:[0,0,1] op_sel_hi:[1,1,1]\n"
"xM v[48:51],a[144:147],v[188:191],v[48:51]\nxA s38,s45,14336\nxT a[164:165],v246 offset:26632\nxF v240,v161,v211,v211 op_sel:[1,0,1] op_sel_hi:[1,1,1]\n"
"xM v[20:23],a[148:151],v[184:187],v[20:23]\nxL v[148:151],v251,s[96:99],s38 offen sc1\nxM v[52:55],a[148:151],v[188:191],v[52:55]\nxW lgkmcnt(5)\nxT a[166:167],v246 offset:30728\nxT a[168:169],v246 offset:27136\n"
"xM v[24:27],a[152:155],v[184:187],v[24:27]\nxW lgkmcnt(5)\nxT a[170:171],v246 offset:31232\nxC v193,v239,v240\n"
"xM v[56:59],a[152:155],v[188:191],v[56:59]\nxA s38,s47,15360\nxT a[172:173],v246 offset:27144\nxF v237,v164,v208,v208 op_sel:[0,0,1] op_sel_hi:[1,1,1]\n"
"xM v[28:31],a[156:159],v[184:187],v[28:31]\nxL v[152:155],v251,s[96:99],s38 offen sc1\nxF v238,v164,v209,v209 op_sel:[1,0,1] op_sel_hi:[1,1,1]\n"
"xM v[60:63],a[156:159],v[188:191],v[60:63]\nxW lgkmcnt(5)\nxT a[174:175],v246 offset:31240\nxT a[176:177],v246 offset:27648\nxC v196,v237,v238\n"
"xM v[64:67],a[160:163],v[184:187],v[64:67]\nxW lgkmcnt(5)\nxT a[178:179],v246 offset:31744\nxF v239,v165,v210,v210 op_sel:[0,0,1] op_sel_hi:[1,1,1]\n"
"xM v[96:99],a[160:163],v[188:191],v[96:99]\nxA s38,s45,15360\nxT a[180:181],v246 offset:27656\nxF v240,v165,v211,v211 op_sel:[1,0,1] op_sel_hi:[1,1,1]\n"
"xM v[68:71],a[164:167],v[184:187],v[68:71]\nxW lgkmcnt(5)\nxL v[156:159],v251,s[96:99],s38 offen sc1\nxC v197,v239,v240\n"
"xM v[100:103],a[164:167],v[188:191],v[100:103]\nxW vmcnt(20)\ns_barrier\n"
"xM v[72:75],a[168:171],v[184:187],v[72:75]\nxT a[182:183],v246 offset:31752\nxT a[184:185],v246 offset:28160\nxR v[208:211],v245 offset:1408\nxF v237,v162,v216,v216 op_sel:[0,0,1] op_sel_hi:[1,1,1]\n"
"xM v[104:107],a[168:171],v[188:191],v[104:107]\nxW lgkmcnt(6)\nxT a[186:187],v246 offset:32256\nxT a[188:189],v246 offset:28168\nxF v238,v162,v217,v217 op_sel:[1,0,1] op_sel_hi:[1,1,1]\n"
"xM v[76:79],a[172:175],v[184:187],v[76:79]\nxT a[190:191],v246 offset:32264\nxC v194,v237,v238\n"
"xM v[108:111],a[172:175],v[188:191],v[108:111]\nxW lgkmcnt(7)\nxT a[128:129],v246 offset:32768\nxF v239,v163,v218,v218 op_sel:[0,0,1] op_sel_hi:[1,1,1]\n"
"xM v[80:83],a[176:179],v[184:187],v[80:83]\nxT a[130:131],v246 offset:36864\nxF v240,v163,v219,v219 op_sel:[1,0,1] op_sel_hi:[1,1,1]\n"
"xM v[112:115],a[176:179],v[188:191],v[112:115]\nxW lgkmcnt(7)\nxT a[132:133],v246 offset:32776\nxC v195,v239,v240\n"
"xM v[84:87],a[180:183],v[184:187],v[84:87]\nxT a[134:135],v246 offset:36872\nxF v237,v166,v216,v216 op_sel:[0,0,1] op_sel_hi:[1,1,1]\n"
"xM v[116:119],a[180:183],v[188:191],v[116:119]\nxW lgkmcnt(6)\nxT a[136:137],v246 offset:33280\nxF v238,v166,v217,v217 op_sel:[1,0,1] op_sel_hi:[1,1,1]\n"
"xM v[88:91],a[184:187],v[184:187],v[88:91]\nxT a[138:139],v246 offset:37376\nxC v198,v237,v238\n"
"xM v[120:123],a[184:187],v[188:191],v[120:123]\nxW lgkmcnt(6)\nxT a[140:141],v246 offset:33288\nxF v239,v167,v218,v218 op_sel:[0,0,1] op_sel_hi:[1,1,1]\n"
"xM v[92:95],a[188:191],v[184:187],v[92:95]\nxT a[142:143],v246 offset:37384\nxF v240,v167,v219,v219 op_sel:[1,0,1] op_sel_hi:[1,1,1]\nxC v199,v239,v240\n"
"xM v[124:127],a[188:191],v[188:191],v[124:127]\nxW lgkmcnt(6)\nxT a[144:145],v246 offset:33792\nxR v[216:219],v245 offset:1424\n"
"xM v[0:3],a[128:131],v[192:195],v[0:3]\nxW lgkmcnt(6)\nxT a[146:147],v246 offset:37888\nxF v237,v168,v208,v208 op_sel:[0,0,1] op_sel_hi:[1,1,1]\n"
"xM v[32:35],a[128:131],v[196:199],v[32:35]\nxA s38,s47,16384\nxT a[148:149],v246 offset:33800\nxF v238,v168,v209,v209 op_sel:[1,0,1] op_sel_hi:[1,1,1]\n"
"xM v[4:7],a[132:135],v[192:195],v[4:7]\nxL v[160:163],v251,s[96:99],s38 offen sc1\nxM v[36:39],a[132:135],v[196:199],v[36:39]\nxW lgkmcnt(6)\nxT a[150:151],v246 offset:37896\nxT a[152:153],v246 offset:34304\n"
"xM v[8:11],a[136:139],v[192:195],v[8:11]\nxW lgkmcnt(6)\nxT a[154:155],v246 offset:38400\nxC v200,v237,v238\n"
"xM v[40:43],a[136:139],v[196:199],v[40:43]\nxA s38,s45,16384\nxT a[156:157],v246 offset:34312\nxF v239,v169,v210,v210 op_sel:[0,0,1] op_sel_hi:[1,1,1]\n"
"xM v[12:15],a[140:143],v[192:195],v[12:15]\nxL v[164:167],v251,s[96:99],s38 offen sc1\nxM v[44:47],a[140:143],v[196:199],v[44:47]\nxW lgkmcnt(5)\nxT a[158:159],v246 offset:38408\nxT a[160:161],v246 offset:34816\n"
"xM v[16:19],a[144:147],v[192:195],v[16:19]\nxA m0,s42,16384\nxT a[162:163],v246 offset:38912\nxF v240,v169,v211,v211 op_sel:[1,0,1] op_sel_hi:[1,1,1]\n"
"xM v[48:51],a[144:147],v[196:199],v[48:51]\nxA s39,s43,3670016\nxT a[164:165],v246 offset:34824\nxC v201,v239,v240\nxW lgkmcnt(7)\n"
"xM v[20:23],a[148:151],v[192:195],v[20:23]\nxL v235,s[20:23],s39 offen offset:0 lds\nxF v237,v172,v208,v208 op_sel:[0,0,1] op_sel_hi:[1,1,1]\n"
"xM v[52:55],a[148:151],v[196:199],v[52:55]\nxW lgkmcnt(5)\nxT a[166:167],v246 offset:38920\nxT a[168:169],v246 offset:35328\nxF v238,v172,v209,v209 op_sel:[1,0,1] op_sel_hi:[1,1,1]\n"
"xM v[24:27],a[152:155],v[192:195],v[24:27]\nxT a[170:171],v246 offset:39424\nxC v204,v237,v238\n"
"xM v[56:59],a[152:155],v[196:199],v[56:59]\nxW lgkmcnt(6)\nxT a[172:173],v246 offset:35336\nxF v239,v173,v210,v210 op_sel:[0,0,1] op_sel_hi:[1,1,1]\n"
"xM v[28:31],a[156:159],v[192:195],v[28:31]\nxL v236,s[20:23],s39 offen offset:1024 lds\nxF v240,v173,v211,v211 op_sel:[1,0,1] op_sel_hi:[1,1,1]\nxC v205,v239,v240\n"
"xM v[60:63],a[156:159],v[196:199],v[60:63]\nxW lgkmcnt(5)\nxT a[174:175],v246 offset:39432\nxT a[176:177],v246 offset:35840\nxR v[208:211],v245 offset:1536\n"
"xM v[64:67],a[160:163],v[192:195],v[64:67]\nxT a[178:179],v246 offset:39936\nxF v237,v170,v216,v216 op_sel:[0,0,1] op_sel_hi:[1,1,1]\n"
"xM v[96:99],a[160:163],v[196:199],v[96:99]\nxW lgkmcnt(7)\nxT a[180:181],v246 offset:35848\nxF v238,v170,v217,v217 op_sel:[1,0,1] op_sel_hi:[1,1,1]\n"
"xM v[68:71],a[164:167],v[192:195],v[68:71]\nxW lgkmcnt(6)\nxL v252,s[20:23],s39 offen offset:2048 lds\nxC v202,v237,v238\n"
"xM v[100:103],a[164:167],v[196:199],v[100:103]\nxW vmcnt(19)\ns_barrier\n"
"xM v[72:75],a[168:171],v[192:195],v[72:75]\nxT a[182:183],v246 offset:39944\nxT a[184:185],v246 offset:36352\nxF v239,v171,v218,v218 op_sel:[0,0,1] op_sel_hi:[1,1,1]\nxF v240,v171,v219,v219 op_sel:[1,0,1] op_sel_hi:[1,1,1]\n"
"xM v[104:107],a[168:171],v[196:199],v[104:107]\nxW lgkmcnt(6)\nxT a[186:187],v246 offset:40448\nxT a[188:189],v246 offset:36360\nxC v203,v239,v240\n"
"xM v[76:79],a[172:175],v[192:195],v[76:79]\nxL v247,s[20:23],s39 offen offset:3072 lds\nxF v237,v174,v216,v216 op_sel:[0,0,1] op_sel_hi:[1,1,1]\n"
"xM v[108:111],a[172:175],v[196:199],v[108:111]\nxW lgkmcnt(5)\nxT a[190:191],v246 offset:40456\nxT a[128:129],v246 offset:40960\nxF v238,v174,v217,v217 op_sel:[1,0,1] op_sel_hi:[1,1,1]\n"
"xM v[80:83],a[176:179],v[192:195],v[80:83]\nxT a[130:131],v246 offset:45056\nxC v206,v237,v238\n"
"xM v[112:115],a[176:179],v[196:199],v[112:115]\nxW lgkmcnt(6)\nxT a[132:133],v246 offset:40968\nxF v239,v175,v218,v218 op_sel:[0,0,1] op_sel_hi:[1,1,1]\n"
"xM v[84:87],a[180:183],v[192:195],v[84:87]\nxT a[134:135],v246 offset:45064\nxF v240,v175,v219,v219 op_sel:[1,0,1] op_sel_hi:[1,1,1]\n"
"xM v[116:119],a[180:183],v[196:199],v[116:119]\nxW lgkmcnt(6)\nxT a[136:137],v246 offset:41472\nxC v207,v239,v240\n"
"xM v[88:91],a[184:187],v[192:195],v[88:91]\nxT a[138:139],v246 offset:45568\nxR v[216:219],v245 offset:1552\n"
"xM v[120:123],a[184:187],v[196:199],v[120:123]\nxW lgkmcnt(7)\nxT a[140:141],v246 offset:41480\nxF v237,v128,v208,v208 op_sel:[0,0,1] op_sel_hi:[1,1,1]\n"
"xM v[92:95],a[188:191],v[192:195],v[92:95]\nxT a[142:143],v246 offset:45576\nxF v238,v128,v209,v209 op_sel:[1,0,1] op_sel_hi:[1,1,1]\n"
"xM v[124:127],a[188:191],v[196:199],v[124:127]\nxW lgkmcnt(7)\nxT a[144:145],v246 offset:41984\nxC v176,v237,v238\n"
"xM v[0:3],a[128:131],v[200:203],v[0:3]\nxW lgkmcnt(6)\nxT a[146:147],v246 offset:46080\nxF v239,v129,v210,v210 op_sel:[0,0,1] op_sel_hi:[1,1,1]\n"
"xM v[32:35],a[128:131],v[204:207],v[32:35]\nxA s38,s47,17408\nxT a[148:149],v246 offset:41992\nxF v240,v129,v211,v211 op_sel:[1,0,1] op_sel_hi:[1,1,1]\n"
"xM v[4:7],a[132:135],v[200:203],v[4:7]\nxL v[168:171],v251,s[96:99],s38 offen sc1\nxC v177,v239,v240\nxF v237,v132,v208,v208 op_sel:[0,0,1] op_sel_hi:[1,1,1]\n"
"xM v[36:39],a[132:135],v[204:207],v[36:39]\nxW lgkmcnt(6)\nxT a[150:151],v246 offset:46088\nxT a[152:153],v246 offset:42496\nxF v238,v132,v209,v209 op_sel:[1,0,1] op_sel_hi:[1,1,1]\n"
"xM v[8:11],a[136:139],v[200:203],v[8:11]\nxW lgkmcnt(5)\nxT a[154:155],v246 offset:46592\nxC v180,v237,v238\n"
"xM v[40:43],a[136:139],v[204:207],v[40:43]\nxA s38,s45,17408\nxT a[156:157],v246 offset:42504\nxF v239,v133,v210,v210 op_sel:[0,0,1] op_sel_hi:[1,1,1]\n"
"xM v[12:15],a[140:143],v[200:203],v[12:15]\nxL v[172:175],v251,s[96:99],s38 offen sc1\nxF v240,v133,v211,v211 op_sel:[1,0,1] op_sel_hi:[1,1,1]\n"
"xM v[44:47],a[140:143],v[204:207],v[44:47]\nxW lgkmcnt(5)\nxT a[158:159],v246 offset:46600\nxT a[160:161],v246 offset:43008\nxC v181,v239,v240\n"
"xM v[16:19],a[144:147],v[200:203],v[16:19]\nxA m0,s42,24576\nxT a[162:163],v246 offset:47104\nxR v[208:211],v245 offset:1664\n"
"xM v[48:51],a[144:147],v[204:207],v[48:51]\nxA s39,s43,3932160\nxT a[164:165],v246 offset:43016\nxF v237,v130,v216,v216 op_sel:[0,0,1] op_sel_hi:[1,1,1]\nxW lgkmcnt(8)\n"
"xM v[20:23],a[148:151],v[200:203],v[20:23]\nxL v235,s[20:23],s39 offen offset:0 lds\nxF v238,v130,v217,v217 op_sel:[1,0,1] op_sel_hi:[1,1,1]\n"
"xM v[52:55],a[148:151],v[204:207],v[52:55]\nxW lgkmcnt(6)\nxT a[166:167],v246 offset:47112\nxT a[168:169],v246 offset:43520\nxC v178,v237,v238\n"
"xM v[24:27],a[152:155],v[200:203],v[24:27]\nxT a[170:171],v246 offset:47616\nxF v239,v131,v218,v218 op_sel:[0,0,1] op_sel_hi:[1,1,1]\n"
"xM v[56:59],a[152:155],v[204:207],v[56:59]\nxW lgkmcnt(7)\nxT a[172:173],v246 offset:43528\nxF v240,v131,v219,v219 op_sel:[1,0,1] op_sel_hi:[1,1,1]\n"
"xM v[28:31],a[156:159],v[200:203],v[28:31]\nxL v236,s[20:23],s39 offen offset:1024 lds\nxC v179,v239,v240\n"
"xM v[60:63],a[156:159],v[204:207],v[60:63]\nxW lgkmcnt(6)\nxT a[174:175],v246 offset:47624\nxT a[176:177],v246 offset:44032\nxF v237,v134,v216,v216 op_sel:[0,0,1] op_sel_hi:[1,1,1]\n"
"xM v[64:67],a[160:163],v[200:203],v[64:67]\nxT a[178:179],v246 offset:48128\nxF v238,v134,v217,v217 op_sel:[1,0,1] op_sel_hi:[1,1,1]\n"
"xM v[96:99],a[160:163],v[204:207],v[96:99]\nxW lgkmcnt(6)\nxT a[180:181],v246 offset:44040\nxC v182,v237,v238\n"
"xM v[68:71],a[164:167],v[200:203],v[68:71]\nxW lgkmcnt(5)\nxL v252,s[20:23],s39 offen offset:2048 lds\nxF v239,v135,v218,v218 op_sel:[0,0,1] op_sel_hi:[1,1,1]\n"
"xM v[100:103],a[164:167],v[204:207],v[100:103]\nxW vmcnt(11)\ns_barrier\n"
"xM v[72:75],a[168:171],v[200:203],v[72:75]\nxT a[182:183],v246 offset:48136\nxT a[184:185],v246 offset:44544\nxF v240,v135,v219,v219 op_sel:[1,0,1] op_sel_hi:[1,1,1]\nxC v183,v239,v240\n"
"xM v[104:107],a[168:171],v[204:207],v[104:107]\nxW lgkmcnt(5)\nxT a[186:187],v246 offset:48640\nxT a[188:189],v246 offset:44552\nxR v[216:219],v245 offset:1680\n"
"xM v[76:79],a[172:175],v[200:203],v[76:79]\nxL v247,s[20:23],s39 offen offset:3072 lds\nxF v237,v136,v208,v208 op_sel:[0,0,1] op_sel_hi:[1,1,1]\nxF v238,v136,v209,v209 op_sel:[1,0,1] op_sel_hi:[1,1,1]\n"
"xM v[108:111],a[172:175],v[204:207],v[108:111]\nxW lgkmcnt(6)\nxT a[190:191],v246 offset:48648\nxT a[128:129],v246 offset:0\nxC v184,v237,v238\n"
"xM v[80:83],a[176:179],v[200:203],v[80:83]\nxA m0,s42,32768\nxT a[130:131],v246 offset:4096\nxF v239,v137,v210,v210 op_sel:[0,0,1] op_sel_hi:[1,1,1]\n"
"xM v[112:115],a[176:179],v[204:207],v[112:115]\nxA s39,s43,4194304\nxT a[132:133],v246 offset:8\nxF v240,v137,v211,v211 op_sel:[1,0,1] op_sel_hi:[1,1,1]\nxW lgkmcnt(8)\n"
"xM v[84:87],a[180:183],v[200:203],v[84:87]\nxL v235,s[20:23],s39 offen offset:0 lds\nxC v185,v239,v240\n"
"xM v[116:119],a[180:183],v[204:207],v[116:119]\nxW lgkmcnt(6)\nxT a[134:135],v246 offset:4104\nxT a[136:137],v246 offset:512\nxF v237,v140,v208,v208 op_sel:[0,0,1] op_sel_hi:[1,1,1]\n"
"xM v[88:91],a[184:187],v[200:203],v[88:91]\nxT a[138:139],v246 offset:4608\nxF v238,v140,v209,v209 op_sel:[1,0,1] op_sel_hi:[1,1,1]\n"
"xM v[120:123],a[184:187],v[204:207],v[120:123]\nxW lgkmcnt(6)\nxT a[140:141],v246 offset:520\nxC v188,v237,v238\n"
"xM v[92:95],a[188:191],v[200:203],v[92:95]\nxL v236,s[20:23],s39 offen offset:1024 lds\nxF v239,v141,v210,v210 op_sel:[0,0,1] op_sel_hi:[1,1,1]\n"
"xM v[124:127],a[188:191],v[204:207],v[124:127]\nxW lgkmcnt(5)\nxT a[142:143],v246 offset:4616\nxT a[144:145],v246 offset:1024\nxF v240,v141,v211,v211 op_sel:[1,0,1] op_sel_hi:[1,1,1]\n"
"xM v[0:3],a[128:131],v[176:179],v[0:3]\nxT a[146:147],v246 offset:5120\nxC v189,v239,v240\n"
"xM v[32:35],a[128:131],v[180:183],v[32:35]\nxW lgkmcnt(6)\nxT a[148:149],v246 offset:1032\nxR v[208:211],v245 offset:1792\n"
"xM v[4:7],a[132:135],v[176:179],v[4:7]\nTCH0_3_%=:\ns_cbranch_execz TC0_3_1_%=\nxL v252,s[20:23],s39 offen offset:2048 lds\nxF v237,v138,v216,v216 op_sel:[0,0,1] op_sel_hi:[1,1,1]\n"
"xM v[36:39],a[132:135],v[180:183],v[36:39]\nxW lgkmcnt(6)\nxT a[150:151],v246 offset:5128\nxT a[152:153],v246 offset:1536\nxF v238,v138,v217,v217 op_sel:[1,0,1] op_sel_hi:[1,1,1]\n"
"xM v[8:11],a[136:139],v[176:179],v[8:11]\nxT a[154:155],v246 offset:5632\nxC v186,v237,v238\n"
"xM v[40:43],a[136:139],v[180:183],v[40:43]\nxW lgkmcnt(7)\nxT a[156:157],v246 offset:1544\nxF v239,v139,v218,v218 op_sel:[0,0,1] op_sel_hi:[1,1,1]\n"
"xM v[12:15],a[140:143],v[176:179],v[12:15]\nxL v247,s[20:23],s39 offen offset:3072 lds\nxF v240,v139,v219,v219 op_sel:[1,0,1] op_sel_hi:[1,1,1]\n"
"xM v[44:47],a[140:143],v[180:183],v[44:47]\nxW lgkmcnt(6)\nxT a[158:159],v246 offset:5640\nxT a[160:161],v246 offset:2048\nxC v187,v239,v240\n"
"xM v[16:19],a[144:147],v[176:179],v[16:19]\nxT a[162:163],v246 offset:6144\nxF v237,v142,v216,v216 op_sel:[0,0,1] op_sel_hi:[1,1,1]\n"
"xM v[48:51],a[144:147],v[180:183],v[48:51]\nxW lgkmcnt(6)\nxT a[164:165],v246 offset:2056\nxF v238,v142,v217,v217 op_sel:[1,0,1] op_sel_hi:[1,1,1]\n"
"xM v[20:23],a[148:151],v[176:179],v[20:23]\nxT a[166:167],v246 offset:6152\nxC v190,v237,v238\nxF v239,v143,v218,v218 op_sel:[0,0,1] op_sel_hi:[1,1,1]\n"
"xM v[52:55],a[148:151],v[180:183],v[52:55]\nxW lgkmcnt(6)\nxT a[168:169],v246 offset:2560\nxF v240,v143,v219,v219 op_sel:[1,0,1] op_sel_hi:[1,1,1]\n"
"xM v[24:27],a[152:155],v[176:179],v[24:27]\nxT a[170:171],v246 offset:6656\nxC v191,v239,v240\n"
"xM v[56:59],a[152:155],v[180:183],v[56:59]\nxW lgkmcnt(6)\nxT a[172:173],v246 offset:2568\nxR v[216:219],v245 offset:1808\n"
"xM v[28:31],a[156:159],v[176:179],v[28:31]\nxT a[174:175],v246 offset:6664\nxT a[176:177],v246 offset:3072\n"
"xM v[60:63],a[156:159],v[180:183],v[60:63]\nxW lgkmcnt(8)\nxT a[178:179],v246 offset:7168\nxT a[180:181],v246 offset:3080\n"
"xM v[64:67],a[160:163],v[176:179],v[64:67]\nxT a[182:183],v246 offset:7176\nxT a[184:185],v246 offset:3584\n"
"xM v[96:99],a[160:163],v[180:183],v[96:99]\nxW lgkmcnt(10)\nxT a[186:187],v246 offset:7680\nxT a[188:189],v246 offset:3592\n"
"xM v[68:71],a[164:167],v[176:179],v[68:71]\nxW lgkmcnt(10)\nxT a[190:191],v246 offset:7688\n"
"xM v[100:103],a[164:167],v[180:183],v[100:103]\nxW vmcnt(14)\ns_barrier\n"
"xM v[72:75],a[168:171],v[176:179],v[72:75]\nxA m0,s42,40960\nxF v237,v144,v208,v208 op_sel:[0,0,1] op_sel_hi:[1,1,1]\nxF v238,v144,v209,v209 op_sel:[1,0,1] op_sel_hi:[1,1,1]\nxC v192,v237,v238\nxF v239,v145,v210,v210 op_sel:[0,0,1] op_sel_hi:[1,1,1]\n"
"xF v240,v145,v211,v211 op_sel:[1,0,1] op_sel_hi:[1,1,1]\nxC v193,v239,v240\nxF v237,v148,v208,v208 op_sel:[0,0,1] op_sel_hi:[1,1,1]\n"
"xM v[104:107],a[168:171],v[180:183],v[104:107]\nxA s39,s43,4456448\nxF v238,v148,v209,v209 op_sel:[1,0,1] op_sel_hi:[1,1,1]\nxC v196,v237,v238\nxW lgkmcnt(8)\n"
"xM v[76:79],a[172:175],v[176:179],v[76:79]\nxL v235,s[20:23],s39 offen offset:0 lds\nxM v[108:111],a[172:175],v[180:183],v[108:111]\nxW lgkmcnt(5)\nxT a[128:129],v246 offset:8192\nxF v239,v149,v210,v210 op_sel:[0,0,1] op_sel_hi:[1,1,1]\n"
"xM v[80:83],a[176:179],v[176:179],v[80:83]\nxT a[130:131],v246 offset:12288\nxF v240,v149,v211,v211 op_sel:[1,0,1] op_sel_hi:[1,1,1]\n"
"xM v[112:115],a[176:179],v[180:183],v[112:115]\nxW lgkmcnt(3)\nxT a[132:133],v246 offset:8200\nxC v197,v239,v240\n"
"xM v[84:87],a[180:183],v[176:179],v[84:87]\nxL v236,s[20:23],s39 offen offset:1024 lds\nxR v[208:211],v245 offset:1920\n"
"xM v[116:119],a[180:183],v[180:183],v[116:119]\nxT a[134:135],v246 offset:12296\nxT a[136:137],v246 offset:8704\nxF v237,v146,v216,v216 op_sel:[0,0,1] op_sel_hi:[1,1,1]\n"
"xM v[88:91],a[184:187],v[176:179],v[88:91]\nxT a[138:139],v246 offset:12800\nxF v238,v146,v217,v217 op_sel:[1,0,1] op_sel_hi:[1,1,1]\n"
"xM v[120:123],a[184:187],v[180:183],v[120:123]\nxW lgkmcnt(7)\nxT a[140:141],v246 offset:8712\nxC v194,v237,v238\n"
"xM v[92:95],a[188:191],v[176:179],v[92:95]\nxL v252,s[20:23],s39 offen offset:2048 lds\nxF v239,v147,v218,v218 op_sel:[0,0,1] op_sel_hi:[1,1,1]\nxF v240,v147,v219,v219 op_sel:[1,0,1] op_sel_hi:[1,1,1]\n"
"xM v[124:127],a[188:191],v[180:183],v[124:127]\nxW lgkmcnt(6)\nxT a[142:143],v246 offset:12808\nxT a[144:145],v246 offset:9216\nxC v195,v239,v240\n"
"xM v[0:3],a[128:131],v[184:187],v[0:3]\nxT a[146:147],v246 offset:13312\nxF v237,v150,v216,v216 op_sel:[0,0,1] op_sel_hi:[1,1,1]\n"
"xM v[32:35],a[128:131],v[188:191],v[32:35]\nxW lgkmcnt(6)\nxT a[148:149],v246 offset:9224\nxF v238,v150,v217,v217 op_sel:[1,0,1] op_sel_hi:[1,1,1]\n"
"xM v[4:7],a[132:135],v[184:187],v[4:7]\nxL v247,s[20:23],s39 offen offset:3072 lds\nxC v198,v237,v238\n"
"xM v[36:39],a[132:135],v[188:191],v[36:39]\nxW lgkmcnt(5)\nxT a[150:151],v246 offset:13320\nxT a[152:153],v246 offset:9728\nxF v239,v151,v218,v218 op_sel:[0,0,1] op_sel_hi:[1,1,1]\n"
"xM v[8:11],a[136:139],v[184:187],v[8:11]\nxT a[154:155],v246 offset:13824\nxF v240,v151,v219,v219 op_sel:[1,0,1] op_sel_hi:[1,1,1]\n"
"xM v[40:43],a[136:139],v[188:191],v[40:43]\nxW lgkmcnt(6)\nxT a[156:157],v246 offset:9736\nxC v199,v239,v240\n"
"xM v[12:15],a[140:143],v[184:187],v[12:15]\nxT a[158:159],v246 offset:13832\nxR v[216:219],v245 offset:1936\n"
"xM v[44:47],a[140:143],v[188:191],v[44:47]\nxW lgkmcnt(7)\nxT a[160:161],v246 offset:10240\nxF v237,v152,v208,v208 op_sel:[0,0,1] op_sel_hi:[1,1,1]\n"
"xM v[16:19],a[144:147],v[184:187],v[16:19]\nxT a[162:163],v246 offset:14336\nxF v238,v152,v209,v209 op_sel:[1,0,1] op_sel_hi:[1,1,1]\n"
"xM v[48:51],a[144:147],v[188:191],v[48:51]\nxW lgkmcnt(7)\nxT a[164:165],v246 offset:10248\nxC v200,v237,v238\n"
"xM v[20:23],a[148:151],v[184:187],v[20:23]\nxT a[166:167],v246 offset:14344\nxF v239,v153,v210,v210 op_sel:[0,0,1] op_sel_hi:[1,1,1]\n"
"xM v[52:55],a[148:151],v[188:191],v[52:55]\nxW lgkmcnt(7)\nxT a[168:169],v246 offset:10752\nxF v240,v153,v211,v211 op_sel:[1,0,1] op_sel_hi:[1,1,1]\n"
"xM v[24:27],a[152:155],v[184:187],v[24:27]\nxT a[170:171],v246 offset:14848\nxC v201,v239,v240\n"
"xM v[56:59],a[152:155],v[188:191],v[56:59]\nxW lgkmcnt(7)\nxT a[172:173],v246 offset:10760\nxF v237,v156,v208,v208 op_sel:[0,0,1] op_sel_hi:[1,1,1]\n"
"xM v[28:31],a[156:159],v[184:187],v[28:31]\nxT a[174:175],v246 offset:14856\nxF v238,v156,v209,v209 op_sel:[1,0,1] op_sel_hi:[1,1,1]\n"
"xM v[60:63],a[156:159],v[188:191],v[60:63]\nxW lgkmcnt(6)\nxT a[176:177],v246 offset:11264\nxC v204,v237,v238\n"
"xM v[64:67],a[160:163],v[184:187],v[64:67]\nxT a[178:179],v246 offset:15360\nxF v239,v157,v210,v210 op_sel:[0,0,1] op_sel_hi:[1,1,1]\n"
"xM v[96:99],a[160:163],v[188:191],v[96:99]\nxW lgkmcnt(6)\nxT a[180:181],v246 offset:11272\nxF v240,v157,v211,v211 op_sel:[1,0,1] op_sel_hi:[1,1,1]\n"
"xM v[68:71],a[164:167],v[184:187],v[68:71]\nxW lgkmcnt(5)\nxT a[182:183],v246 offset:15368\nxC v205,v239,v240\n"
"xM v[100:103],a[164:167],v[188:191],v[100:103]\nxW vmcnt(12)\ns_barrier\n"
"xM v[72:75],a[168:171],v[184:187],v[72:75]\nxT a[184:185],v246 offset:11776\nxT a[186:187],v246 offset:15872\nxR v[208:211],v245 offset:2048\nxF v237,v154,v216,v216 op_sel:[0,0,1] op_sel_hi:[1,1,1]\nxF v238,v154,v217,v217 op_sel:[1,0,1] op_sel_hi:[1,1,1]\n"
"xM v[104:107],a[168:171],v[188:191],v[104:107]\nxW lgkmcnt(7)\nxT a[188:189],v246 offset:11784\nxC v202,v237,v238\n"
"xM v[76:79],a[172:175],v[184:187],v[76:79]\nxT a[190:191],v246 offset:15880\nxF v239,v155,v218,v218 op_sel:[0,0,1] op_sel_hi:[1,1,1]\n"
"xM v[108:111],a[172:175],v[188:191],v[108:111]\nxW lgkmcnt(7)\nxT a[128:129],v246 offset:16384\nxF v240,v155,v219,v219 op_sel:[1,0,1] op_sel_hi:[1,1,1]\n"
"xM v[80:83],a[176:179],v[184:187],v[80:83]\nxT a[130:131],v246 offset:20480\nxC v203,v239,v240\n"
"xM v[112:115],a[176:179],v[188:191],v[112:115]\nxW lgkmcnt(7)\nxT a[132:133],v246 offset:16392\nxF v237,v158,v216,v216 op_sel:[0,0,1] op_sel_hi:[1,1,1]\n"
"xM v[84:87],a[180:183],v[184:187],v[84:87]\nxT a[134:135],v246 offset:20488\nxF v238,v158,v217,v217 op_sel:[1,0,1] op_sel_hi:[1,1,1]\n"
"xM v[116:119],a[180:183],v[188:191],v[116:119]\nxW lgkmcnt(7)\nxT a[136:137],v246 offset:16896\nxC v206,v237,v238\n"
"xM v[88:91],a[184:187],v[184:187],v[88:91]\nxT a[138:139],v246 offset:20992\nxF v239,v159,v218,v218 op_sel:[0,0,1] op_sel_hi:[1,1,1]\n"
"xM v[120:123],a[184:187],v[188:191],v[120:123]\nxW lgkmcnt(6)\nxT a[140:141],v246 offset:16904\nxF v240,v159,v219,v219 op_sel:[1,0,1] op_sel_hi:[1,1,1]\n"
"xM v[92:95],a[188:191],v[184:187],v[92:95]\nxT a[142:143],v246 offset:21000\nxC v207,v239,v240\n"
"xM v[124:127],a[188:191],v[188:191],v[124:127]\nxW lgkmcnt(6)\nxT a[144:145],v246 offset:17408\nxR v[216:219],v245 offset:2064\n"
"xM v[0:3],a[128:131],v[192:195],v[0:3]\nxT a[146:147],v246 offset:21504\nxF v237,v160,v208,v208 op_sel:[0,0,1] op_sel_hi:[1,1,1]\n"
"xM v[32:35],a[128:131],v[196:199],v[32:35]\nxW lgkmcnt(7)\nxT a[148:149],v246 offset:17416\nxF v238,v160,v209,v209 op_sel:[1,0,1] op_sel_hi:[1,1,1]\n"
"xM v[4:7],a[132:135],v[192:195],v[4:7]\nxW lgkmcnt(6)\nxT a[150:151],v246 offset:21512\nxC v176,v237,v238\n"
"xM v[36:39],a[132:135],v[196:199],v[36:39]\nxW vmcnt(8)\ns_barrier\n"
"xM v[8:11],a[136:139],v[192:195],v[8:11]\nxT a[152:153],v246 offset:17920\nxT a[154:155],v246 offset:22016\nxF v239,v161,v210,v210 op_sel:[0,0,1] op_sel_hi:[1,1,1]\nxF v240,v161,v211,v211 op_sel:[1,0,1] op_sel_hi:[1,1,1]\n"
"xM v[40:43],a[136:139],v[196:199],v[40:43]\nxW lgkmcnt(7)\nxT a[156:157],v246 offset:17928\nxC v177,v239,v240\n"
"xM v[12:15],a[140:143],v[192:195],v[12:15]\nxT a[158:159],v246 offset:22024\nxF v237,v164,v208,v208 op_sel:[0,0,1] op_sel_hi:[1,1,1]\nxF v238,v164,v209,v209 op_sel:[1,0,1] op_sel_hi:[1,1,1]\n"
"xM v[44:47],a[140:143],v[196:199],v[44:47]\nxW lgkmcnt(6)\nxT a[160:161],v246 offset:24576\nxC v180,v237,v238\n"
"xM v[16:19],a[144:147],v[192:195],v[16:19]\nxT a[162:163],v246 offset:28672\nxF v239,v165,v210,v210 op_sel:[0,0,1] op_sel_hi:[1,1,1]\n"
"xM v[48:51],a[144:147],v[196:199],v[48:51]\nxW lgkmcnt(6)\nxT a[164:165],v246 offset:24584\nxF v240,v165,v211,v211 op_sel:[1,0,1] op_sel_hi:[1,1,1]\n"
"xM v[20:23],a[148:151],v[192:195],v[20:23]\nxT a[166:167],v246 offset:28680\nxC v181,v239,v240\n"
"xM v[52:55],a[148:151],v[196:199],v[52:55]\nxW lgkmcnt(6)\nxT a[168:169],v246 offset:25088\nxR v[208:211],v245 offset:2176\n"
"xM v[24:27],a[152:155],v[192:195],v[24:27]\nxT a[170:171],v246 offset:29184\nxF v237,v162,v216,v216 op_sel:[0,0,1] op_sel_hi:[1,1,1]\n"
"xM v[56:59],a[152:155],v[196:199],v[56:59]\nxW lgkmcnt(7)\nxT a[172:173],v246 offset:25096\nxF v238,v162,v217,v217 op_sel:[1,0,1] op_sel_hi:[1,1,1]\n"
"xM v[28:31],a[156:159],v[192:195],v[28:31]\nxT a[174:175],v246 offset:29192\nxC v178,v237,v238\n"
"xM v[60:63],a[156:159],v[196:199],v[60:63]\nxW lgkmcnt(7)\nxT a[176:177],v246 offset:25600\nxF v239,v163,v218,v218 op_sel:[0,0,1] op_sel_hi:[1,1,1]\n"
"xM v[0:3],a[160:163],v[200:203],v[0:3]\nxT a[178:179],v246 offset:29696\nxF v240,v163,v219,v219 op_sel:[1,0,1] op_sel_hi:[1,1,1]\n"
"xM v[32:35],a[160:163],v[204:207],v[32:35]\nxW lgkmcnt(7)\nxT a[180:181],v246 offset:25608\nxC v179,v239,v240\n"
"xM v[4:7],a[164:167],v[200:203],v[4:7]\nxW lgkmcnt(5)\nxT a[182:183],v246 offset:29704\nxF v237,v166,v216,v216 op_sel:[0,0,1] op_sel_hi:[1,1,1]\n"
"xM v[36:39],a[164:167],v[204:207],v[36:39]\nxW vmcnt(4)\ns_barrier\n"
"xM v[8:11],a[168:171],v[200:203],v[8:11]\nxT a[184:185],v246 offset:26112\nxT a[186:187],v246 offset:30208\nxF v238,v166,v217,v217 op_sel:[1,0,1] op_sel_hi:[1,1,1]\nxC v182,v237,v238\n"
"xM v[40:43],a[168:171],v[204:207],v[40:43]\nxW lgkmcnt(6)\nxT a[188:189],v246 offset:26120\nxF v239,v167,v218,v218 op_sel:[0,0,1] op_sel_hi:[1,1,1]\n"
"xM v[12:15],a[172:175],v[200:203],v[12:15]\nxT a[190:191],v246 offset:30216\nxF v240,v167,v219,v219 op_sel:[1,0,1] op_sel_hi:[1,1,1]\n"
"xM v[44:47],a[172:175],v[204:207],v[44:47]\nxW lgkmcnt(6)\nxT a[128:129],v246 offset:32768\nxC v183,v239,v240\n"
"xM v[16:19],a[176:179],v[200:203],v[16:19]\nxT a[130:131],v246 offset:36864\nxR v[216:219],v245 offset:2192\n"
"xM v[48:51],a[176:179],v[204:207],v[48:51]\nxW lgkmcnt(7)\nxT a[132:133],v246 offset:32776\nxF v237,v168,v208,v208 op_sel:[0,0,1] op_sel_hi:[1,1,1]\n"
"xM v[20:23],a[180:183],v[200:203],v[20:23]\nxT a[134:135],v246 offset:36872\nxF v238,v168,v209,v209 op_sel:[1,0,1] op_sel_hi:[1,1,1]\nxC v184,v237,v238\n"
"xM v[52:55],a[180:183],v[204:207],v[52:55]\nxW lgkmcnt(7)\nxT a[136:137],v246 offset:33280\nxF v239,v169,v210,v210 op_sel:[0,0,1] op_sel_hi:[1,1,1]\n"
"xM v[24:27],a[184:187],v[200:203],v[24:27]\nxT a[138:139],v246 offset:37376\nxF v240,v169,v211,v211 op_sel:[1,0,1] op_sel_hi:[1,1,1]\n"
"xM v[56:59],a[184:187],v[204:207],v[56:59]\nxW lgkmcnt(7)\nxT a[140:141],v246 offset:33288\nxC v185,v239,v240\n"
"xM v[28:31],a[188:191],v[200:203],v[28:31]\nxT a[142:143],v246 offset:37384\nxF v237,v172,v208,v208 op_sel:[0,0,1] op_sel_hi:[1,1,1]\n"
"xM v[60:63],a[188:191],v[204:207],v[60:63]\nxW lgkmcnt(7)\nxT a[144:145],v246 offset:33792\nxF v238,v172,v209,v209 op_sel:[1,0,1] op_sel_hi:[1,1,1]\n"
"xM v[0:3],a[128:131],v[176:179],v[0:3]\nxT a[146:147],v246 offset:37888\nxC v188,v237,v238\n"
"xM v[32:35],a[128:131],v[180:183],v[32:35]\nxW lgkmcnt(6)\nxT a[148:149],v246 offset:33800\nxF v239,v173,v210,v210 op_sel:[0,0,1] op_sel_hi:[1,1,1]\n"
"xM v[4:7],a[132:135],v[176:179],v[4:7]\nxW lgkmcnt(5)\nxT a[150:151],v246 offset:37896\nxF v240,v173,v211,v211 op_sel:[1,0,1] op_sel_hi:[1,1,1]\n"
"xM v[36:39],a[132:135],v[180:183],v[36:39]\nxW vmcnt(0)\ns_barrier\n"
"xM v[8:11],a[136:139],v[176:179],v[8:11]\nxT a[152:153],v246 offset:34304\nxT a[154:155],v246 offset:38400\nxC v189,v239,v240\nxF v237,v170,v216,v216 op_sel:[0,0,1] op_sel_hi:[1,1,1]\n"
"xM v[40:43],a[136:139],v[180:183],v[40:43]\nxW lgkmcnt(6)\nxT a[156:157],v246 offset:34312\nxF v238,v170,v217,v217 op_sel:[1,0,1] op_sel_hi:[1,1,1]\n"
"xM v[12:15],a[140:143],v[176:179],v[12:15]\nxT a[158:159],v246 offset:38408\nxC v186,v237,v238\n"
"xM v[44:47],a[140:143],v[180:183],v[44:47]\nxW lgkmcnt(6)\nxT a[160:161],v246 offset:40960\nxF v239,v171,v218,v218 op_sel:[0,0,1] op_sel_hi:[1,1,1]\n"
"xM v[16:19],a[144:147],v[176:179],v[16:19]\nxT a[162:163],v246 offset:45056\nxF v240,v171,v219,v219 op_sel:[1,0,1] op_sel_hi:[1,1,1]\n"
"xM v[48:51],a[144:147],v[180:183],v[48:51]\nxW lgkmcnt(6)\nxT a[164:165],v246 offset:40968\nxC v187,v239,v240\n"
"xM v[20:23],a[148:151],v[176:179],v[20:23]\nxT a[166:167],v246 offset:45064\nxF v237,v174,v216,v216 op_sel:[0,0,1] op_sel_hi:[1,1,1]\n"
"xM v[52:55],a[148:151],v[180:183],v[52:55]\nxW lgkmcnt(6)\nxT a[168:169],v246 offset:41472\nxF v238,v174,v217,v217 op_sel:[1,0,1] op_sel_hi:[1,1,1]\n"
"xM v[24:27],a[152:155],v[176:179],v[24:27]\nxT a[170:171],v246 offset:45568\nxC v190,v237,v238\n"
"xM v[56:59],a[152:155],v[180:183],v[56:59]\nxW lgkmcnt(6)\nxT a[172:173],v246 offset:41480\nxF v239,v175,v218,v218 op_sel:[0,0,1] op_sel_hi:[1,1,1]\n"
"xM v[28:31],a[156:159],v[176:179],v[28:31]\nxT a[174:175],v246 offset:45576\nxF v240,v175,v219,v219 op_sel:[1,0,1] op_sel_hi:[1,1,1]\nxC v191,v239,v240\n"
"xM v[60:63],a[156:159],v[180:183],v[60:63]\nxW lgkmcnt(6)\nxT a[176:177],v246 offset:41984\nxR a[64:67],v224 offset:0\n"
"xM v[0:3],a[160:163],v[184:187],v[0:3]\nxT a[178:179],v246 offset:46080\nxR a[68:71],v225 offset:0\n"
"xM v[32:35],a[160:163],v[188:191],v[32:35]\nxW lgkmcnt(8)\nxT a[180:181],v246 offset:41992\nxR a[72:75],v226 offset:0\n"
"xM v[4:7],a[164:167],v[184:187],v[4:7]\nxT a[182:183],v246 offset:46088\nxR a[76:79],v227 offset:0\n"
"xM v[36:39],a[164:167],v[188:191],v[36:39]\nxW lgkmcnt(10)\nxT a[184:185],v246 offset:42496\nxR a[80:83],v224 offset:4096\n"
"xM v[8:11],a[168:171],v[184:187],v[8:11]\nxT a[186:187],v246 offset:46592\nxR a[84:87],v225 offset:4096\n"
"xM v[40:43],a[168:171],v[188:191],v[40:43]\nxW lgkmcnt(12)\nxT a[188:189],v246 offset:42504\nxR a[88:91],v226 offset:4096\n"
"xM v[12:15],a[172:175],v[184:187],v[12:15]\nxT a[190:191],v246 offset:46600\nxR a[92:95],v227 offset:4096\n"
"xM v[44:47],a[172:175],v[188:191],v[44:47]\nxW lgkmcnt(13)\nxT a[128:129],v246 offset:18432\nxR a[96:99],v224 offset:8192\n"
"xM v[16:19],a[176:179],v[184:187],v[16:19]\nxT a[130:131],v246 offset:22528\nxR a[100:103],v225 offset:8192\n"
"xM v[48:51],a[176:179],v[188:191],v[48:51]\nxW lgkmcnt(13)\nxT a[132:133],v246 offset:18440\nxR a[104:107],v226 offset:8192\n"
"xM v[20:23],a[180:183],v[184:187],v[20:23]\nxT a[134:135],v246 offset:22536\nxR a[108:111],v227 offset:8192\n"
"xM v[52:55],a[180:183],v[188:191],v[52:55]\nxW lgkmcnt(13)\nxT a[136:137],v246 offset:18944\nxR a[112:115],v224 offset:12288\n"
"xM v[24:27],a[184:187],v[184:187],v[24:27]\nxT a[138:139],v246 offset:23040\nxR a[116:119],v225 offset:12288\n"
"xM v[56:59],a[184:187],v[188:191],v[56:59]\nxW lgkmcnt(13)\nxT a[140:141],v246 offset:18952\nxR a[120:123],v226 offset:12288\n"
"xM v[28:31],a[188:191],v[184:187],v[28:31]\nxT a[142:143],v246 offset:23048\nxR a[124:127],v227 offset:12288\n"
"xM v[60:63],a[188:191],v[188:191],v[60:63]\nxW lgkmcnt(13)\nxT a[144:145],v246 offset:19456\nxT a[146:147],v246 offset:23552\n"
"xM v[64:67],a[128:131],v[192:195],v[64:67]\nxT a[148:149],v246 offset:19464\nxT a[150:151],v246 offset:23560\n"
"xM v[96:99],a[128:131],v[196:199],v[96:99]\nxW lgkmcnt(13)\nxT a[152:153],v246 offset:19968\nxT a[154:155],v246 offset:24064\n"
"xM v[68:71],a[132:135],v[192:195],v[68:71]\nxT a[156:157],v246 offset:19976\nxT a[158:159],v246 offset:24072\n"
"xM v[100:103],a[132:135],v[196:199],v[100:103]\nxW lgkmcnt(13)\nxT a[160:161],v246 offset:26624\n"
"xM v[72:75],a[136:139],v[192:195],v[72:75]\nxC v0,v0,v1\nxC v2,v2,v3\n"
"xM v[104:107],a[136:139],v[196:199],v[104:107]\nxW lgkmcnt(10)\nxC v4,v4,v5\nxC v6,v6,v7\n"
"xM v[76:79],a[140:143],v[192:195],v[76:79]\nxP a0,v0\nxP a1,v2\n"
"xM v[108:111],a[140:143],v[196:199],v[108:111]\nxW lgkmcnt(7)\nxP a2,v4\nxP a3,v6\n"
"xM v[80:83],a[144:147],v[192:195],v[80:83]\nxT a[162:163],v246 offset:30720\nxC v8,v8,v9\n"
"xM v[112:115],a[144:147],v[196:199],v[112:115]\nxW lgkmcnt(4)\nxT a[164:165],v246 offset:26632\nxC v10,v10,v11\n"
"xM v[84:87],a[148:151],v[192:195],v[84:87]\nxT a[166:167],v246 offset:30728\nxC v12,v12,v13\n"
"xM v[116:119],a[148:151],v[196:199],v[116:119]\nxT a[168:169],v246 offset:27136\nxC v14,v14,v15\n"
"xM v[88:91],a[152:155],v[192:195],v[88:91]\nxT a[170:171],v246 offset:31232\nxP a4,v8\n"
"xM v[120:123],a[152:155],v[196:199],v[120:123]\nxW lgkmcnt(5)\nxT a[172:173],v246 offset:27144\nxP a5,v10\n"
"xM v[92:95],a[156:159],v[192:195],v[92:95]\nxT a[174:175],v246 offset:31240\nxP a6,v12\n"
"xM v[124:127],a[156:159],v[196:199],v[124:127]\nxW lgkmcnt(6)\nxT a[176:177],v246 offset:27648\nxP a7,v14\n"
"xM v[64:67],a[160:163],v[200:203],v[64:67]\nxT a[178:179],v246 offset:31744\nxC v16,v16,v17\n"
"xM v[96:99],a[160:163],v[204:207],v[96:99]\nxW lgkmcnt(6)\nxT a[180:181],v246 offset:27656\nxC v18,v18,v19\n"
"xM v[68:71],a[164:167],v[200:203],v[68:71]\nxT a[182:183],v246 offset:31752\nxC v20,v20,v21\n"
"xM v[100:103],a[164:167],v[204:207],v[100:103]\nxW lgkmcnt(6)\nxT a[184:185],v246 offset:28160\nxC v22,v22,v23\n"
"xM v[72:75],a[168:171],v[200:203],v[72:75]\nxT a[186:187],v246 offset:32256\nxP a8,v16\n"
"xM v[104:107],a[168:171],v[204:207],v[104:107]\nxW lgkmcnt(6)\nxT a[188:189],v246 offset:28168\nxP a9,v18\n"
"xM v[76:79],a[172:175],v[200:203],v[76:79]\nxT a[190:191],v246 offset:32264\nxP a10,v20\n"
"xM v[108:111],a[172:175],v[204:207],v[108:111]\nxW lgkmcnt(6)\nxT a[128:129],v246 offset:34816\nxP a11,v22\n"
"xM v[80:83],a[176:179],v[200:203],v[80:83]\nxT a[130:131],v246 offset:38912\nxC v24,v24,v25\n"
"xM v[112:115],a[176:179],v[204:207],v[112:115]\nxW lgkmcnt(6)\nxT a[132:133],v246 offset:34824\nxC v26,v26,v27\n"
"xM v[84:87],a[180:183],v[200:203],v[84:87]\nTCH0_0_%=:\ns_cbranch_execz TDONE0_%=\nxT a[134:135],v246 offset:38920\nxC v28,v28,v29\n"
"xM v[116:119],a[180:183],v[204:207],v[116:119]\nxW lgkmcnt(6)\nxT a[136:137],v246 offset:35328\nxC v30,v30,v31\n"
"xM v[88:91],a[184:187],v[200:203],v[88:91]\nxT a[138:139],v246 offset:39424\nxP a12,v24\n"
"xM v[120:123],a[184:187],v[204:207],v[120:123]\nxW lgkmcnt(6)\nxT a[140:141],v246 offset:35336\nxP a13,v26\n"
"xM v[92:95],a[188:191],v[200:203],v[92:95]\nxT a[142:143],v246 offset:39432\nxP a14,v28\n"
"xM v[124:127],a[188:191],v[204:207],v[124:127]\nxW lgkmcnt(6)\nxT a[144:145],v246 offset:35840\nxP a15,v30\n"
"xM v[64:67],a[128:131],v[176:179],v[64:67]\nxT a[146:147],v246 offset:39936\nxC v32,v32,v33\n"
"xM v[96:99],a[128:131],v[180:183],v[96:99]\nxW lgkmcnt(6)\nxT a[148:149],v246 offset:35848\nxC v34,v34,v35\n"
"xM v[68:71],a[132:135],v[176:179],v[68:71]\nxT a[150:151],v246 offset:39944\nxC v36,v36,v37\n"
"xM v[100:103],a[132:135],v[180:183],v[100:103]\nxW lgkmcnt(6)\nxT a[152:153],v246 offset:36352\nxC v38,v38,v39\n"
"xM v[72:75],a[136:139],v[176:179],v[72:75]\nxT a[154:155],v246 offset:40448\nxP a16,v32\n"
"xM v[104:107],a[136:139],v[180:183],v[104:107]\nxW lgkmcnt(6)\nxT a[156:157],v246 offset:36360\nxP a17,v34\n"
"xM v[76:79],a[140:143],v[176:179],v[76:79]\nxT a[158:159],v246 offset:40456\nxP a18,v36\n"
"xM v[108:111],a[140:143],v[180:183],v[108:111]\nxW lgkmcnt(6)\nxT a[160:161],v246 offset:43008\nxP a19,v38\n"
"xM v[80:83],a[144:147],v[176:179],v[80:83]\nxT a[162:163],v246 offset:47104\nxC v40,v40,v41\n"
"xM v[112:115],a[144:147],v[180:183],v[112:115]\nxW lgkmcnt(6)\nxT a[164:165],v246 offset:43016\nxC v42,v42,v43\n"
"xM v[84:87],a[148:151],v[176:179],v[84:87]\nxT a[166:167],v246 offset:47112\nxC v44,v44,v45\n"
"xM v[116:119],a[148:151],v[180:183],v[116:119]\nxW lgkmcnt(6)\nxT a[168:169],v246 offset:43520\nxC v46,v46,v47\n"
"xM v[88:91],a[152:155],v[176:179],v[88:91]\nxT a[170:171],v246 offset:47616\nxP a20,v40\n"
"xM v[120:123],a[152:155],v[180:183],v[120:123]\nxW lgkmcnt(6)\nxT a[172:173],v246 offset:43528\nxP a21,v42\n"
"xM v[92:95],a[156:159],v[176:179],v[92:95]\nxT a[174:175],v246 offset:47624\nxP a22,v44\n"
"xM v[124:127],a[156:159],v[180:183],v[124:127]\nxW lgkmcnt(6)\nxT a[176:177],v246 offset:44032\nxP a23,v46\n"
"xM v[64:67],a[160:163],v[184:187],v[64:67]\nxT a[178:179],v246 offset:48128\nxC v48,v48,v49\n"
"xM v[96:99],a[160:163],v[188:191],v[96:99]\nxW lgkmcnt(6)\nxT a[180:181],v246 offset:44040\nxC v50,v50,v51\n"
"xM v[68:71],a[164:167],v[184:187],v[68:71]\nxT a[182:183],v246 offset:48136\nxC v52,v52,v53\n"
"xM v[100:103],a[164:167],v[188:191],v[100:103]\nxW lgkmcnt(6)\nxT a[184:185],v246 offset:44544\nxC v54,v54,v55\n"
"xM v[72:75],a[168:171],v[184:187],v[72:75]\nxT a[186:187],v246 offset:48640\nxP a24,v48\n"
"xM v[104:107],a[168:171],v[188:191],v[104:107]\nxW lgkmcnt(6)\nxT a[188:189],v246 offset:44552\nxP a25,v50\n"
"xM v[76:79],a[172:175],v[184:187],v[76:79]\nxT a[190:191],v246 offset:48648\nxP a26,v52\n"
"xM v[108:111],a[172:175],v[188:191],v[108:111]\nxW lgkmcnt(6)\nxP a27,v54\nxC v56,v56,v57\n"
"xM v[80:83],a[176:179],v[184:187],v[80:83]\nxC v58,v58,v59\nxC v60,v60,v61\n"
"xM v[112:115],a[176:179],v[188:191],v[112:115]\nxW lgkmcnt(4)\nxC v62,v62,v63\nxP a28,v56\n"
"xM v[84:87],a[180:183],v[184:187],v[84:87]\nxP a29,v58\nxP a30,v60\n"
"xM v[116:119],a[180:183],v[188:191],v[116:119]\nxW lgkmcnt(2)\nxP a31,v62\n"
"xM v[88:91],a[184:187],v[184:187],v[88:91]\nxM v[120:123],a[184:187],v[188:191],v[120:123]\nxW lgkmcnt(0)\n"
"xM v[92:95],a[188:191],v[184:187],v[92:95]\nxM v[124:127],a[188:191],v[188:191],v[124:127]\ns_cmp_lg_u32 s61, 0\ns_cbranch_scc1 FINX_%=\nv_accvgpr_read_b32 v237, a204\nv_accvgpr_read_b32 v238, a205\n"
"v_accvgpr_read_b32 v239, a206\nv_accvgpr_read_b32 v240, a207\ns_sub_u32 s37, %[bid], 198\ns_mov_b32 s50, 1.0\ns_mov_b32 s51, 0\ns_cmp_ge_u32 s37, 27\n"
"s_cselect_b32 s50, 0x3fb8aa3b, s50\ns_cselect_b32 s51, 0x0, s51\ns_cmp_ge_u32 s37, 36\ns_cselect_b32 s50, 0x3fb8aa3b, s50\ns_cselect_b32 s51, 0x3fb8aa3b, s51\ns_cmp_ge_u32 s37, 45\n"
"s_cselect_b32 s50, 0x3ef5fdf0, s50\ns_cselect_b32 s51, 0x0, s51\nv_add_f32 v237, v237, v238\nv_add_f32 v239, v239, v240\nv_add_f32 v237, v237, v239\nxC v237,0,v237\n"
"v_mov_b32 v238, s51\nv_fmac_f32 v238, s50, v237\nv_lshlrev_b32 v239, 2, v255\ns_lshl_b32 s37, s37, 8\nxA s37,s37,1048576\nbuffer_store_dword v238, v239, s[96:99], s37 offen sc1\n"
"FINX_%=:\ns_mov_b64 exec, 0\ns_cmp_eq_u32 %[wid], 0\ns_cbranch_scc1 TCH2_0_%=\ns_cmp_eq_u32 %[wid], 1\ns_cbranch_scc1 TCH2_1_%=\n"
"s_cmp_eq_u32 %[wid], 2\ns_cbranch_scc1 TCH2_2_%=\ns_cmp_eq_u32 %[wid], 3\ns_cbranch_scc1 TCH2_3_%=\nTDONE2_%=:\ns_mov_b64 exec, -1\n"
"s_nop 1\nxM v[128:131],a[64:67],a[0:3],0\nxM v[128:131],a[68:71],a[4:7],v[128:131]\n"
"xM v[128:131],a[72:75],a[8:11],v[128:131]\nxW vmcnt(52) lgkmcnt(0)\ns_barrier\n"
"xM v[128:131],a[76:79],a[12:15],v[128:131]\nxC v64,v64,v65\nxC v66,v66,v67\n"
"xM v[144:147],a[64:67],a[16:19],0\nxA m0,s36,65536\nxC v68,v68,v69\nxC v70,v70,v71\n"
"xM v[144:147],a[68:71],a[20:23],v[144:147]\nxL v234,s[16:19],s33 offen offset:0 lds\nxM v[144:147],a[72:75],a[24:27],v[144:147]\nxP a32,v64\nxP a33,v66\n"
"xM v[144:147],a[76:79],a[28:31],v[144:147]\nxP a34,v68\nxP a35,v70\n"
"xM v[132:135],a[80:83],a[0:3],0\nxC v72,v72,v73\nxC v74,v74,v75\n"
"xM v[132:135],a[84:87],a[4:7],v[132:135]\nxL v234,s[16:19],s33 offen offset:2048 lds\nxM v[132:135],a[88:91],a[8:11],v[132:135]\nxA s33,s33,16384\nxC v76,v76,v77\nxC v78,v78,v79\n"
"xM v[132:135],a[92:95],a[12:15],v[132:135]\nxP a36,v72\nxP a37,v74\n"
"xM v[148:151],a[80:83],a[16:19],0\nxA m0,s36,66560\nxP a38,v76\nxP a39,v78\n"
"xM v[148:151],a[84:87],a[20:23],v[148:151]\nxL v234,s[16:19],s34 offen offset:0 lds\nxM v[148:151],a[88:91],a[24:27],v[148:151]\nxC v80,v80,v81\nxC v82,v82,v83\n"
"xM v[148:151],a[92:95],a[28:31],v[148:151]\nxC v84,v84,v85\nxC v86,v86,v87\n"
"xM v[136:139],a[96:99],a[0:3],0\nxP a40,v80\nxP a41,v82\n"
"xM v[136:139],a[100:103],a[4:7],v[136:139]\nxL v234,s[16:19],s34 offen offset:2048 lds\nxM v[136:139],a[104:107],a[8:11],v[136:139]\nxA s34,s34,16384\nxP a42,v84\nxP a43,v86\n"
"xM v[136:139],a[108:111],a[12:15],v[136:139]\nxT a[128:129],v228 offset:0\nxC v88,v88,v89\n"
"xM v[152:155],a[96:99],a[16:19],0\nxA m0,s36,49152\nxT a[130:131],v228 offset:4096\nxC v90,v90,v91\n"
"xM v[152:155],a[100:103],a[20:23],v[152:155]\nxL v230,s[12:15],s32 offen offset:0 lds\nxM v[152:155],a[104:107],a[24:27],v[152:155]\nxT a[160:161],v228 offset:8192\nxC v92,v92,v93\n"
"xM v[152:155],a[108:111],a[28:31],v[152:155]\nxT a[162:163],v228 offset:12288\nxC v94,v94,v95\n"
"xM v[140:143],a[112:115],a[0:3],0\nxT a[132:133],v229 offset:0\nxP a44,v88\n"
"xM v[140:143],a[116:119],a[4:7],v[140:143]\nxL v231,s[12:15],s32 offen offset:1024 lds\nxP a45,v90\nxP a46,v92\nxP a47,v94\nxC v96,v96,v97\n"
"xM v[140:143],a[120:123],a[8:11],v[140:143]\nxT a[134:135],v229 offset:4096\nxC v98,v98,v99\nxC v100,v100,v101\nxC v102,v102,v103\nxP a48,v96\n"
"xP a49,v98\nxP a50,v100\nxP a51,v102\nxC v104,v104,v105\n"
"xM v[140:143],a[124:127],a[12:15],v[140:143]\nxT a[164:165],v229 offset:8192\nxC v106,v106,v107\nxC v108,v108,v109\nxC v110,v110,v111\nxP a52,v104\n"
"xP a53,v106\nxP a54,v108\nxP a55,v110\n"
"xM v[156:159],a[112:115],a[16:19],0\nxT a[166:167],v229 offset:12288\nxC v112,v112,v113\nxC v114,v114,v115\nxC v116,v116,v117\nxC v118,v118,v119\n"
"xP a56,v112\nxP a57,v114\nxP a58,v116\nxP a59,v118\n"
"xM v[156:159],a[116:119],a[20:23],v[156:159]\nxL v232,s[12:15],s32 offen offset:2048 lds\nxC v120,v120,v121\nxC v122,v122,v123\nxC v124,v124,v125\nxC v126,v126,v127\n"
"xP a60,v120\nxP a61,v122\nxP a62,v124\nxP a63,v126\n"
"xM v[156:159],a[120:123],a[24:27],v[156:159]\nxT a[136:137],v228 offset:512\nv_mov_b64 v[64:65], 0\n"
"xM v[156:159],a[124:127],a[28:31],v[156:159]\nxT a[138:139],v228 offset:4608\nv_mov_b64 v[66:67], 0\n"
"xM v[160:163],a[64:67],a[32:35],0\nxT a[168:169],v228 offset:8704\nv_mov_b64 v[68:69], 0\n"
"xM v[160:163],a[68:71],a[36:39],v[160:163]\nxL v233,s[12:15],s32 offen offset:3072 lds\nxM v[160:163],a[72:75],a[40:43],v[160:163]\nxA s32,s32,16384\nxT a[170:171],v228 offset:12800\nv_mov_b64 v[70:71], 0\n"
"xM v[160:163],a[76:79],a[44:47],v[160:163]\nxT a[140:141],v229 offset:512\nv_mov_b64 v[72:73], 0\n"
"xM v[176:179],a[64:67],a[48:51],0\nxA m0,s36,81920\nxT a[142:143],v229 offset:4608\nv_mov_b64 v[74:75], 0\n"
"xM v[176:179],a[68:71],a[52:55],v[176:179]\nxL v234,s[16:19],s33 offen offset:0 lds\nxM v[176:179],a[72:75],a[56:59],v[176:179]\nxT a[172:173],v229 offset:8704\nv_mov_b64 v[76:77], 0\n"
"xM v[176:179],a[76:79],a[60:63],v[176:179]\nxT a[174:175],v229 offset:12800\nv_mov_b64 v[78:79], 0\nv_mov_b64 v[80:81], 0\ns_cmp_lg_u32 s61, 0\ns_cbranch_scc1 FINY_%=\n"
"xW vmcnt(9)\nv_mov_b32 v242, s68\nv_mov_b32 v243, s69\nv_lshlrev_b32 v241, 11, v255\ns_lshl_b32 s37, %[bid], 3\nxA s37,s37,1212416\n"
"s_mov_b64 exec, 0xffff\nbuffer_store_dwordx2 v[242:243], v241, s[96:99], s37 offen sc1\ns_mov_b64 exec, -1\nFINY_%=:\n"
"xM v[164:167],a[80:83],a[32:35],0\nxT a[144:145],v228 offset:1024\nv_mov_b64 v[82:83], 0\nv_mov_b64 v[84:85], 0\nv_mov_b64 v[86:87], 0\nv_mov_b64 v[88:89], 0\n"
"v_mov_b64 v[90:91], 0\nv_mov_b64 v[92:93], 0\nv_mov_b64 v[94:95], 0\n"
"xM v[164:167],a[84:87],a[36:39],v[164:167]\nxL v234,s[16:19],s33 offen offset:2048 lds\nv_mov_b64 v[96:97], 0\nv_mov_b64 v[98:99], 0\nv_mov_b64 v[100:101], 0\nv_mov_b64 v[102:103], 0\n"
"v_mov_b64 v[104:105], 0\nv_mov_b64 v[106:107], 0\nv_mov_b64 v[108:109], 0\nv_mov_b64 v[110:111], 0\n"
"xM v[164:167],a[88:91],a[40:43],v[164:167]\nxA s33,s33,16384\nxT a[146:147],v228 offset:5120\nxX v237,v128,v129,v130\nxX v237,v131,v132,v237\nxX v237,v133,v134,v237\n"
"xX v237,v135,v136,v237\nxX v237,v137,v138,v237\nxX v237,v139,v140,v237\nxX v237,v141,v142,v237\n"
"xM v[164:167],a[92:95],a[44:47],v[164:167]\nxT a[176:177],v228 offset:9216\nv_max_f32 v237, v143, v237\nv_mov_b32 v238, v237\ns_nop 1\nv_permlane32_swap_b32 v237, v238\n"
"v_max_f32 v237, v237, v238\nv_mov_b32 v238, v237\ns_nop 1\n"
"xM v[180:183],a[80:83],a[48:51],0\nxA m0,s36,82944\nxT a[178:179],v228 offset:13312\nv_permlane16_swap_b32 v237, v238\nv_max_f32 v237, v237, v238\nv_add_f32 v237, s41, v237\n"
"v_sub_f32 v128, v128, v237\nv_sub_f32 v129, v129, v237\nv_sub_f32 v130, v130, v237\n"
"xM v[180:183],a[84:87],a[52:55],v[180:183]\nxL v234,s[16:19],s34 offen offset:0 lds\nv_sub_f32 v131, v131, v237\nv_sub_f32 v132, v132, v237\nv_sub_f32 v133, v133, v237\nv_sub_f32 v134, v134, v237\n"
"v_sub_f32 v135, v135, v237\nv_sub_f32 v136, v136, v237\nv_sub_f32 v137, v137, v237\nv_sub_f32 v138, v138, v237\n"
"xM v[180:183],a[88:91],a[56:59],v[180:183]\nxT a[148:149],v229 offset:1024\nv_sub_f32 v139, v139, v237\nv_sub_f32 v140, v140, v237\nv_sub_f32 v141, v141, v237\nv_sub_f32 v142, v142, v237\n"
"v_sub_f32 v143, v143, v237\nv_sub_f32 v192, 0, v237\nv_sub_f32 v193, 0, v237\n"
"xM v[180:183],a[92:95],a[60:63],v[180:183]\nxT a[150:151],v229 offset:5120\nv_sub_f32 v194, 0, v237\nv_sub_f32 v195, 0, v237\nxX v237,v144,v145,v146\nxX v237,v147,v148,v237\n"
"xX v237,v149,v150,v237\nxX v237,v151,v152,v237\nxX v237,v153,v154,v237\nxX v237,v155,v156,v237\n"
"xM v[168:171],a[96:99],a[32:35],0\nxT a[180:181],v229 offset:9216\nxX v237,v157,v158,v237\nv_max_f32 v237, v159, v237\nv_mov_b32 v238, v237\ns_nop 1\n"
"v_permlane32_swap_b32 v237, v238\nv_max_f32 v237, v237, v238\nxM v[168:171],a[100:103],a[36:39],v[168:171]\nxL v234,s[16:19],s34 offen offset:2048 lds\nv_mov_b32 v238, v237\ns_nop 1\n"
"v_permlane16_swap_b32 v237, v238\nv_max_f32 v237, v237, v238\nv_add_f32 v237, s41, v237\nv_sub_f32 v144, v144, v237\nv_sub_f32 v145, v145, v237\n"
"xM v[168:171],a[104:107],a[40:43],v[168:171]\nxA s34,s34,16384\nxT a[182:183],v229 offset:13312\nv_sub_f32 v146, v146, v237\nv_sub_f32 v147, v147, v237\nv_sub_f32 v148, v148, v237\n"
"v_sub_f32 v149, v149, v237\nv_sub_f32 v150, v150, v237\nv_sub_f32 v151, v151, v237\nv_sub_f32 v152, v152, v237\n"
"xM v[168:171],a[108:111],a[44:47],v[168:171]\nxT a[152:153],v228 offset:1536\nv_sub_f32 v153, v153, v237\nv_sub_f32 v154, v154, v237\nv_sub_f32 v155, v155, v237\nv_sub_f32 v156, v156, v237\n"
"v_sub_f32 v157, v157, v237\nv_sub_f32 v158, v158, v237\nv_sub_f32 v159, v159, v237\nv_sub_f32 v196, 0, v237\n"
"xM v[184:187],a[96:99],a[48:51],0\nxA m0,s36,0\nxT a[154:155],v228 offset:5632\nv_sub_f32 v197, 0, v237\nv_sub_f32 v198, 0, v237\nv_sub_f32 v199, 0, v237\n"
"xE v128,v128\nxE v129,v129\nxM v[184:187],a[100:103],a[52:55],v[184:187]\nxL v230,s[12:15],s32 offen offset:0 lds\nxE v130,v130\nxC v128,v128,v129\n"
"xE v131,v131\nxE v132,v132\nxC v129,v130,v131\n"
"xM v[184:187],a[104:107],a[56:59],v[184:187]\nxT a[184:185],v228 offset:9728\nxE v133,v133\nxE v134,v134\nxC v130,v132,v133\nxE v135,v135\n"
"xM v[184:187],a[108:111],a[60:63],v[184:187]\nxT a[186:187],v228 offset:13824\nxE v144,v144\nxC v131,v134,v135\nxE v145,v145\nxE v146,v146\n"
"xC v144,v144,v145\nxM v[172:175],a[112:115],a[32:35],0\nxT a[156:157],v229 offset:1536\nxE v147,v147\nxE v148,v148\nxC v145,v146,v147\n"
"xE v149,v149\nxM v[172:175],a[116:119],a[36:39],v[172:175]\nxL v231,s[12:15],s32 offen offset:1024 lds\nxE v150,v150\nxC v146,v148,v149\nxE v151,v151\n"
"xE v136,v136\nxC v147,v150,v151\nxM v[172:175],a[120:123],a[40:43],v[172:175]\nxT a[158:159],v229 offset:5632\nxE v137,v137\nxE v138,v138\n"
"xC v136,v136,v137\nxE v139,v139\nxM v[172:175],a[124:127],a[44:47],v[172:175]\nxT a[188:189],v229 offset:9728\nxE v140,v140\nxC v137,v138,v139\n"
"xE v141,v141\nxE v142,v142\nxC v138,v140,v141\n"
"xM v[188:191],a[112:115],a[48:51],0\nxT a[190:191],v229 offset:13824\nxE v143,v143\nxE v152,v152\nxC v139,v142,v143\nxE v153,v153\n"
"xM v[188:191],a[116:119],a[52:55],v[188:191]\nxL v232,s[12:15],s32 offen offset:2048 lds\nxE v154,v154\nxC v152,v152,v153\nxE v155,v155\nxE v156,v156\n"
"xC v153,v154,v155\nxM v[188:191],a[120:123],a[56:59],v[188:191]\nxR a[64:67],v224 offset:16384\nxE v157,v157\nxE v158,v158\nxC v154,v156,v157\n"
"xE v159,v159\ns_nop 0\nxC v155,v158,v159\n"
"xM v[188:191],a[124:127],a[60:63],v[188:191]\nxW lgkmcnt(5)\nxR a[68:71],v225 offset:16384\n"
"xM v[0:3],a[128:131],v[128:131],0\nxR a[72:75],v226 offset:16384\nxM v[0:3],a[160:163],v[136:139],v[0:3]\nxL v233,s[12:15],s32 offen offset:3072 lds\n"
"xM v[32:35],a[128:131],v[144:147],0\nxA s32,s32,16384\nxR a[76:79],v227 offset:16384\n"
"xM v[32:35],a[160:163],v[152:155],v[32:35]\nxR a[80:83],v224 offset:20480\nxX v237,v160,v161,v162\n"
"xM v[4:7],a[132:135],v[128:131],0\nxR a[84:87],v225 offset:20480\nxX v237,v163,v164,v237\n"
"xM v[4:7],a[164:167],v[136:139],v[4:7]\nxR a[88:91],v226 offset:20480\nv_accvgpr_read_b32 v244, a218\n"
"xM v[36:39],a[132:135],v[144:147],0\nxR a[92:95],v227 offset:20480\nxX v237,v165,v166,v237\n"
"xM v[36:39],a[164:167],v[152:155],v[36:39]\nbuffer_load_dwordx2 a[208:209], v244, s[28:31], 0 offen offset:0\nxM v[8:11],a[136:139],v[128:131],0\nxR a[96:99],v224 offset:24576\nxX v237,v167,v168,v237\n"
"xM v[8:11],a[168:171],v[136:139],v[8:11]\nxX v237,v169,v170,v237\nxX v237,v171,v172,v237\n"
"xM v[40:43],a[136:139],v[144:147],0\nxX v237,v173,v174,v237\nv_max_f32 v237, v175, v237\n"
"xM v[40:43],a[168:171],v[152:155],v[40:43]\nbuffer_load_dwordx2 a[210:211], v244, s[28:31], 0 offen offset:256\nxM v[12:15],a[140:143],v[128:131],0\nv_mov_b32 v238, v237\ns_nop 1\n"
"xM v[12:15],a[172:175],v[136:139],v[12:15]\nv_permlane32_swap_b32 v237, v238\nxM v[44:47],a[140:143],v[144:147],0\nv_max_f32 v237, v237, v238\nv_mov_b32 v238, v237\n"
"xM v[44:47],a[172:175],v[152:155],v[44:47]\nbuffer_load_dwordx2 a[212:213], v244, s[28:31], 0 offen offset:512\nxM v[16:19],a[144:147],v[128:131],0\ns_nop 1\nv_permlane16_swap_b32 v237, v238\n"
"xM v[16:19],a[176:179],v[136:139],v[16:19]\nv_max_f32 v237, v237, v238\nv_add_f32 v237, s41, v237\nv_sub_f32 v160, v160, v237\nv_sub_f32 v161, v161, v237\nv_sub_f32 v162, v162, v237\n"
"v_sub_f32 v163, v163, v237\nv_sub_f32 v164, v164, v237\nxM v[48:51],a[144:147],v[144:147],0\nv_sub_f32 v165, v165, v237\nv_sub_f32 v166, v166, v237\nv_sub_f32 v167, v167, v237\n"
"v_sub_f32 v168, v168, v237\nv_sub_f32 v169, v169, v237\nv_sub_f32 v170, v170, v237\nv_sub_f32 v171, v171, v237\n"
"xM v[48:51],a[176:179],v[152:155],v[48:51]\nbuffer_load_dwordx2 a[214:215], v244, s[28:31], 0 offen offset:768\nv_sub_f32 v172, v172, v237\nv_sub_f32 v173, v173, v237\nv_sub_f32 v174, v174, v237\nv_sub_f32 v175, v175, v237\n"
"v_sub_f32 v200, 0, v237\nv_sub_f32 v201, 0, v237\nv_sub_f32 v202, 0, v237\nv_sub_f32 v203, 0, v237\n"
"xM v[20:23],a[148:151],v[128:131],0\nxX v237,v176,v177,v178\nxX v237,v179,v180,v237\nxX v237,v181,v182,v237\nxX v237,v183,v184,v237\nxX v237,v185,v186,v237\n"
"xX v237,v187,v188,v237\nxX v237,v189,v190,v237\nxM v[20:23],a[180:183],v[136:139],v[20:23]\nv_max_f32 v237, v191, v237\nv_mov_b32 v238, v237\ns_nop 1\n"
"v_permlane32_swap_b32 v237, v238\nv_max_f32 v237, v237, v238\nv_mov_b32 v238, v237\ns_nop 1\n"
"xM v[52:55],a[148:151],v[144:147],0\nv_permlane16_swap_b32 v237, v238\nv_max_f32 v237, v237, v238\nv_add_f32 v237, s41, v237\nv_sub_f32 v176, v176, v237\nv_sub_f32 v177, v177, v237\n"
"v_sub_f32 v178, v178, v237\nxM v[52:55],a[180:183],v[152:155],v[52:55]\nbuffer_load_dwordx2 a[216:217], v244, s[28:31], 0 offen offset:1024\nv_sub_f32 v179, v179, v237\nv_sub_f32 v180, v180, v237\nv_sub_f32 v181, v181, v237\n"
"v_sub_f32 v182, v182, v237\nv_sub_f32 v183, v183, v237\nv_sub_f32 v184, v184, v237\nv_sub_f32 v185, v185, v237\nv_sub_f32 v186, v186, v237\n"
"xM v[24:27],a[152:155],v[128:131],0\nv_sub_f32 v187, v187, v237\nv_sub_f32 v188, v188, v237\nv_sub_f32 v189, v189, v237\nv_sub_f32 v190, v190, v237\nv_sub_f32 v191, v191, v237\n"
"v_sub_f32 v204, 0, v237\nv_sub_f32 v205, 0, v237\nxM v[24:27],a[184:187],v[136:139],v[24:27]\nTC0_1_1_%=:\ns_cbranch_execz TDONE0_%=\nv_sub_f32 v206, 0, v237\n"
"v_sub_f32 v207, 0, v237\nxE v160,v160\nxE v161,v161\nxE v162,v162\n"
"xM v[56:59],a[152:155],v[144:147],0\nxC v160,v160,v161\nxE v163,v163\nxE v164,v164\nxC v161,v162,v163\nxE v165,v165\n"
"xM v[56:59],a[184:187],v[152:155],v[56:59]\nxW lgkmcnt(0)\nxE v166,v166\nxC v162,v164,v165\nxE v167,v167\nxE v176,v176\n"
"xM v[28:31],a[156:159],v[128:131],0\nxC v163,v166,v167\nxE v177,v177\nxE v178,v178\nxC v176,v176,v177\nxE v179,v179\n"
"xM v[28:31],a[188:191],v[136:139],v[28:31]\nxE v180,v180\nxC v177,v178,v179\nxE v181,v181\nxE v182,v182\n"
"xM v[60:63],a[156:159],v[144:147],0\nxC v178,v180,v181\nxE v183,v183\nxE v168,v168\nxC v179,v182,v183\nxE v169,v169\n"
"xM v[60:63],a[188:191],v[152:155],v[60:63]\nxE v170,v170\nxC v168,v168,v169\nxE v171,v171\nxE v172,v172\n"
"xM v[208:211],a[192:195],v[128:131],0\nxC v169,v170,v171\nxE v173,v173\nxE v174,v174\nxC v170,v172,v173\nxE v175,v175\n"
"xM v[208:211],a[192:195],v[136:139],v[208:211]\nxE v184,v184\nxC v171,v174,v175\nxE v185,v185\nxE v186,v186\n"
"xM v[212:215],a[192:195],v[144:147],0\nxC v184,v184,v185\nxE v187,v187\nv_mov_b64 v[112:113], 0\nv_mov_b64 v[114:115], 0\nv_mov_b64 v[116:117], 0\n"
"v_mov_b64 v[118:119], 0\nxM v[212:215],a[192:195],v[152:155],v[212:215]\nv_mov_b64 v[120:121], 0\nv_mov_b64 v[122:123], 0\nv_mov_b64 v[124:125], 0\nv_mov_b64 v[126:127], 0\n"
"v_mov_b64 v[216:217], 0\nv_mov_b64 v[218:219], 0\nv_mov_b64 v[220:221], 0\nv_mov_b64 v[222:223], 0\ns_lshr_b32 s39, s71, 1\ns_mul_i32 s39, s39, 589824\n"
"xA s24,s80,s39\ns_addc_u32 s25, s81, 0\ns_and_b32 s25, s25, 0xffff\ns_mov_b32 s26, 590720\ns_mov_b32 s27, 0x20000\ns_sub_u32 s24, s24, 896\n"
"s_subb_u32 s25, s25, 0\ns_lshl_b32 s44, %[wid], 12\ns_and_b32 s39, s71, 1\ns_mul_i32 s42, s39, 294912\ns_mul_i32 s39, %[wid], 18432\nxA s42,s42,s39\n"
"xW lgkmcnt(0)\ns_mov_b32 s43, 0x07060302\ns_mov_b32 s49, 0\ns_mov_b32 s60, 0x4d800000\ns_mov_b32 s35, 10\n.p2align 6\n"
"LOOP_%=:\nxM v[128:131],a[64:67],a[0:3],v[192:195]\nxR a[100:103],v225 offset:24576\nxC v185,v186,v187\n"
"xM v[128:131],a[68:71],a[4:7],v[128:131]\nxW lgkmcnt(3)\nxE v188,v188\n"
"xM v[128:131],a[72:75],a[8:11],v[128:131]\nxR a[104:107],v226 offset:24576\nxM v[128:131],a[76:79],a[12:15],v[128:131]\nxE v189,v189\n"
"xM v[144:147],a[64:67],a[16:19],v[196:199]\nxR a[108:111],v227 offset:24576\nxC v186,v188,v189\n"
"xM v[144:147],a[68:71],a[20:23],v[144:147]\nxE v190,v190\nxM v[144:147],a[72:75],a[24:27],v[144:147]\nxR a[112:115],v224 offset:28672\n"
"xM v[144:147],a[76:79],a[28:31],v[144:147]\nxE v191,v191\nxM v[132:135],a[80:83],a[0:3],v[192:195]\nxR a[116:119],v225 offset:28672\nxC v187,v190,v191\n"
"xM v[132:135],a[84:87],a[4:7],v[132:135]\nxM v[132:135],a[88:91],a[8:11],v[132:135]\nxW lgkmcnt(3)\nxR a[120:123],v226 offset:28672\n"
"xM v[132:135],a[92:95],a[12:15],v[132:135]\nxM v[148:151],a[80:83],a[16:19],v[196:199]\nxM v[148:151],a[84:87],a[20:23],v[148:151]\nxE v128,v128\n"
"xM v[148:151],a[88:91],a[24:27],v[148:151]\nxR a[124:127],v227 offset:28672\nxM v[148:151],a[92:95],a[28:31],v[148:151]\nxE v129,v129\n"
"xM v[136:139],a[96:99],a[0:3],v[192:195]\nxC v128,v128,v129\nxM v[136:139],a[100:103],a[4:7],v[136:139]\nxE v130,v130\n"
"xM v[136:139],a[104:107],a[8:11],v[136:139]\nxW lgkmcnt(1)\nxM v[136:139],a[108:111],a[12:15],v[136:139]\nxE v131,v131\n"
"xM v[152:155],a[96:99],a[16:19],v[196:199]\nxC v129,v130,v131\nxM v[152:155],a[100:103],a[20:23],v[152:155]\nxE v132,v132\n"
"xM v[152:155],a[104:107],a[24:27],v[152:155]\nxM v[152:155],a[108:111],a[28:31],v[152:155]\nxE v133,v133\n"
"xM v[140:143],a[112:115],a[0:3],v[192:195]\nxC v130,v132,v133\nxM v[140:143],a[116:119],a[4:7],v[140:143]\nxE v134,v134\n"
"xM v[140:143],a[120:123],a[8:11],v[140:143]\nxW lgkmcnt(0)\nxM v[140:143],a[124:127],a[12:15],v[140:143]\nxE v135,v135\n"
"xM v[156:159],a[112:115],a[16:19],v[196:199]\nxC v131,v134,v135\nxM v[156:159],a[116:119],a[20:23],v[156:159]\nxE v144,v144\n"
"xM v[156:159],a[120:123],a[24:27],v[156:159]\nxM v[156:159],a[124:127],a[28:31],v[156:159]\nxE v145,v145\n"
"xM v[64:67],a[128:131],v[160:163],v[64:67]\nxW vmcnt(9)\ns_barrier\n"
"xM v[64:67],a[160:163],v[168:171],v[64:67]\nxE v146,v146\nxM v[96:99],a[128:131],v[176:179],v[96:99]\nxC v144,v144,v145\n"
"xM v[96:99],a[160:163],v[184:187],v[96:99]\nxE v147,v147\nxM v[68:71],a[132:135],v[160:163],v[68:71]\nxT a[128:129],v228 offset:32768\nxC v145,v146,v147\n"
"xM v[68:71],a[164:167],v[168:171],v[68:71]\nxA m0,s36,32768\nxE v148,v148\n"
"xM v[100:103],a[132:135],v[176:179],v[100:103]\nxL v234,s[16:19],s33 offen offset:0 lds\nxM v[100:103],a[164:167],v[184:187],v[100:103]\nxE v149,v149\n"
"xM v[72:75],a[136:139],v[160:163],v[72:75]\ns_min_u32 s37, s49, 7\nxT a[130:131],v228 offset:36864\nxC v146,v148,v149\n"
"xM v[72:75],a[168:171],v[168:171],v[72:75]\nxE v150,v150\nxM v[104:107],a[136:139],v[176:179],v[104:107]\ns_lshr_b32 s38, s37, 1\nxT a[160:161],v228 offset:40960\n"
"xM v[104:107],a[168:171],v[184:187],v[104:107]\nxE v151,v151\nxM v[76:79],a[140:143],v[160:163],v[76:79]\ns_mul_i32 s39, s38, 12288\nxT a[162:163],v228 offset:45056\nxC v147,v150,v151\n"
"xM v[76:79],a[172:175],v[168:171],v[76:79]\nxE v136,v136\nxM v[108:111],a[140:143],v[176:179],v[108:111]\ns_lshl_b32 s50, s37, 11\nxT a[132:133],v229 offset:32768\n"
"xM v[108:111],a[172:175],v[184:187],v[108:111]\nxE v137,v137\nxM v[80:83],a[144:147],v[160:163],v[80:83]\nxA s39,s39,s50\nxT a[134:135],v229 offset:36864\nxC v136,v136,v137\n"
"xM v[80:83],a[176:179],v[168:171],v[80:83]\nxE v138,v138\nxM v[112:115],a[144:147],v[176:179],v[112:115]\nxA s51,s39,s44\nxT a[164:165],v229 offset:40960\n"
"xM v[112:115],a[176:179],v[184:187],v[112:115]\nxE v139,v139\nxM v[84:87],a[148:151],v[160:163],v[84:87]\ns_mul_i32 s38, s38, 55296\nxT a[166:167],v229 offset:45056\nxC v137,v138,v139\n"
"xM v[84:87],a[180:183],v[168:171],v[84:87]\nxE v140,v140\nxM v[116:119],a[148:151],v[176:179],v[116:119]\ns_mul_i32 s37, s37, 9216\n"
"xM v[116:119],a[180:183],v[184:187],v[116:119]\nxE v141,v141\nxM v[88:91],a[152:155],v[160:163],v[88:91]\nxA s37,s37,s38\nxT a[136:137],v228 offset:33280\nxC v138,v140,v141\n"
"xM v[88:91],a[184:187],v[168:171],v[88:91]\nxE v142,v142\nxM v[120:123],a[152:155],v[176:179],v[120:123]\nxA s37,s37,s42\nxL v234,s[16:19],s33 offen offset:2048 lds\n"
"xM v[120:123],a[184:187],v[184:187],v[120:123]\nxA s33,s33,16384\nxE v143,v143\n"
"xM v[92:95],a[156:159],v[160:163],v[92:95]\nxA s37,s37,1152\nxT a[138:139],v228 offset:37376\nxC v139,v142,v143\n"
"xM v[92:95],a[188:191],v[168:171],v[92:95]\nxE v152,v152\nxM v[124:127],a[156:159],v[176:179],v[124:127]\nxT a[168:169],v228 offset:41472\n"
"xM v[124:127],a[188:191],v[184:187],v[124:127]\nxE v153,v153\nxM v[216:219],a[192:195],v[160:163],v[216:219]\nxT a[170:171],v228 offset:45568\nxC v152,v152,v153\n"
"xM v[216:219],a[192:195],v[168:171],v[216:219]\nxA m0,s36,33792\nxE v154,v154\n"
"xM v[220:223],a[192:195],v[176:179],v[220:223]\nxL v234,s[16:19],s34 offen offset:0 lds\nxM v[220:223],a[192:195],v[184:187],v[220:223]\nxE v155,v155\n"
"xM v[160:163],a[64:67],a[32:35],v[200:203]\nxT a[140:141],v229 offset:33280\nxC v153,v154,v155\n"
"xM v[160:163],a[68:71],a[36:39],v[160:163]\nxE v156,v156\nxM v[160:163],a[72:75],a[40:43],v[160:163]\nxT a[142:143],v229 offset:37376\n"
"xM v[160:163],a[76:79],a[44:47],v[160:163]\nxE v157,v157\nxM v[176:179],a[64:67],a[48:51],v[204:207]\nxT a[172:173],v229 offset:41472\nxC v154,v156,v157\n"
"xM v[176:179],a[68:71],a[52:55],v[176:179]\nxE v158,v158\nxM v[176:179],a[72:75],a[56:59],v[176:179]\nxT a[174:175],v229 offset:45568\n"
"xM v[176:179],a[76:79],a[60:63],v[176:179]\nxE v159,v159\nxM v[164:167],a[80:83],a[32:35],v[200:203]\nxT a[144:145],v228 offset:33792\nxC v155,v158,v159\n"
"xM v[164:167],a[84:87],a[36:39],v[164:167]\nxL v234,s[16:19],s34 offen offset:2048 lds\nxM v[164:167],a[88:91],a[40:43],v[164:167]\nxA s34,s34,16384\nxT a[146:147],v228 offset:37888\n"
"xM v[164:167],a[92:95],a[44:47],v[164:167]\nxT a[176:177],v228 offset:41984\nxM v[180:183],a[80:83],a[48:51],v[204:207]\nxT a[178:179],v228 offset:46080\n"
"xM v[180:183],a[84:87],a[52:55],v[180:183]\nxE v160,v160\nxM v[180:183],a[88:91],a[56:59],v[180:183]\nxT a[148:149],v229 offset:33792\n"
"xM v[180:183],a[92:95],a[60:63],v[180:183]\nxA m0,s51,98304\nxE v161,v161\n"
"xM v[168:171],a[96:99],a[32:35],v[200:203]\nxL v248,s[24:27],s37 offen lds\nxM v[168:171],a[100:103],a[36:39],v[168:171]\nxE v162,v162\n"
"xM v[168:171],a[104:107],a[40:43],v[168:171]\nxT a[150:151],v229 offset:37888\nxC v160,v160,v161\n"
"xM v[168:171],a[108:111],a[44:47],v[168:171]\nxE v163,v163\nxM v[184:187],a[96:99],a[48:51],v[204:207]\nxT a[180:181],v229 offset:41984\nxC v161,v162,v163\n"
"xM v[184:187],a[100:103],a[52:55],v[184:187]\nxE v164,v164\nxM v[184:187],a[104:107],a[56:59],v[184:187]\nxT a[182:183],v229 offset:46080\n"
"xM v[184:187],a[108:111],a[60:63],v[184:187]\nxE v165,v165\nxM v[172:175],a[112:115],a[32:35],v[200:203]\nxT a[152:153],v228 offset:34304\nxC v162,v164,v165\n"
"xM v[172:175],a[116:119],a[36:39],v[172:175]\nxA m0,s36,16384\nxE v166,v166\n"
"xM v[172:175],a[120:123],a[40:43],v[172:175]\nxL v230,s[12:15],s32 offen offset:0 lds\nxM v[172:175],a[124:127],a[44:47],v[172:175]\nxE v167,v167\n"
"xM v[188:191],a[112:115],a[48:51],v[204:207]\nxT a[154:155],v228 offset:38400\nxC v163,v166,v167\n"
"xM v[188:191],a[116:119],a[52:55],v[188:191]\nxE v176,v176\nxM v[188:191],a[120:123],a[56:59],v[188:191]\nxT a[184:185],v228 offset:42496\n"
"xM v[188:191],a[124:127],a[60:63],v[188:191]\nxW lgkmcnt(3)\nxE v177,v177\n"
"xM v[0:3],a[128:131],v[128:131],v[0:3]\nxT a[186:187],v228 offset:46592\nxC v176,v176,v177\n"
"xM v[0:3],a[160:163],v[136:139],v[0:3]\nxE v178,v178\nxM v[32:35],a[128:131],v[144:147],v[32:35]\nxT a[156:157],v229 offset:34304\n"
"xM v[32:35],a[160:163],v[152:155],v[32:35]\nxE v179,v179\nxM v[4:7],a[132:135],v[128:131],v[4:7]\nxT a[158:159],v229 offset:38400\nxC v177,v178,v179\n"
"xM v[4:7],a[164:167],v[136:139],v[4:7]\nxE v180,v180\nxM v[36:39],a[132:135],v[144:147],v[36:39]\nxL v231,s[12:15],s32 offen offset:1024 lds\n"
"xM v[36:39],a[164:167],v[152:155],v[36:39]\nxE v181,v181\nxM v[8:11],a[136:139],v[128:131],v[8:11]\nxT a[188:189],v229 offset:42496\nxC v178,v180,v181\n"
"xM v[8:11],a[168:171],v[136:139],v[8:11]\nxE v182,v182\nxM v[40:43],a[136:139],v[144:147],v[40:43]\nxT a[190:191],v229 offset:46592\n"
"xM v[40:43],a[168:171],v[152:155],v[40:43]\nxE v183,v183\nxM v[12:15],a[140:143],v[128:131],v[12:15]\nxR a[64:67],v224 offset:49152\nxC v179,v182,v183\n"
"xM v[12:15],a[172:175],v[136:139],v[12:15]\nxE v168,v168\nxM v[44:47],a[140:143],v[144:147],v[44:47]\nxR a[68:71],v225 offset:49152\n"
"xM v[44:47],a[172:175],v[152:155],v[44:47]\nxE v169,v169\nxM v[16:19],a[144:147],v[128:131],v[16:19]\nxR a[72:75],v226 offset:49152\nxC v168,v168,v169\n"
"xM v[16:19],a[176:179],v[136:139],v[16:19]\nxE v170,v170\nxM v[48:51],a[144:147],v[144:147],v[48:51]\nxL v232,s[12:15],s32 offen offset:2048 lds\n"
"xM v[48:51],a[176:179],v[152:155],v[48:51]\nxE v171,v171\nxM v[20:23],a[148:151],v[128:131],v[20:23]\nxR a[76:79],v227 offset:49152\nxC v169,v170,v171\n"
"xM v[20:23],a[180:183],v[136:139],v[20:23]\nxE v172,v172\nxM v[52:55],a[148:151],v[144:147],v[52:55]\n"
"xM v[52:55],a[180:183],v[152:155],v[52:55]\nxW lgkmcnt(2)\nxE v173,v173\n"
"xM v[24:27],a[152:155],v[128:131],v[24:27]\nxR a[80:83],v224 offset:53248\nxC v170,v172,v173\n"
"xM v[24:27],a[184:187],v[136:139],v[24:27]\nxE v174,v174\nxM v[56:59],a[152:155],v[144:147],v[56:59]\nxR a[84:87],v225 offset:53248\n"
"xM v[56:59],a[184:187],v[152:155],v[56:59]\nxE v175,v175\nxM v[28:31],a[156:159],v[128:131],v[28:31]\nxR a[88:91],v226 offset:53248\nxC v171,v174,v175\n"
"xM v[28:31],a[188:191],v[136:139],v[28:31]\nxE v184,v184\nxM v[60:63],a[156:159],v[144:147],v[60:63]\nxL v233,s[12:15],s32 offen offset:3072 lds\n"
"xM v[60:63],a[188:191],v[152:155],v[60:63]\nxA s32,s32,16384\nxE v185,v185\n"
"xM v[208:211],a[192:195],v[128:131],v[208:211]\nxR a[92:95],v227 offset:53248\nxC v184,v184,v185\n"
"xM v[208:211],a[192:195],v[136:139],v[208:211]\nxE v186,v186\nxM v[212:215],a[192:195],v[144:147],v[212:215]\nxR a[96:99],v224 offset:57344\n"
"xM v[212:215],a[192:195],v[152:155],v[212:215]\nxE v187,v187\nxM v[128:131],a[64:67],a[0:3],v[192:195]\nxR a[100:103],v225 offset:57344\nxC v185,v186,v187\n"
"xM v[128:131],a[68:71],a[4:7],v[128:131]\nxW lgkmcnt(3)\nxE v188,v188\n"
"xM v[128:131],a[72:75],a[8:11],v[128:131]\nxR a[104:107],v226 offset:57344\nxM v[128:131],a[76:79],a[12:15],v[128:131]\nxE v189,v189\n"
"xM v[144:147],a[64:67],a[16:19],v[196:199]\nxR a[108:111],v227 offset:57344\nxC v186,v188,v189\n"
"xM v[144:147],a[68:71],a[20:23],v[144:147]\nxE v190,v190\nxM v[144:147],a[72:75],a[24:27],v[144:147]\nxR a[112:115],v224 offset:61440\n"
"xM v[144:147],a[76:79],a[28:31],v[144:147]\nxE v191,v191\nxM v[132:135],a[80:83],a[0:3],v[192:195]\nxR a[116:119],v225 offset:61440\nxC v187,v190,v191\n"
"xM v[132:135],a[84:87],a[4:7],v[132:135]\nxM v[132:135],a[88:91],a[8:11],v[132:135]\nxW lgkmcnt(3)\nxR a[120:123],v226 offset:61440\n"
"xM v[132:135],a[92:95],a[12:15],v[132:135]\nxM v[148:151],a[80:83],a[16:19],v[196:199]\nxM v[148:151],a[84:87],a[20:23],v[148:151]\nxE v128,v128\n"
"xM v[148:151],a[88:91],a[24:27],v[148:151]\nxR a[124:127],v227 offset:61440\nxM v[148:151],a[92:95],a[28:31],v[148:151]\nxE v129,v129\n"
"xM v[136:139],a[96:99],a[0:3],v[192:195]\nxC v128,v128,v129\nxM v[136:139],a[100:103],a[4:7],v[136:139]\nxE v130,v130\n"
"xM v[136:139],a[104:107],a[8:11],v[136:139]\nxW lgkmcnt(1)\nxM v[136:139],a[108:111],a[12:15],v[136:139]\nxE v131,v131\n"
"xM v[152:155],a[96:99],a[16:19],v[196:199]\nxC v129,v130,v131\nxM v[152:155],a[100:103],a[20:23],v[152:155]\nxE v132,v132\n"
"xM v[152:155],a[104:107],a[24:27],v[152:155]\nxM v[152:155],a[108:111],a[28:31],v[152:155]\nxE v133,v133\n"
"xM v[140:143],a[112:115],a[0:3],v[192:195]\nxC v130,v132,v133\nxM v[140:143],a[116:119],a[4:7],v[140:143]\nxE v134,v134\n"
"xM v[140:143],a[120:123],a[8:11],v[140:143]\nxW lgkmcnt(0)\nxM v[140:143],a[124:127],a[12:15],v[140:143]\nxE v135,v135\n"
"xM v[156:159],a[112:115],a[16:19],v[196:199]\nxC v131,v134,v135\nxM v[156:159],a[116:119],a[20:23],v[156:159]\nxE v144,v144\n"
"xM v[156:159],a[120:123],a[24:27],v[156:159]\nxM v[156:159],a[124:127],a[28:31],v[156:159]\nxE v145,v145\n"
"xM v[64:67],a[128:131],v[160:163],v[64:67]\nxW vmcnt(9)\ns_barrier\n"
"xM v[64:67],a[160:163],v[168:171],v[64:67]\nxE v146,v146\nxM v[96:99],a[128:131],v[176:179],v[96:99]\nxC v144,v144,v145\n"
"xM v[96:99],a[160:163],v[184:187],v[96:99]\nxE v147,v147\nxM v[68:71],a[132:135],v[160:163],v[68:71]\nxT a[128:129],v228 offset:49152\nxC v145,v146,v147\n"
"xM v[68:71],a[164:167],v[168:171],v[68:71]\nxA m0,s36,65536\nxE v148,v148\n"
"xM v[100:103],a[132:135],v[176:179],v[100:103]\nxL v234,s[16:19],s33 offen offset:0 lds\nxM v[100:103],a[164:167],v[184:187],v[100:103]\nxE v149,v149\n"
"xM v[72:75],a[136:139],v[160:163],v[72:75]\ns_min_u32 s37, s49, 7\nxT a[130:131],v228 offset:53248\nxC v146,v148,v149\n"
"xM v[72:75],a[168:171],v[168:171],v[72:75]\nxE v150,v150\nxM v[104:107],a[136:139],v[176:179],v[104:107]\ns_lshr_b32 s38, s37, 1\nxT a[160:161],v228 offset:57344\n"
"xM v[104:107],a[168:171],v[184:187],v[104:107]\nxE v151,v151\nxM v[76:79],a[140:143],v[160:163],v[76:79]\ns_mul_i32 s39, s38, 12288\nxT a[162:163],v228 offset:61440\nxC v147,v150,v151\n"
"xM v[76:79],a[172:175],v[168:171],v[76:79]\nxE v136,v136\nxM v[108:111],a[140:143],v[176:179],v[108:111]\ns_lshl_b32 s50, s37, 11\nxT a[132:133],v229 offset:49152\n"
"xM v[108:111],a[172:175],v[184:187],v[108:111]\nxE v137,v137\nxM v[80:83],a[144:147],v[160:163],v[80:83]\nxA s39,s39,s50\nxT a[134:135],v229 offset:53248\nxC v136,v136,v137\n"
"xM v[80:83],a[176:179],v[168:171],v[80:83]\nxE v138,v138\nxM v[112:115],a[144:147],v[176:179],v[112:115]\nxA s51,s39,s44\nxT a[164:165],v229 offset:57344\n"
"xM v[112:115],a[176:179],v[184:187],v[112:115]\nxE v139,v139\nxM v[84:87],a[148:151],v[160:163],v[84:87]\ns_mul_i32 s38, s38, 55296\nxT a[166:167],v229 offset:61440\nxC v137,v138,v139\n"
"xM v[84:87],a[180:183],v[168:171],v[84:87]\nxE v140,v140\nxM v[116:119],a[148:151],v[176:179],v[116:119]\ns_mul_i32 s37, s37, 9216\n"
"xM v[116:119],a[180:183],v[184:187],v[116:119]\nxE v141,v141\nxM v[88:91],a[152:155],v[160:163],v[88:91]\nxA s37,s37,s38\nxT a[136:137],v228 offset:49664\nxC v138,v140,v141\n"
"xM v[88:91],a[184:187],v[168:171],v[88:91]\nxE v142,v142\nxM v[120:123],a[152:155],v[176:179],v[120:123]\nxA s37,s37,s42\nxL v234,s[16:19],s33 offen offset:2048 lds\n"
"xM v[120:123],a[184:187],v[184:187],v[120:123]\nxA s33,s33,16384\nxE v143,v143\n"
"xM v[92:95],a[156:159],v[160:163],v[92:95]\nxA s37,s37,1280\nxT a[138:139],v228 offset:53760\nxC v139,v142,v143\n"
"xM v[92:95],a[188:191],v[168:171],v[92:95]\nxE v152,v152\nxM v[124:127],a[156:159],v[176:179],v[124:127]\nxA s49,s49,1\nxT a[168:169],v228 offset:57856\n"
"xM v[124:127],a[188:191],v[184:187],v[124:127]\nxE v153,v153\nxM v[216:219],a[192:195],v[160:163],v[216:219]\nxT a[170:171],v228 offset:61952\nxC v152,v152,v153\n"
"xM v[216:219],a[192:195],v[168:171],v[216:219]\nxA m0,s36,66560\nxE v154,v154\n"
"xM v[220:223],a[192:195],v[176:179],v[220:223]\nxL v234,s[16:19],s34 offen offset:0 lds\nxM v[220:223],a[192:195],v[184:187],v[220:223]\nxE v155,v155\n"
"xM v[160:163],a[64:67],a[32:35],v[200:203]\nxT a[140:141],v229 offset:49664\nxC v153,v154,v155\n"
"xM v[160:163],a[68:71],a[36:39],v[160:163]\nxE v156,v156\nxM v[160:163],a[72:75],a[40:43],v[160:163]\nxT a[142:143],v229 offset:53760\n"
"xM v[160:163],a[76:79],a[44:47],v[160:163]\nxE v157,v157\nxM v[176:179],a[64:67],a[48:51],v[204:207]\nxT a[172:173],v229 offset:57856\nxC v154,v156,v157\n"
"xM v[176:179],a[68:71],a[52:55],v[176:179]\nxE v158,v158\nxM v[176:179],a[72:75],a[56:59],v[176:179]\nxT a[174:175],v229 offset:61952\n"
"xM v[176:179],a[76:79],a[60:63],v[176:179]\nxE v159,v159\nxM v[164:167],a[80:83],a[32:35],v[200:203]\nxT a[144:145],v228 offset:50176\nxC v155,v158,v159\n"
"xM v[164:167],a[84:87],a[36:39],v[164:167]\nxL v234,s[16:19],s34 offen offset:2048 lds\nxM v[164:167],a[88:91],a[40:43],v[164:167]\nxA s34,s34,16384\nxT a[146:147],v228 offset:54272\n"
"xM v[164:167],a[92:95],a[44:47],v[164:167]\nxT a[176:177],v228 offset:58368\nxM v[180:183],a[80:83],a[48:51],v[204:207]\nxT a[178:179],v228 offset:62464\n"
"xM v[180:183],a[84:87],a[52:55],v[180:183]\nxE v160,v160\nxM v[180:183],a[88:91],a[56:59],v[180:183]\nxT a[148:149],v229 offset:50176\n"
"xM v[180:183],a[92:95],a[60:63],v[180:183]\nxA m0,s51,99328\nxE v161,v161\n"
"xM v[168:171],a[96:99],a[32:35],v[200:203]\nxL v248,s[24:27],s37 offen lds\nxM v[168:171],a[100:103],a[36:39],v[168:171]\nTC0_2_1_%=:\ns_cbranch_execz TDONE0_%=\nxE v162,v162\n"
"xM v[168:171],a[104:107],a[40:43],v[168:171]\nxT a[150:151],v229 offset:54272\nxC v160,v160,v161\n"
"xM v[168:171],a[108:111],a[44:47],v[168:171]\nxE v163,v163\nxM v[184:187],a[96:99],a[48:51],v[204:207]\nxT a[180:181],v229 offset:58368\nxC v161,v162,v163\n"
"xM v[184:187],a[100:103],a[52:55],v[184:187]\nxE v164,v164\nxM v[184:187],a[104:107],a[56:59],v[184:187]\nxT a[182:183],v229 offset:62464\n"
"xM v[184:187],a[108:111],a[60:63],v[184:187]\nxE v165,v165\nxM v[172:175],a[112:115],a[32:35],v[200:203]\nxT a[152:153],v228 offset:50688\nxC v162,v164,v165\n"
"xM v[172:175],a[116:119],a[36:39],v[172:175]\nxA m0,s36,49152\nxE v166,v166\n"
"xM v[172:175],a[120:123],a[40:43],v[172:175]\nxL v230,s[12:15],s32 offen offset:0 lds\nxM v[172:175],a[124:127],a[44:47],v[172:175]\nxE v167,v167\n"
"xM v[188:191],a[112:115],a[48:51],v[204:207]\nxT a[154:155],v228 offset:54784\nxC v163,v166,v167\n"
"xM v[188:191],a[116:119],a[52:55],v[188:191]\nxE v176,v176\nxM v[188:191],a[120:123],a[56:59],v[188:191]\nxT a[184:185],v228 offset:58880\n"
"xM v[188:191],a[124:127],a[60:63],v[188:191]\nxW lgkmcnt(3)\nxE v177,v177\n"
"xM v[0:3],a[128:131],v[128:131],v[0:3]\nxT a[186:187],v228 offset:62976\nxC v176,v176,v177\n"
"xM v[0:3],a[160:163],v[136:139],v[0:3]\nxE v178,v178\nxM v[32:35],a[128:131],v[144:147],v[32:35]\nxT a[156:157],v229 offset:50688\n"
"xM v[32:35],a[160:163],v[152:155],v[32:35]\nxE v179,v179\nxM v[4:7],a[132:135],v[128:131],v[4:7]\nxT a[158:159],v229 offset:54784\nxC v177,v178,v179\n"
"xM v[4:7],a[164:167],v[136:139],v[4:7]\nxE v180,v180\nxM v[36:39],a[132:135],v[144:147],v[36:39]\nxL v231,s[12:15],s32 offen offset:1024 lds\n"
"xM v[36:39],a[164:167],v[152:155],v[36:39]\nxE v181,v181\nxM v[8:11],a[136:139],v[128:131],v[8:11]\nxT a[188:189],v229 offset:58880\nxC v178,v180,v181\n"
"xM v[8:11],a[168:171],v[136:139],v[8:11]\nxE v182,v182\nxM v[40:43],a[136:139],v[144:147],v[40:43]\nxT a[190:191],v229 offset:62976\n"
"xM v[40:43],a[168:171],v[152:155],v[40:43]\nxE v183,v183\nxM v[12:15],a[140:143],v[128:131],v[12:15]\nxR a[64:67],v224 offset:0\nxC v179,v182,v183\n"
"xM v[12:15],a[172:175],v[136:139],v[12:15]\nxE v168,v168\nxM v[44:47],a[140:143],v[144:147],v[44:47]\nxR a[68:71],v225 offset:0\n"
"xM v[44:47],a[172:175],v[152:155],v[44:47]\nxE v169,v169\nxM v[16:19],a[144:147],v[128:131],v[16:19]\nxR a[72:75],v226 offset:0\nxC v168,v168,v169\n"
"xM v[16:19],a[176:179],v[136:139],v[16:19]\nxE v170,v170\nxM v[48:51],a[144:147],v[144:147],v[48:51]\nxL v232,s[12:15],s32 offen offset:2048 lds\n"
"xM v[48:51],a[176:179],v[152:155],v[48:51]\nxE v171,v171\nxM v[20:23],a[148:151],v[128:131],v[20:23]\nxR a[76:79],v227 offset:0\nxC v169,v170,v171\n"
"xM v[20:23],a[180:183],v[136:139],v[20:23]\nxE v172,v172\nxM v[52:55],a[148:151],v[144:147],v[52:55]\n"
"xM v[52:55],a[180:183],v[152:155],v[52:55]\nxW lgkmcnt(2)\nxE v173,v173\n"
"xM v[24:27],a[152:155],v[128:131],v[24:27]\nxR a[80:83],v224 offset:4096\nxC v170,v172,v173\n"
"xM v[24:27],a[184:187],v[136:139],v[24:27]\nxE v174,v174\nxM v[56:59],a[152:155],v[144:147],v[56:59]\nxR a[84:87],v225 offset:4096\n"
"xM v[56:59],a[184:187],v[152:155],v[56:59]\nxE v175,v175\nxM v[28:31],a[156:159],v[128:131],v[28:31]\nxR a[88:91],v226 offset:4096\nxC v171,v174,v175\n"
"xM v[28:31],a[188:191],v[136:139],v[28:31]\nxE v184,v184\nxM v[60:63],a[156:159],v[144:147],v[60:63]\nxL v233,s[12:15],s32 offen offset:3072 lds\n"
"xM v[60:63],a[188:191],v[152:155],v[60:63]\nxA s32,s32,16384\nxE v185,v185\n"
"xM v[208:211],a[192:195],v[128:131],v[208:211]\nxR a[92:95],v227 offset:4096\nxC v184,v184,v185\n"
"xM v[208:211],a[192:195],v[136:139],v[208:211]\nxE v186,v186\nxM v[212:215],a[192:195],v[144:147],v[212:215]\nxR a[96:99],v224 offset:8192\n"
"xM v[212:215],a[192:195],v[152:155],v[212:215]\nxE v187,v187\nxM v[128:131],a[64:67],a[0:3],v[192:195]\nxR a[100:103],v225 offset:8192\nxC v185,v186,v187\n"
"xM v[128:131],a[68:71],a[4:7],v[128:131]\nxW lgkmcnt(3)\nxE v188,v188\n"
"xM v[128:131],a[72:75],a[8:11],v[128:131]\nxR a[104:107],v226 offset:8192\nxM v[128:131],a[76:79],a[12:15],v[128:131]\nxE v189,v189\n"
"xM v[144:147],a[64:67],a[16:19],v[196:199]\nxR a[108:111],v227 offset:8192\nxC v186,v188,v189\n"
"xM v[144:147],a[68:71],a[20:23],v[144:147]\nxE v190,v190\nxM v[144:147],a[72:75],a[24:27],v[144:147]\nxR a[112:115],v224 offset:12288\n"
"xM v[144:147],a[76:79],a[28:31],v[144:147]\nxE v191,v191\nxM v[132:135],a[80:83],a[0:3],v[192:195]\nxR a[116:119],v225 offset:12288\nxC v187,v190,v191\n"
"xM v[132:135],a[84:87],a[4:7],v[132:135]\nxM v[132:135],a[88:91],a[8:11],v[132:135]\nxW lgkmcnt(3)\nxR a[120:123],v226 offset:12288\n"
"xM v[132:135],a[92:95],a[12:15],v[132:135]\nxM v[148:151],a[80:83],a[16:19],v[196:199]\nxM v[148:151],a[84:87],a[20:23],v[148:151]\nxE v128,v128\n"
"xM v[148:151],a[88:91],a[24:27],v[148:151]\nxR a[124:127],v227 offset:12288\nxM v[148:151],a[92:95],a[28:31],v[148:151]\nxE v129,v129\n"
"xM v[136:139],a[96:99],a[0:3],v[192:195]\nxC v128,v128,v129\nxM v[136:139],a[100:103],a[4:7],v[136:139]\nxE v130,v130\n"
"xM v[136:139],a[104:107],a[8:11],v[136:139]\nxW lgkmcnt(1)\nxM v[136:139],a[108:111],a[12:15],v[136:139]\nxE v131,v131\n"
"xM v[152:155],a[96:99],a[16:19],v[196:199]\nxC v129,v130,v131\nxM v[152:155],a[100:103],a[20:23],v[152:155]\nxE v132,v132\n"
"xM v[152:155],a[104:107],a[24:27],v[152:155]\nxM v[152:155],a[108:111],a[28:31],v[152:155]\nxE v133,v133\n"
"xM v[140:143],a[112:115],a[0:3],v[192:195]\nxC v130,v132,v133\nxM v[140:143],a[116:119],a[4:7],v[140:143]\nxE v134,v134\n"
"xM v[140:143],a[120:123],a[8:11],v[140:143]\nxW lgkmcnt(0)\nxM v[140:143],a[124:127],a[12:15],v[140:143]\nxE v135,v135\n"
"xM v[156:159],a[112:115],a[16:19],v[196:199]\nxC v131,v134,v135\nxM v[156:159],a[116:119],a[20:23],v[156:159]\nxE v144,v144\n"
"xM v[156:159],a[120:123],a[24:27],v[156:159]\nxM v[156:159],a[124:127],a[28:31],v[156:159]\nxE v145,v145\n"
"xM v[64:67],a[128:131],v[160:163],v[64:67]\nxW vmcnt(9)\ns_barrier\n"
"xM v[64:67],a[160:163],v[168:171],v[64:67]\nxE v146,v146\nxM v[96:99],a[128:131],v[176:179],v[96:99]\nxC v144,v144,v145\n"
"xM v[96:99],a[160:163],v[184:187],v[96:99]\nxE v147,v147\nxM v[68:71],a[132:135],v[160:163],v[68:71]\nxT a[128:129],v228 offset:0\nxC v145,v146,v147\n"
"xM v[68:71],a[164:167],v[168:171],v[68:71]\nxA m0,s36,81920\nxE v148,v148\n"
"xM v[100:103],a[132:135],v[176:179],v[100:103]\nxL v234,s[16:19],s33 offen offset:0 lds\nxM v[100:103],a[164:167],v[184:187],v[100:103]\nxE v149,v149\n"
"xM v[72:75],a[136:139],v[160:163],v[72:75]\nxT a[130:131],v228 offset:4096\nxC v146,v148,v149\n"
"xM v[72:75],a[168:171],v[168:171],v[72:75]\nxE v150,v150\nxM v[104:107],a[136:139],v[176:179],v[104:107]\nxT a[160:161],v228 offset:8192\n"
"xM v[104:107],a[168:171],v[184:187],v[104:107]\nxE v151,v151\nxM v[76:79],a[140:143],v[160:163],v[76:79]\nxT a[162:163],v228 offset:12288\nxC v147,v150,v151\n"
"xM v[76:79],a[172:175],v[168:171],v[76:79]\nxE v136,v136\nxM v[108:111],a[140:143],v[176:179],v[108:111]\nxT a[132:133],v229 offset:0\n"
"xM v[108:111],a[172:175],v[184:187],v[108:111]\nxE v137,v137\nxM v[80:83],a[144:147],v[160:163],v[80:83]\nxT a[134:135],v229 offset:4096\nxC v136,v136,v137\n"
"xM v[80:83],a[176:179],v[168:171],v[80:83]\nxE v138,v138\nxM v[112:115],a[144:147],v[176:179],v[112:115]\nxT a[164:165],v229 offset:8192\n"
"xM v[112:115],a[176:179],v[184:187],v[112:115]\nxE v139,v139\nxM v[84:87],a[148:151],v[160:163],v[84:87]\nxT a[166:167],v229 offset:12288\nxC v137,v138,v139\n"
"xM v[84:87],a[180:183],v[168:171],v[84:87]\nxE v140,v140\nxM v[116:119],a[148:151],v[176:179],v[116:119]\nv_max_f32 v236, v216, v220\nv_cmp_lt_f32 vcc, s60, v236\n"
"xM v[116:119],a[180:183],v[184:187],v[116:119]\nxE v141,v141\nxM v[88:91],a[152:155],v[160:163],v[88:91]\nxT a[136:137],v228 offset:512\nxC v138,v140,v141\n"
"xM v[88:91],a[184:187],v[168:171],v[88:91]\nxE v142,v142\nxM v[120:123],a[152:155],v[176:179],v[120:123]\nxL v234,s[16:19],s33 offen offset:2048 lds\n"
"xM v[120:123],a[184:187],v[184:187],v[120:123]\nxA s33,s33,16384\nxE v143,v143\n"
"xM v[92:95],a[156:159],v[160:163],v[92:95]\nxT a[138:139],v228 offset:4608\nxC v139,v142,v143\n"
"xM v[92:95],a[188:191],v[168:171],v[92:95]\nxE v152,v152\nxM v[124:127],a[156:159],v[176:179],v[124:127]\nxT a[168:169],v228 offset:8704\n"
"xM v[124:127],a[188:191],v[184:187],v[124:127]\nxE v153,v153\nxM v[216:219],a[192:195],v[160:163],v[216:219]\nxT a[170:171],v228 offset:12800\nxC v152,v152,v153\n"
"xM v[216:219],a[192:195],v[168:171],v[216:219]\nxA m0,s36,82944\nxE v154,v154\n"
"xM v[220:223],a[192:195],v[176:179],v[220:223]\nxL v234,s[16:19],s34 offen offset:0 lds\nxM v[220:223],a[192:195],v[184:187],v[220:223]\ns_cbranch_vccnz LB6_1_%=\nRET6_1_%=:\nxE v155,v155\n"
"xM v[160:163],a[64:67],a[32:35],v[200:203]\nxT a[140:141],v229 offset:512\nxC v153,v154,v155\n"
"xM v[160:163],a[68:71],a[36:39],v[160:163]\nxE v156,v156\nxM v[160:163],a[72:75],a[40:43],v[160:163]\nxT a[142:143],v229 offset:4608\n"
"xM v[160:163],a[76:79],a[44:47],v[160:163]\nxE v157,v157\nxM v[176:179],a[64:67],a[48:51],v[204:207]\nxT a[172:173],v229 offset:8704\nxC v154,v156,v157\n"
"xM v[176:179],a[68:71],a[52:55],v[176:179]\nxE v158,v158\nxM v[176:179],a[72:75],a[56:59],v[176:179]\nxT a[174:175],v229 offset:12800\n"
"xM v[176:179],a[76:79],a[60:63],v[176:179]\nxE v159,v159\nxM v[164:167],a[80:83],a[32:35],v[200:203]\nxT a[144:145],v228 offset:1024\nxC v155,v158,v159\n"
"xM v[164:167],a[84:87],a[36:39],v[164:167]\nxL v234,s[16:19],s34 offen offset:2048 lds\nxM v[164:167],a[88:91],a[40:43],v[164:167]\nxA s34,s34,16384\nxT a[146:147],v228 offset:5120\n"
"xM v[164:167],a[92:95],a[44:47],v[164:167]\nxT a[176:177],v228 offset:9216\nxM v[180:183],a[80:83],a[48:51],v[204:207]\nxT a[178:179],v228 offset:13312\n"
"xM v[180:183],a[84:87],a[52:55],v[180:183]\nxE v160,v160\nxM v[180:183],a[88:91],a[56:59],v[180:183]\nxT a[148:149],v229 offset:1024\n"
"xM v[180:183],a[92:95],a[60:63],v[180:183]\nxE v161,v161\nxM v[168:171],a[96:99],a[32:35],v[200:203]\nbuffer_load_dword a255, v253, s[56:59], s48 offen\n"
"xM v[168:171],a[100:103],a[36:39],v[168:171]\nxA s48,s48,s46\nxE v162,v162\n"
"xM v[168:171],a[104:107],a[40:43],v[168:171]\nxT a[150:151],v229 offset:5120\nxC v160,v160,v161\n"
"xM v[168:171],a[108:111],a[44:47],v[168:171]\nxE v163,v163\nxM v[184:187],a[96:99],a[48:51],v[204:207]\nxT a[180:181],v229 offset:9216\nxC v161,v162,v163\n"
"xM v[184:187],a[100:103],a[52:55],v[184:187]\nxE v164,v164\nxM v[184:187],a[104:107],a[56:59],v[184:187]\nxT a[182:183],v229 offset:13312\n"
"xM v[184:187],a[108:111],a[60:63],v[184:187]\nxE v165,v165\nxM v[172:175],a[112:115],a[32:35],v[200:203]\nxT a[152:153],v228 offset:1536\nxC v162,v164,v165\n"
"xM v[172:175],a[116:119],a[36:39],v[172:175]\nxA m0,s36,0\nxE v166,v166\n"
"xM v[172:175],a[120:123],a[40:43],v[172:175]\nxL v230,s[12:15],s32 offen offset:0 lds\nxM v[172:175],a[124:127],a[44:47],v[172:175]\nxE v167,v167\n"
"xM v[188:191],a[112:115],a[48:51],v[204:207]\nxT a[154:155],v228 offset:5632\nxC v163,v166,v167\n"
"xM v[188:191],a[116:119],a[52:55],v[188:191]\nxE v176,v176\nxM v[188:191],a[120:123],a[56:59],v[188:191]\nxT a[184:185],v228 offset:9728\n"
"xM v[188:191],a[124:127],a[60:63],v[188:191]\nxW lgkmcnt(3)\nxE v177,v177\n"
"xM v[0:3],a[128:131],v[128:131],v[0:3]\ns_sub_u32 s35, s35, 1\nxT a[186:187],v228 offset:13824\nxC v176,v176,v177\n"
"xM v[0:3],a[160:163],v[136:139],v[0:3]\nxE v178,v178\nxM v[32:35],a[128:131],v[144:147],v[32:35]\nxT a[156:157],v229 offset:1536\n"
"xM v[32:35],a[160:163],v[152:155],v[32:35]\nxE v179,v179\nxM v[4:7],a[132:135],v[128:131],v[4:7]\nxT a[158:159],v229 offset:5632\nxC v177,v178,v179\n"
"xM v[4:7],a[164:167],v[136:139],v[4:7]\nxE v180,v180\nxM v[36:39],a[132:135],v[144:147],v[36:39]\nxL v231,s[12:15],s32 offen offset:1024 lds\n"
"xM v[36:39],a[164:167],v[152:155],v[36:39]\nxE v181,v181\nxM v[8:11],a[136:139],v[128:131],v[8:11]\nxT a[188:189],v229 offset:9728\nxC v178,v180,v181\n"
"xM v[8:11],a[168:171],v[136:139],v[8:11]\nxE v182,v182\nxM v[40:43],a[136:139],v[144:147],v[40:43]\nxT a[190:191],v229 offset:13824\n"
"xM v[40:43],a[168:171],v[152:155],v[40:43]\nxE v183,v183\nxM v[12:15],a[140:143],v[128:131],v[12:15]\nxR a[64:67],v224 offset:16384\nxC v179,v182,v183\n"
"xM v[12:15],a[172:175],v[136:139],v[12:15]\nxE v168,v168\nxM v[44:47],a[140:143],v[144:147],v[44:47]\nxR a[68:71],v225 offset:16384\n"
"xM v[44:47],a[172:175],v[152:155],v[44:47]\nxE v169,v169\nxM v[16:19],a[144:147],v[128:131],v[16:19]\nxR a[72:75],v226 offset:16384\nxC v168,v168,v169\n"
"xM v[16:19],a[176:179],v[136:139],v[16:19]\nxE v170,v170\nxM v[48:51],a[144:147],v[144:147],v[48:51]\nxL v232,s[12:15],s32 offen offset:2048 lds\n"
"xM v[48:51],a[176:179],v[152:155],v[48:51]\nxE v171,v171\nxM v[20:23],a[148:151],v[128:131],v[20:23]\nxR a[76:79],v227 offset:16384\nxC v169,v170,v171\n"
"xM v[20:23],a[180:183],v[136:139],v[20:23]\nxE v172,v172\nxM v[52:55],a[148:151],v[144:147],v[52:55]\nv_max_f32 v235, v208, v212\nv_cmp_lt_f32 vcc, s60, v235\n"
"xM v[52:55],a[180:183],v[152:155],v[52:55]\nxW lgkmcnt(2)\nxE v173,v173\n"
"xM v[24:27],a[152:155],v[128:131],v[24:27]\nxR a[80:83],v224 offset:20480\nxC v170,v172,v173\n"
"xM v[24:27],a[184:187],v[136:139],v[24:27]\nxE v174,v174\nxM v[56:59],a[152:155],v[144:147],v[56:59]\nxR a[84:87],v225 offset:20480\n"
"xM v[56:59],a[184:187],v[152:155],v[56:59]\nxE v175,v175\nxM v[28:31],a[156:159],v[128:131],v[28:31]\nxR a[88:91],v226 offset:20480\nxC v171,v174,v175\n"
"xM v[28:31],a[188:191],v[136:139],v[28:31]\nxE v184,v184\nxM v[60:63],a[156:159],v[144:147],v[60:63]\nxL v233,s[12:15],s32 offen offset:3072 lds\n"
"xM v[60:63],a[188:191],v[152:155],v[60:63]\nxA s32,s32,16384\nxE v185,v185\n"
"xM v[208:211],a[192:195],v[128:131],v[208:211]\ns_mov_b32 s60, 0x4d800000\nxR a[92:95],v227 offset:20480\nxC v184,v184,v185\n"
"xM v[208:211],a[192:195],v[136:139],v[208:211]\nxE v186,v186\nxM v[212:215],a[192:195],v[144:147],v[212:215]\nxR a[96:99],v224 offset:24576\n"
"xM v[212:215],a[192:195],v[152:155],v[212:215]\ns_cbranch_vccnz LB6_0_%=\nRET6_0_%=:\nxE v187,v187\ns_cmp_lg_u32 s35, 0\ns_cbranch_scc1 LOOP_%=\n"
"xM v[128:131],a[64:67],a[0:3],v[192:195]\nxR a[100:103],v225 offset:24576\nxC v185,v186,v187\nv_min_u32 v245, 35, v255\nv_lshlrev_b32 v245, 3, v245\ns_lshl_b32 s53, s70, 11\n"
"xA s53,s53,1214144\nbuffer_load_dwordx2 v[246:247], v245, s[96:99], s53 offen sc1\ns_getpc_b64 s[28:29]\nPCOSTART_%=:\nxA s28,s28,OSTART_%=-PCOSTART_%=\ns_addc_u32 s29, s29, 0\n"
"s_and_b32 s29, s29, 0xffff\ns_mov_b32 s30, KEND_%=-OSTART_%=\ns_mov_b32 s31, 0x20000\nv_lshlrev_b32 v252, 7, v255\ns_mul_i32 s38, %[wid], 16384\nbuffer_load_dword a255, v252, s[28:31], s38 offen\n"
"xA s38,s38,8192\nbuffer_load_dword a255, v252, s[28:31], s38 offen\nxA s38,s38,8192\n"
"xM v[128:131],a[68:71],a[4:7],v[128:131]\nxW lgkmcnt(3)\nxE v188,v188\n"
"xM v[128:131],a[72:75],a[8:11],v[128:131]\nxR a[104:107],v226 offset:24576\nv_lshrrev_b32 v231, 4, v255\n"
"xM v[128:131],a[76:79],a[12:15],v[128:131]\nxE v189,v189\nxM v[144:147],a[64:67],a[16:19],v[196:199]\nxR a[108:111],v227 offset:24576\nxC v186,v188,v189\n"
"xM v[144:147],a[68:71],a[20:23],v[144:147]\nxE v190,v190\nxM v[144:147],a[72:75],a[24:27],v[144:147]\nxR a[112:115],v224 offset:28672\nv_mul_u32_u24 v239, 9216, v231\n"
"xM v[144:147],a[76:79],a[28:31],v[144:147]\nxE v191,v191\nxM v[132:135],a[80:83],a[0:3],v[192:195]\nxR a[116:119],v225 offset:28672\nxC v187,v190,v191\n"
"xM v[132:135],a[84:87],a[4:7],v[132:135]\nxW lgkmcnt(4)\nv_and_b32 v232, 15, v255\nv_lshl_add_u32 v239, v232, 4, v239\n"
"xM v[132:135],a[88:91],a[8:11],v[132:135]\ns_lshl_b32 s62, %[wid], 8\nxR a[120:123],v226 offset:28672\nv_add_u32 v239, s62, v239\n"
"xM v[132:135],a[92:95],a[12:15],v[132:135]\nv_lshrrev_b32 v232, 5, v255\nv_lshlrev_b32 v232, 6, v232\n"
"xM v[148:151],a[80:83],a[16:19],v[196:199]\nv_and_b32 v231, 1, v231\nv_lshl_add_u32 v232, v231, 4, v232\n"
"xM v[148:151],a[84:87],a[20:23],v[148:151]\nxE v128,v128\nxM v[148:151],a[88:91],a[24:27],v[148:151]\nxR a[124:127],v227 offset:28672\nv_bfe_u32 v231, v255, 3, 1\n"
"xM v[148:151],a[92:95],a[28:31],v[148:151]\nxE v129,v129\nxM v[136:139],a[96:99],a[0:3],v[192:195]\nxW lgkmcnt(2)\nxC v128,v128,v129\nv_lshl_add_u32 v232, v231, 2, v232\n"
"xM v[136:139],a[100:103],a[4:7],v[136:139]\ns_lshr_b32 s62, %[wid], 1\nxE v130,v130\n"
"xM v[136:139],a[104:107],a[8:11],v[136:139]\ns_lshl_b32 s62, s62, 5\nxM v[136:139],a[108:111],a[12:15],v[136:139]\ns_and_b32 s63, %[wid], 1\nxE v131,v131\n"
"xM v[152:155],a[96:99],a[16:19],v[196:199]\ns_lshl_b32 s63, s63, 3\nxC v129,v130,v131\n"
"xM v[152:155],a[100:103],a[20:23],v[152:155]\nxA s62,s62,s63\nxE v132,v132\n"
"xM v[152:155],a[104:107],a[24:27],v[152:155]\nv_add_u32 v232, s62, v232\nv_lshlrev_b32 v241, 2, v232\n"
"xM v[152:155],a[108:111],a[28:31],v[152:155]\nxE v133,v133\nxM v[140:143],a[112:115],a[0:3],v[192:195]\nxC v130,v132,v133\nv_and_b32 v231, 7, v255\n"
"xM v[140:143],a[116:119],a[4:7],v[140:143]\nxW lgkmcnt(0)\nxE v134,v134\n"
"xM v[140:143],a[120:123],a[8:11],v[140:143]\nv_mul_u32_u24 v231, 1152, v231\nv_lshl_add_u32 v240, v232, 1, v231\n"
"xM v[140:143],a[124:127],a[12:15],v[140:143]\nxE v135,v135\nxM v[156:159],a[112:115],a[16:19],v[196:199]\nxC v131,v134,v135\nv_mov_b32 v242, s68\n"
"xM v[156:159],a[116:119],a[20:23],v[156:159]\nxE v144,v144\nxM v[156:159],a[120:123],a[24:27],v[156:159]\nv_mov_b32 v243, s69\nv_lshlrev_b32 v238, 3, v255\n"
"xM v[156:159],a[124:127],a[28:31],v[156:159]\ns_min_u32 s62, %[wid], 1\nxE v145,v145\n"
"xM v[64:67],a[128:131],v[160:163],v[64:67]\nxW vmcnt(3)\ns_barrier\n"
"xM v[64:67],a[160:163],v[168:171],v[64:67]\nxA m0,s44,0\nxE v146,v146\n"
"xM v[96:99],a[128:131],v[176:179],v[96:99]\nxA s39,s42,896\nxC v144,v144,v145\ns_lshl_b32 s62, s62, 30\n"
"xM v[96:99],a[160:163],v[184:187],v[96:99]\nxL v248,s[24:27],s39 offen lds\nxE v147,v147\n"
"xM v[68:71],a[132:135],v[160:163],v[68:71]\nxT a[128:129],v228 offset:32768\nxC v145,v146,v147\n"
"xM v[68:71],a[164:167],v[168:171],v[68:71]\nxE v148,v148\nxM v[100:103],a[132:135],v[176:179],v[100:103]\nxA m0,s44,1024\nv_add_u32 v238, s62, v238\ns_lshl_b32 s49, s70, 4\n"
"xM v[100:103],a[164:167],v[184:187],v[100:103]\nxA s39,s42,1024\nxE v149,v149\n"
"xM v[72:75],a[136:139],v[160:163],v[72:75]\nxL v248,s[24:27],s39 offen lds\nxT a[130:131],v228 offset:36864\nxC v146,v148,v149\n"
"xM v[72:75],a[168:171],v[168:171],v[72:75]\nxE v150,v150\nxM v[104:107],a[136:139],v[176:179],v[104:107]\nxT a[160:161],v228 offset:40960\nxA s49,s49,s71\n"
"xM v[104:107],a[168:171],v[184:187],v[104:107]\ns_mul_i32 s62, s49, 9216\nxE v151,v151\n"
"xM v[76:79],a[140:143],v[160:163],v[76:79]\nxA m0,s44,2048\nxT a[162:163],v228 offset:45056\nxC v147,v150,v151\n"
"xM v[76:79],a[172:175],v[168:171],v[76:79]\nxA s39,s42,10112\nxE v136,v136\n"
"xM v[108:111],a[140:143],v[176:179],v[108:111]\nxL v248,s[24:27],s39 offen lds\nxT a[132:133],v229 offset:32768\n"
"xM v[108:111],a[172:175],v[184:187],v[108:111]\nxE v137,v137\nxM v[80:83],a[144:147],v[160:163],v[80:83]\nxT a[134:135],v229 offset:36864\nxC v136,v136,v137\n"
"xM v[80:83],a[176:179],v[168:171],v[80:83]\nxA m0,s44,3072\nxE v138,v138\n"
"xM v[112:115],a[144:147],v[176:179],v[112:115]\nxA s39,s42,10240\nxT a[164:165],v229 offset:40960\nTC0_3_1_%=:\ns_cbranch_execz TDONE0_%=\nxA s12,s72,s62\n"
"s_addc_u32 s13, s73, 0\nxM v[112:115],a[176:179],v[184:187],v[112:115]\ns_and_b32 s13, s13, 0xffff\nxL v248,s[24:27],s39 offen lds\nxE v139,v139\n"
"xM v[84:87],a[148:151],v[160:163],v[84:87]\ns_mov_b32 s14, 9216\nxT a[166:167],v229 offset:45056\nxC v137,v138,v139\n"
"xM v[84:87],a[180:183],v[168:171],v[84:87]\ns_mov_b32 s15, 0x20000\nxE v140,v140\n"
"xM v[116:119],a[148:151],v[176:179],v[116:119]\nxA m0,s44,16384\ns_mul_i32 s49, s49, 73728\nxA s49,s49,7864320\n"
"xM v[116:119],a[180:183],v[184:187],v[116:119]\nxA s39,s42,74624\nxE v141,v141\n"
"xM v[88:91],a[152:155],v[160:163],v[88:91]\nxL v248,s[24:27],s39 offen lds\nxT a[136:137],v228 offset:33280\nxC v138,v140,v141\n"
"xM v[88:91],a[184:187],v[168:171],v[88:91]\nxE v142,v142\nxM v[120:123],a[152:155],v[176:179],v[120:123]\ns_lshl_b32 s51, s70, 11\n"
"xM v[120:123],a[184:187],v[184:187],v[120:123]\nxA s51,s51,1130496\nxE v143,v143\n"
"xM v[92:95],a[156:159],v[160:163],v[92:95]\nxA m0,s44,17408\nxT a[138:139],v228 offset:37376\nxC v139,v142,v143\n"
"xM v[92:95],a[188:191],v[168:171],v[92:95]\nxA s39,s42,74752\nxE v152,v152\n"
"xM v[124:127],a[156:159],v[176:179],v[124:127]\nxL v248,s[24:27],s39 offen lds\nxT a[168:169],v228 offset:41472\n"
"xM v[124:127],a[188:191],v[184:187],v[124:127]\nxE v153,v153\nxM v[216:219],a[192:195],v[160:163],v[216:219]\nxT a[170:171],v228 offset:45568\nxC v152,v152,v153\n"
"xM v[216:219],a[192:195],v[168:171],v[216:219]\nxA m0,s44,18432\nxE v154,v154\n"
"xM v[220:223],a[192:195],v[176:179],v[220:223]\nxA s39,s42,83840\ns_and_b32 s62, s71, 1\n"
"xM v[220:223],a[192:195],v[184:187],v[220:223]\ns_lshl_b32 s62, s62, 10\nxL v248,s[24:27],s39 offen lds\nxE v155,v155\n"
"xM v[160:163],a[64:67],a[32:35],v[200:203]\nxA s50,s51,s62\nxT a[140:141],v229 offset:33280\nxC v153,v154,v155\n"
"xM v[160:163],a[68:71],a[36:39],v[160:163]\ns_lshr_b32 s63, s71, 1\nxE v156,v156\n"
"xM v[160:163],a[72:75],a[40:43],v[160:163]\nxA m0,s44,19456\nxT a[142:143],v229 offset:37376\ns_lshl_b32 s62, s63, 2\n"
"xM v[160:163],a[76:79],a[44:47],v[160:163]\nxA s39,s42,83968\nxE v157,v157\n"
"xM v[176:179],a[64:67],a[48:51],v[204:207]\nxL v248,s[24:27],s39 offen lds\nxT a[172:173],v229 offset:41472\nxC v154,v156,v157\n"
"xM v[176:179],a[68:71],a[52:55],v[176:179]\nxE v158,v158\nxM v[176:179],a[72:75],a[56:59],v[176:179]\nxT a[174:175],v229 offset:45568\ns_lshl_b32 s54, 15, s62\n"
"xM v[176:179],a[76:79],a[60:63],v[176:179]\nxA m0,s44,32768\nxE v159,v159\n"
"xM v[164:167],a[80:83],a[32:35],v[200:203]\nxA s39,s42,148352\nxT a[144:145],v228 offset:33792\nxC v155,v158,v159\n"
"xM v[164:167],a[84:87],a[36:39],v[164:167]\nxL v248,s[24:27],s39 offen lds\nxM v[164:167],a[88:91],a[40:43],v[164:167]\nxT a[146:147],v228 offset:37888\nxA s62,s62,%[wid]\n"
"xM v[164:167],a[92:95],a[44:47],v[164:167]\ns_lshl_b32 s62, s62, 3\nxT a[176:177],v228 offset:41984\nxA s50,s50,s62\n"
"xM v[180:183],a[80:83],a[48:51],v[204:207]\nxA m0,s44,33792\nxT a[178:179],v228 offset:46080\ns_xor_b32 s62, s63, s71\n"
"xM v[180:183],a[84:87],a[52:55],v[180:183]\nxA s39,s42,148480\nxE v160,v160\n"
"xM v[180:183],a[88:91],a[56:59],v[180:183]\nxL v248,s[24:27],s39 offen lds\nxT a[148:149],v229 offset:33792\n"
"xM v[180:183],a[92:95],a[60:63],v[180:183]\nxE v161,v161\nxM v[168:171],a[96:99],a[32:35],v[200:203]\ns_and_b32 s62, s62, 1\ns_sub_u32 s62, s62, 1\n"
"xM v[168:171],a[100:103],a[36:39],v[168:171]\nxA m0,s44,34816\nxE v162,v162\n"
"xM v[168:171],a[104:107],a[40:43],v[168:171]\nxA s39,s42,157568\nxT a[150:151],v229 offset:37888\nxC v160,v160,v161\n"
"xM v[168:171],a[108:111],a[44:47],v[168:171]\nxL v248,s[24:27],s39 offen lds\nxE v163,v163\n"
"xM v[184:187],a[96:99],a[48:51],v[204:207]\nxT a[180:181],v229 offset:41984\nxC v161,v162,v163\n"
"xM v[184:187],a[100:103],a[52:55],v[184:187]\nxE v164,v164\nxM v[184:187],a[104:107],a[56:59],v[184:187]\nxA m0,s44,35840\nxT a[182:183],v229 offset:46080\ns_and_b32 s54, s54, s62\n"
"xM v[184:187],a[108:111],a[60:63],v[184:187]\nxA s39,s42,157696\nxE v165,v165\n"
"xM v[172:175],a[112:115],a[32:35],v[200:203]\nxL v248,s[24:27],s39 offen lds\nxT a[152:153],v228 offset:34304\nxC v162,v164,v165\n"
"xM v[172:175],a[116:119],a[36:39],v[172:175]\nxE v166,v166\nxM v[172:175],a[120:123],a[40:43],v[172:175]\ns_not_b32 s54, s54\n"
"xM v[172:175],a[124:127],a[44:47],v[172:175]\ns_and_b32 s62, s63, 1\nxE v167,v167\n"
"xM v[188:191],a[112:115],a[48:51],v[204:207]\nxA m0,s44,49152\nxT a[154:155],v228 offset:38400\nxC v163,v166,v167\n"
"xM v[188:191],a[116:119],a[52:55],v[188:191]\nxA s39,s42,222080\nxE v176,v176\n"
"xM v[188:191],a[120:123],a[56:59],v[188:191]\nxL v248,s[24:27],s39 offen lds\nxT a[184:185],v228 offset:42496\n"
"xM v[188:191],a[124:127],a[60:63],v[188:191]\nxW lgkmcnt(3)\nxE v177,v177\n"
"xM v[0:3],a[128:131],v[128:131],v[0:3]\nxT a[186:187],v228 offset:46592\nxC v176,v176,v177\n"
"xM v[0:3],a[160:163],v[136:139],v[0:3]\nxA m0,s44,50176\nxE v178,v178\n"
"xM v[32:35],a[128:131],v[144:147],v[32:35]\nxA s39,s42,222208\nxT a[156:157],v229 offset:34304\ns_lshl_b32 s62, s62, 10\n"
"xM v[32:35],a[160:163],v[152:155],v[32:35]\nxA s51,s51,s62\nxL v248,s[24:27],s39 offen lds\nxE v179,v179\n"
"xM v[4:7],a[132:135],v[128:131],v[4:7]\ns_lshl_b32 s34, %[wid], 10\nxT a[158:159],v229 offset:38400\nxC v177,v178,v179\n"
"xM v[4:7],a[164:167],v[136:139],v[4:7]\ns_min_u32 s35, s34, 2048\nxE v180,v180\n"
"xM v[36:39],a[132:135],v[144:147],v[36:39]\nxA m0,s44,51200\ns_and_b32 s43, s71, 1\ns_mul_i32 s32, %[wid], 3072\n"
"xM v[36:39],a[164:167],v[152:155],v[36:39]\nxA s39,s42,231296\nxE v181,v181\n"
"xM v[8:11],a[136:139],v[128:131],v[8:11]\nxL v248,s[24:27],s39 offen lds\nxT a[188:189],v229 offset:42496\nxC v178,v180,v181\n"
"xM v[8:11],a[168:171],v[136:139],v[8:11]\nxE v182,v182\nxM v[40:43],a[136:139],v[144:147],v[40:43]\nxT a[190:191],v229 offset:46592\ns_mul_i32 s36, %[wid], 6144\n"
"xM v[40:43],a[168:171],v[152:155],v[40:43]\ns_mul_i32 s33, %[wid], 221184\nxE v183,v183\n"
"xM v[12:15],a[140:143],v[128:131],v[12:15]\nxA m0,s44,52224\nxC v179,v182,v183\ns_lshl_b32 s62, s43, 6\n"
"xM v[12:15],a[172:175],v[136:139],v[12:15]\nxA s39,s42,231424\nxE v168,v168\n"
"xM v[44:47],a[140:143],v[144:147],v[44:47]\nxL v248,s[24:27],s39 offen lds\nxM v[44:47],a[172:175],v[152:155],v[44:47]\nxE v169,v169\n"
"xM v[16:19],a[144:147],v[128:131],v[16:19]\nxC v168,v168,v169\nxA s33,s33,s62\n"
"xM v[16:19],a[176:179],v[136:139],v[16:19]\ns_mul_i32 s62, s71, 576\nxE v170,v170\n"
"xM v[48:51],a[144:147],v[144:147],v[48:51]\nxA s16,s82,s62\ns_addc_u32 s17, s83, 0\n"
"xM v[48:51],a[176:179],v[152:155],v[48:51]\ns_and_b32 s17, s17, 0xffff\nxE v171,v171\n"
"xM v[20:23],a[148:151],v[128:131],v[20:23]\ns_mov_b32 s18, 5299776\nxC v169,v170,v171\n"
"xM v[20:23],a[180:183],v[136:139],v[20:23]\ns_mov_b32 s19, 0x20000\nxE v172,v172\n"
"xM v[52:55],a[148:151],v[144:147],v[52:55]\nxA s20,s84,s62\ns_addc_u32 s21, s85, 0\n"
"xM v[52:55],a[180:183],v[152:155],v[52:55]\ns_and_b32 s21, s21, 0xffff\nxE v173,v173\nxW lgkmcnt(6)\n"
"xM v[24:27],a[152:155],v[128:131],v[24:27]\ns_mov_b32 s22, 5299776\nxC v170,v172,v173\nxW lgkmcnt(4)\n"
"xM v[24:27],a[184:187],v[136:139],v[24:27]\ns_mov_b32 s23, 0x20000\nxE v174,v174\n"
"xM v[56:59],a[152:155],v[144:147],v[56:59]\ns_and_b32 s63, s71, 1\nxM v[56:59],a[184:187],v[152:155],v[56:59]\ns_xor_b32 s62, s63, 1\nxE v175,v175\nxW lgkmcnt(2)\n"
"xM v[28:31],a[156:159],v[128:131],v[28:31]\ns_mul_i32 s45, s62, 294912\nxC v171,v174,v175\nxW lgkmcnt(0)\n"
"xM v[28:31],a[188:191],v[136:139],v[28:31]\ns_mul_i32 s62, %[wid], 18432\nxE v184,v184\n"
"xM v[60:63],a[156:159],v[144:147],v[60:63]\nxA s45,s45,s62\nxM v[60:63],a[188:191],v[152:155],v[60:63]\ns_lshl_b32 s46, s70, 3\nxE v185,v185\n"
"xM v[208:211],a[192:195],v[128:131],v[208:211]\nxC v184,v184,v185\nxM v[208:211],a[192:195],v[136:139],v[208:211]\nxE v186,v186\n"
"xM v[212:215],a[192:195],v[144:147],v[212:215]\nxM v[212:215],a[192:195],v[152:155],v[212:215]\nxE v187,v187\ns_barrier\nxC v185,v186,v187\nxE v188,v188\n"
"xE v189,v189\ns_nop 0\nxC v186,v188,v189\nxA s46,s46,s63\ns_mul_i32 s46, s46, 147456\ns_lshr_b32 s62, s71, 1\n"
"s_mul_i32 s62, s62, 9216\nxA s46,s46,s62\ns_lshl_b32 s62, %[wid], 9\nxA s46,s46,s62\nxA s46,s46,7864320\nxE v190,v190\n"
"s_lshl_b32 s62, s70, 4\nxA s62,s62,s71\ns_lshl_b32 s47, s62, 15\nxA s47,s47,102170624\ns_lshl_b32 s60, s62, 7\ns_lshl_b32 s63, %[wid], 3\n"
"xA s60,s60,s63\nxA s60,s60,1245280\nxE v191,v191\ns_nop 0\nxC v187,v190,v191\ns_xor_b32 s62, s62, 1\n"
"s_lshl_b32 s48, s62, 15\nxA s48,s48,102170624\ns_lshl_b32 s61, s62, 7\nxA s61,s61,1245184\nv_bfe_u32 v231, v255, 3, 1\nv_mul_u32_u24 v231, 73728, v231\n"
"v_and_b32 v232, 7, v255\nv_lshl_add_u32 v231, v232, 4, v231\nv_lshrrev_b32 v232, 4, v255\nv_lshl_add_u32 v249, v232, 7, v231\nv_add_u32 v250, 294912, v249\nv_add_u32 v251, 589824, v249\n"
"v_add_u32 v252, 884736, v249\nv_lshrrev_b32 v233, 5, v255\nv_lshl_add_u32 v231, v233, 7, v231\nv_and_b32 v232, 1, v232\nv_mul_u32_u24 v233, 294912, v232\nv_add_u32 v253, v231, v233\n"
"v_bfe_u32 v231, v255, 4, 1\nv_and_b32 v233, 3, v255\nv_xor_b32 v231, v231, v233\nv_lshlrev_b32 v231, 4, v231\nv_and_b32 v244, 0xffffffcf, v248\nv_add_u32 v244, v244, v231\n"
"s_mul_i32 s62, %[wid], 18432\nv_add_u32 v244, s62, v244\ns_lshl_b32 s62, %[wid], 9\ns_sub_u32 s62, 32768, s62\nv_subrev_u32 v192, s62, v228\nv_xor_b32 v193, 32, v192\n"
"v_add_u32 v194, 65536, v192\nv_add_u32 v195, 65536, v193\nv_add_u32 v196, 131072, v192\nv_add_u32 v197, 131072, v193\nv_lshrrev_b32 v230, 5, v255\nv_lshlrev_b32 v198, 10, v230\n"
"v_bfe_u32 v230, v255, 2, 3\nv_lshl_add_u32 v198, v230, 6, v198\nv_and_b32 v230, 1, v255\nv_lshl_add_u32 v198, v230, 5, v198\nv_bfe_u32 v230, v255, 1, 1\nv_lshl_add_u32 v198, v230, 3, v198\n"
"s_and_b32 s62, %[wid], 1\nv_bfe_u32 v230, v255, 4, 1\nv_xor_b32 v230, s62, v230\nv_lshl_add_u32 v198, v230, 4, v198\ns_lshr_b32 s62, %[wid], 1\ns_lshl_b32 s62, s62, 9\n"
"v_add_u32 v198, s62, v198\nv_lshlrev_b32 v199, 4, v255\nv_add_u32 v199, 65536, v199\nv_add_u32 v234, s34, v199\ns_lshl_b32 s37, %[wid], 8\nv_rcp_f32 v225, v212\n"
"s_nop 0\nxU v32,v32,v225\nxU v33,v33,v225\nxU v34,v34,v225\nxU v35,v35,v225\nxU v36,v36,v225\n"
"xU v37,v37,v225\nxU v38,v38,v225\nxU v39,v39,v225\nxC v32,v32,v33\nxC v33,v34,v35\nxC v34,v36,v37\n"
"xC v35,v38,v39\nxA s63,s47,0\nxS v[32:35],v234,s[96:99],s63 offen sc1\nxU v40,v40,v225\nxU v41,v41,v225\nxU v42,v42,v225\n"
"xU v43,v43,v225\nxU v44,v44,v225\nxU v45,v45,v225\nxU v46,v46,v225\nxU v47,v47,v225\nxC v40,v40,v41\n"
"xC v41,v42,v43\nxC v42,v44,v45\nxC v43,v46,v47\nxA s63,s47,4096\nxS v[40:43],v234,s[96:99],s63 offen sc1\nxU v48,v48,v225\n"
"xU v49,v49,v225\nxU v50,v50,v225\nxU v51,v51,v225\nxU v52,v52,v225\nxU v53,v53,v225\nxU v54,v54,v225\n"
"xU v55,v55,v225\nxC v48,v48,v49\nxM v[64:67],a[128:131],v[160:163],v[64:67]\nxC v49,v50,v51\nxC v50,v52,v53\n"
"xM v[64:67],a[160:163],v[168:171],v[64:67]\nxC v51,v54,v55\nxA s63,s47,8192\n"
"xM v[96:99],a[128:131],v[176:179],v[96:99]\nxS v[48:51],v234,s[96:99],s63 offen sc1\nxU v56,v56,v225\n"
"xM v[96:99],a[160:163],v[184:187],v[96:99]\nxU v57,v57,v225\nxU v58,v58,v225\n"
"xM v[68:71],a[132:135],v[160:163],v[68:71]\nxU v59,v59,v225\nxU v60,v60,v225\n"
"xM v[68:71],a[164:167],v[168:171],v[68:71]\nxU v61,v61,v225\nxU v62,v62,v225\n"
"xM v[100:103],a[132:135],v[176:179],v[100:103]\nxU v63,v63,v225\nxC v56,v56,v57\n"
"xM v[100:103],a[164:167],v[184:187],v[100:103]\nxC v57,v58,v59\nxC v58,v60,v61\n"
"xM v[72:75],a[136:139],v[160:163],v[72:75]\nxC v59,v62,v63\nxA s63,s47,12288\n"
"xM v[72:75],a[168:171],v[168:171],v[72:75]\nxS v[56:59],v234,s[96:99],s63 offen sc1\nv_rcp_f32 v224, v208\n"
"xM v[104:107],a[136:139],v[176:179],v[104:107]\nxU v0,v0,v224\nxU v1,v1,v224\n"
"xM v[104:107],a[168:171],v[184:187],v[104:107]\nxU v2,v2,v224\nxU v3,v3,v224\n"
"xM v[76:79],a[140:143],v[160:163],v[76:79]\nxU v4,v4,v224\nxU v5,v5,v224\n"
"xM v[76:79],a[172:175],v[168:171],v[76:79]\nxU v6,v6,v224\nxU v7,v7,v224\n"
"xM v[108:111],a[140:143],v[176:179],v[108:111]\nxC v0,v0,v1\nxC v1,v2,v3\n"
"xM v[108:111],a[172:175],v[184:187],v[108:111]\nxC v2,v4,v5\nxC v3,v6,v7\n"
"xM v[80:83],a[144:147],v[160:163],v[80:83]\nds_write_b128 v234, v[0:3] offset:0\nxU v8,v8,v224\n"
"xM v[80:83],a[176:179],v[168:171],v[80:83]\nxU v9,v9,v224\nxU v10,v10,v224\n"
"xM v[112:115],a[144:147],v[176:179],v[112:115]\nxU v11,v11,v224\nxU v12,v12,v224\n"
"xM v[112:115],a[176:179],v[184:187],v[112:115]\nxU v13,v13,v224\nxU v14,v14,v224\n"
"xM v[84:87],a[148:151],v[160:163],v[84:87]\nxU v15,v15,v224\nxC v8,v8,v9\n"
"xM v[84:87],a[180:183],v[168:171],v[84:87]\nxC v9,v10,v11\nxC v10,v12,v13\n"
"xM v[116:119],a[148:151],v[176:179],v[116:119]\nxC v11,v14,v15\nds_write_b128 v234, v[8:11] offset:4096\n"
"xM v[116:119],a[180:183],v[184:187],v[116:119]\nxU v16,v16,v224\nxU v17,v17,v224\n"
"xM v[88:91],a[152:155],v[160:163],v[88:91]\nxU v18,v18,v224\nxU v19,v19,v224\n"
"xM v[88:91],a[184:187],v[168:171],v[88:91]\nxU v20,v20,v224\nxU v21,v21,v224\n"
"xM v[120:123],a[152:155],v[176:179],v[120:123]\nxU v22,v22,v224\nxU v23,v23,v224\n"
"xM v[120:123],a[184:187],v[184:187],v[120:123]\nxC v16,v16,v17\nxC v17,v18,v19\n"
"xM v[92:95],a[156:159],v[160:163],v[92:95]\nxC v18,v20,v21\nxC v19,v22,v23\n"
"xM v[92:95],a[188:191],v[168:171],v[92:95]\nds_write_b128 v234, v[16:19] offset:8192\nxU v24,v24,v224\n"
"xM v[124:127],a[156:159],v[176:179],v[124:127]\nxU v25,v25,v224\nxU v26,v26,v224\n"
"xM v[124:127],a[188:191],v[184:187],v[124:127]\nxU v27,v27,v224\nxU v28,v28,v224\n"
"xM v[216:219],a[192:195],v[160:163],v[216:219]\nxU v29,v29,v224\nxU v30,v30,v224\n"
"xM v[216:219],a[192:195],v[168:171],v[216:219]\nxU v31,v31,v224\nxC v24,v24,v25\n"
"xM v[220:223],a[192:195],v[176:179],v[220:223]\nxC v25,v26,v27\nxC v26,v28,v29\n"
"xM v[220:223],a[192:195],v[184:187],v[220:223]\nxC v27,v30,v31\nds_write_b128 v234, v[24:27] offset:12288\ns_branch SKIP_%=\nLB6_1_%=:\ns_call_b64 s[62:63], RENF1_%=\n"
"s_branch RET6_1_%=\nLB6_0_%=:\ns_call_b64 s[62:63], RENF0_%=\ns_branch RET6_0_%=\nRENF0_%=:\ns_nop 7\n"
"v_log_f32 v237, v208\nv_cmp_gt_f32 vcc, 0x71800000, v208\nv_add_f32 v237, s41, v237\nv_max_f32 v237, 0, v237\nv_min_f32 v237, 0x42f00000, v237\nxE v238,-v237\n"
"v_mov_b32 v239, 0x7f800000\nv_cndmask_b32 v238, v239, v238, vcc\nv_sub_f32 v192, v192, v237\nv_sub_f32 v193, v193, v237\nv_sub_f32 v194, v194, v237\nv_sub_f32 v195, v195, v237\n"
"v_pk_mul_f32 v[0:1], v[0:1], v[238:239] op_sel_hi:[1,0]\nv_pk_mul_f32 v[2:3], v[2:3], v[238:239] op_sel_hi:[1,0]\nv_pk_mul_f32 v[4:5], v[4:5], v[238:239] op_sel_hi:[1,0]\nv_pk_mul_f32 v[6:7], v[6:7], v[238:239] op_sel_hi:[1,0]\nv_pk_mul_f32 v[8:9], v[8:9], v[238:239] op_sel_hi:[1,0]\nv_pk_mul_f32 v[10:11], v[10:11], v[238:239] op_sel_hi:[1,0]\n"
"v_pk_mul_f32 v[12:13], v[12:13], v[238:239] op_sel_hi:[1,0]\nv_pk_mul_f32 v[14:15], v[14:15], v[238:239] op_sel_hi:[1,0]\nv_pk_mul_f32 v[16:17], v[16:17], v[238:239] op_sel_hi:[1,0]\nv_pk_mul_f32 v[18:19], v[18:19], v[238:239] op_sel_hi:[1,0]\nv_pk_mul_f32 v[20:21], v[20:21], v[238:239] op_sel_hi:[1,0]\nv_pk_mul_f32 v[22:23], v[22:23], v[238:239] op_sel_hi:[1,0]\n"
"v_pk_mul_f32 v[24:25], v[24:25], v[238:239] op_sel_hi:[1,0]\nv_pk_mul_f32 v[26:27], v[26:27], v[238:239] op_sel_hi:[1,0]\nv_pk_mul_f32 v[28:29], v[28:29], v[238:239] op_sel_hi:[1,0]\nv_pk_mul_f32 v[30:31], v[30:31], v[238:239] op_sel_hi:[1,0]\nv_pk_mul_f32 v[208:209], v[208:209], v[238:239] op_sel_hi:[1,0]\nv_pk_mul_f32 v[210:211], v[210:211], v[238:239] op_sel_hi:[1,0]\n"
"v_log_f32 v237, v212\nv_cmp_gt_f32 vcc, 0x71800000, v212\nv_add_f32 v237, s41, v237\nv_max_f32 v237, 0, v237\nv_min_f32 v237, 0x42f00000, v237\nxE v238,-v237\n"
"v_mov_b32 v239, 0x7f800000\nv_cndmask_b32 v238, v239, v238, vcc\nv_sub_f32 v196, v196, v237\nv_sub_f32 v197, v197, v237\nv_sub_f32 v198, v198, v237\nv_sub_f32 v199, v199, v237\n"
"v_pk_mul_f32 v[32:33], v[32:33], v[238:239] op_sel_hi:[1,0]\nv_pk_mul_f32 v[34:35], v[34:35], v[238:239] op_sel_hi:[1,0]\nv_pk_mul_f32 v[36:37], v[36:37], v[238:239] op_sel_hi:[1,0]\nv_pk_mul_f32 v[38:39], v[38:39], v[238:239] op_sel_hi:[1,0]\nv_pk_mul_f32 v[40:41], v[40:41], v[238:239] op_sel_hi:[1,0]\nv_pk_mul_f32 v[42:43], v[42:43], v[238:239] op_sel_hi:[1,0]\n"
"v_pk_mul_f32 v[44:45], v[44:45], v[238:239] op_sel_hi:[1,0]\nv_pk_mul_f32 v[46:47], v[46:47], v[238:239] op_sel_hi:[1,0]\nv_pk_mul_f32 v[48:49], v[48:49], v[238:239] op_sel_hi:[1,0]\nv_pk_mul_f32 v[50:51], v[50:51], v[238:239] op_sel_hi:[1,0]\nv_pk_mul_f32 v[52:53], v[52:53], v[238:239] op_sel_hi:[1,0]\nv_pk_mul_f32 v[54:55], v[54:55], v[238:239] op_sel_hi:[1,0]\n"
"v_pk_mul_f32 v[56:57], v[56:57], v[238:239] op_sel_hi:[1,0]\nv_pk_mul_f32 v[58:59], v[58:59], v[238:239] op_sel_hi:[1,0]\nv_pk_mul_f32 v[60:61], v[60:61], v[238:239] op_sel_hi:[1,0]\nv_pk_mul_f32 v[62:63], v[62:63], v[238:239] op_sel_hi:[1,0]\nv_pk_mul_f32 v[212:213], v[212:213], v[238:239] op_sel_hi:[1,0]\nv_pk_mul_f32 v[214:215], v[214:215], v[238:239] op_sel_hi:[1,0]\n"
"s_nop 1\ns_setpc_b64 s[62:63]\nRENF1_%=:\ns_nop 7\nv_log_f32 v237, v216\nv_cmp_gt_f32 vcc, 0x71800000, v216\n"
"v_add_f32 v237, s41, v237\nv_max_f32 v237, 0, v237\nv_min_f32 v237, 0x42f00000, v237\nxE v238,-v237\nv_mov_b32 v239, 0x7f800000\nv_cndmask_b32 v238, v239, v238, vcc\n"
"v_sub_f32 v200, v200, v237\nv_sub_f32 v201, v201, v237\nv_sub_f32 v202, v202, v237\nv_sub_f32 v203, v203, v237\nv_pk_mul_f32 v[64:65], v[64:65], v[238:239] op_sel_hi:[1,0]\nv_pk_mul_f32 v[66:67], v[66:67], v[238:239] op_sel_hi:[1,0]\n"
"v_pk_mul_f32 v[68:69], v[68:69], v[238:239] op_sel_hi:[1,0]\nv_pk_mul_f32 v[70:71], v[70:71], v[238:239] op_sel_hi:[1,0]\nv_pk_mul_f32 v[72:73], v[72:73], v[238:239] op_sel_hi:[1,0]\nv_pk_mul_f32 v[74:75], v[74:75], v[238:239] op_sel_hi:[1,0]\nv_pk_mul_f32 v[76:77], v[76:77], v[238:239] op_sel_hi:[1,0]\nv_pk_mul_f32 v[78:79], v[78:79], v[238:239] op_sel_hi:[1,0]\n"
"v_pk_mul_f32 v[80:81], v[80:81], v[238:239] op_sel_hi:[1,0]\nv_pk_mul_f32 v[82:83], v[82:83], v[238:239] op_sel_hi:[1,0]\nv_pk_mul_f32 v[84:85], v[84:85], v[238:239] op_sel_hi:[1,0]\nv_pk_mul_f32 v[86:87], v[86:87], v[238:239] op_sel_hi:[1,0]\nv_pk_mul_f32 v[88:89], v[88:89], v[238:239] op_sel_hi:[1,0]\nv_pk_mul_f32 v[90:91], v[90:91], v[238:239] op_sel_hi:[1,0]\n"
"v_pk_mul_f32 v[92:93], v[92:93], v[238:239] op_sel_hi:[1,0]\nv_pk_mul_f32 v[94:95], v[94:95], v[238:239] op_sel_hi:[1,0]\nv_pk_mul_f32 v[216:217], v[216:217], v[238:239] op_sel_hi:[1,0]\nv_pk_mul_f32 v[218:219], v[218:219], v[238:239] op_sel_hi:[1,0]\nv_log_f32 v237, v220\nv_cmp_gt_f32 vcc, 0x71800000, v220\n"
"v_add_f32 v237, s41, v237\nv_max_f32 v237, 0, v237\nv_min_f32 v237, 0x42f00000, v237\nxE v238,-v237\nv_mov_b32 v239, 0x7f800000\nv_cndmask_b32 v238, v239, v238, vcc\n"
"v_sub_f32 v204, v204, v237\nv_sub_f32 v205, v205, v237\nv_sub_f32 v206, v206, v237\nv_sub_f32 v207, v207, v237\nv_pk_mul_f32 v[96:97], v[96:97], v[238:239] op_sel_hi:[1,0]\nv_pk_mul_f32 v[98:99], v[98:99], v[238:239] op_sel_hi:[1,0]\n"
"v_pk_mul_f32 v[100:101], v[100:101], v[238:239] op_sel_hi:[1,0]\nv_pk_mul_f32 v[102:103], v[102:103], v[238:239] op_sel_hi:[1,0]\nv_pk_mul_f32 v[104:105], v[104:105], v[238:239] op_sel_hi:[1,0]\nv_pk_mul_f32 v[106:107], v[106:107], v[238:239] op_sel_hi:[1,0]\nv_pk_mul_f32 v[108:109], v[108:109], v[238:239] op_sel_hi:[1,0]\nv_pk_mul_f32 v[110:111], v[110:111], v[238:239] op_sel_hi:[1,0]\n"
"v_pk_mul_f32 v[112:113], v[112:113], v[238:239] op_sel_hi:[1,0]\nv_pk_mul_f32 v[114:115], v[114:115], v[238:239] op_sel_hi:[1,0]\nv_pk_mul_f32 v[116:117], v[116:117], v[238:239] op_sel_hi:[1,0]\nv_pk_mul_f32 v[118:119], v[118:119], v[238:239] op_sel_hi:[1,0]\nv_pk_mul_f32 v[120:121], v[120:121], v[238:239] op_sel_hi:[1,0]\nv_pk_mul_f32 v[122:123], v[122:123], v[238:239] op_sel_hi:[1,0]\n"
"v_pk_mul_f32 v[124:125], v[124:125], v[238:239] op_sel_hi:[1,0]\nv_pk_mul_f32 v[126:127], v[126:127], v[238:239] op_sel_hi:[1,0]\nv_pk_mul_f32 v[220:221], v[220:221], v[238:239] op_sel_hi:[1,0]\nv_pk_mul_f32 v[222:223], v[222:223], v[238:239] op_sel_hi:[1,0]\ns_nop 1\ns_setpc_b64 s[62:63]\n"
"SKIP_%=:\ns_nop 4\nv_mov_b32 v230, 81920\ns_mov_b32 s39, 0x71800000\ns_mov_b64 s[62:63], 0\nv_cmp_ngt_f32 vcc, s39, v208\n"
"s_or_b64 s[62:63], s[62:63], vcc\nv_cmp_ngt_f32 vcc, s39, v212\ns_or_b64 s[62:63], s[62:63], vcc\nv_cmp_ngt_f32 vcc, s39, v216\ns_or_b64 s[62:63], s[62:63], vcc\nv_cmp_ngt_f32 vcc, s39, v220\n"
"TCH2_0_%=:\ns_cbranch_execz TDONE2_%=\ns_or_b64 s[62:63], s[62:63], vcc\ns_cbranch_scc0 NOBAD_%=\nds_write_b32 v230, v230\nxW lgkmcnt(0)\n"
"NOBAD_%=:\nxW vmcnt(12) lgkmcnt(0)\nv_cmp_ne_u64 vcc, s[68:69], v[246:247]\ns_and_b64 vcc, vcc, s[100:101]\ns_cmp_lg_u64 vcc, 0\ns_cbranch_scc1 SPINM2_%=\n"
"RETM2_%=:\ns_barrier\nOSTART_%=:\nds_read_b32 v235, v230\nxR a[64:67],v199 offset:0\nxR a[68:71],v199 offset:1024\n"
"xR a[72:75],v199 offset:2048\nxR a[76:79],v199 offset:3072\nxR a[80:83],v199 offset:4096\nxR a[84:87],v199 offset:5120\nxR a[88:91],v199 offset:6144\nxR a[92:95],v199 offset:7168\n"
"xR a[96:99],v199 offset:8192\nxR a[100:103],v199 offset:9216\nxR a[104:107],v199 offset:10240\nxR a[108:111],v199 offset:11264\nxR a[112:115],v199 offset:12288\nxR a[116:119],v199 offset:13312\n"
"xR a[120:123],v199 offset:14336\nxR a[124:127],v199 offset:15360\nxT v[144:145],v192 offset:0\nxT v[146:147],v192 offset:4096\nxT v[148:149],v193 offset:0\nxT v[150:151],v193 offset:4096\n"
"xT v[152:153],v192 offset:8192\nxT v[154:155],v192 offset:12288\nxT v[156:157],v193 offset:8192\nxT v[158:159],v193 offset:12288\nxT v[160:161],v192 offset:16384\nxT v[162:163],v192 offset:20480\n"
"xT v[164:165],v193 offset:16384\nxT v[166:167],v193 offset:20480\nxT v[168:169],v192 offset:24576\nxT v[170:171],v192 offset:28672\nv_rcp_f32 v227, v220\ns_nop 0\n"
"v_pk_mul_f32 v[96:97], v[96:97], v[226:227] op_sel:[0,1] op_sel_hi:[1,1]\nv_pk_mul_f32 v[98:99], v[98:99], v[226:227] op_sel:[0,1] op_sel_hi:[1,1]\nv_pk_mul_f32 v[100:101], v[100:101], v[226:227] op_sel:[0,1] op_sel_hi:[1,1]\nv_pk_mul_f32 v[102:103], v[102:103], v[226:227] op_sel:[0,1] op_sel_hi:[1,1]\nxC v96,v96,v97\nxC v97,v98,v99\n"
"xC v98,v100,v101\nxC v99,v102,v103\nxA s63,s47,16384\nxS v[96:99],v234,s[96:99],s63 offen sc1\nv_pk_mul_f32 v[104:105], v[104:105], v[226:227] op_sel:[0,1] op_sel_hi:[1,1]\nv_pk_mul_f32 v[106:107], v[106:107], v[226:227] op_sel:[0,1] op_sel_hi:[1,1]\n"
"v_pk_mul_f32 v[108:109], v[108:109], v[226:227] op_sel:[0,1] op_sel_hi:[1,1]\nv_pk_mul_f32 v[110:111], v[110:111], v[226:227] op_sel:[0,1] op_sel_hi:[1,1]\nxC v104,v104,v105\nxC v105,v106,v107\nxC v106,v108,v109\nxC v107,v110,v111\n"
"xA s63,s47,20480\nxS v[104:107],v234,s[96:99],s63 offen sc1\nv_pk_mul_f32 v[112:113], v[112:113], v[226:227] op_sel:[0,1] op_sel_hi:[1,1]\nv_pk_mul_f32 v[114:115], v[114:115], v[226:227] op_sel:[0,1] op_sel_hi:[1,1]\nv_pk_mul_f32 v[116:117], v[116:117], v[226:227] op_sel:[0,1] op_sel_hi:[1,1]\nv_pk_mul_f32 v[118:119], v[118:119], v[226:227] op_sel:[0,1] op_sel_hi:[1,1]\n"
"xC v112,v112,v113\nxC v113,v114,v115\nxC v114,v116,v117\nxC v115,v118,v119\nxA s63,s47,24576\nxS v[112:115],v234,s[96:99],s63 offen sc1\n"
"v_pk_mul_f32 v[120:121], v[120:121], v[226:227] op_sel:[0,1] op_sel_hi:[1,1]\nv_pk_mul_f32 v[122:123], v[122:123], v[226:227] op_sel:[0,1] op_sel_hi:[1,1]\nv_pk_mul_f32 v[124:125], v[124:125], v[226:227] op_sel:[0,1] op_sel_hi:[1,1]\nv_pk_mul_f32 v[126:127], v[126:127], v[226:227] op_sel:[0,1] op_sel_hi:[1,1]\nxC v120,v120,v121\nxC v121,v122,v123\n"
"xC v122,v124,v125\nxC v123,v126,v127\nxA s63,s47,28672\nxS v[120:123],v234,s[96:99],s63 offen sc1\nv_rcp_f32 v226, v216\ns_nop 0\n"
"v_pk_mul_f32 v[64:65], v[64:65], v[226:227] op_sel:[0,0] op_sel_hi:[1,0]\nv_pk_mul_f32 v[66:67], v[66:67], v[226:227] op_sel:[0,0] op_sel_hi:[1,0]\nv_pk_mul_f32 v[68:69], v[68:69], v[226:227] op_sel:[0,0] op_sel_hi:[1,0]\nv_pk_mul_f32 v[70:71], v[70:71], v[226:227] op_sel:[0,0] op_sel_hi:[1,0]\nxC v64,v64,v65\nxC v65,v66,v67\n"
"xC v66,v68,v69\nxC v67,v70,v71\nv_pk_mul_f32 v[72:73], v[72:73], v[226:227] op_sel:[0,0] op_sel_hi:[1,0]\nv_pk_mul_f32 v[74:75], v[74:75], v[226:227] op_sel:[0,0] op_sel_hi:[1,0]\nv_pk_mul_f32 v[76:77], v[76:77], v[226:227] op_sel:[0,0] op_sel_hi:[1,0]\nv_pk_mul_f32 v[78:79], v[78:79], v[226:227] op_sel:[0,0] op_sel_hi:[1,0]\n"
"xC v72,v72,v73\nxC v73,v74,v75\nxC v74,v76,v77\nxC v75,v78,v79\nv_pk_mul_f32 v[80:81], v[80:81], v[226:227] op_sel:[0,0] op_sel_hi:[1,0]\nv_pk_mul_f32 v[82:83], v[82:83], v[226:227] op_sel:[0,0] op_sel_hi:[1,0]\n"
"v_pk_mul_f32 v[84:85], v[84:85], v[226:227] op_sel:[0,0] op_sel_hi:[1,0]\nv_pk_mul_f32 v[86:87], v[86:87], v[226:227] op_sel:[0,0] op_sel_hi:[1,0]\nxC v80,v80,v81\nxC v81,v82,v83\nxC v82,v84,v85\nxC v83,v86,v87\n"
"v_pk_mul_f32 v[88:89], v[88:89], v[226:227] op_sel:[0,0] op_sel_hi:[1,0]\nv_pk_mul_f32 v[90:91], v[90:91], v[226:227] op_sel:[0,0] op_sel_hi:[1,0]\nv_pk_mul_f32 v[92:93], v[92:93], v[226:227] op_sel:[0,0] op_sel_hi:[1,0]\nv_pk_mul_f32 v[94:95], v[94:95], v[226:227] op_sel:[0,0] op_sel_hi:[1,0]\nxC v88,v88,v89\nxC v89,v90,v91\n"
"xC v90,v92,v93\nxC v91,v94,v95\nxW lgkmcnt(15)\nv_cmp_ne_u32 vcc, 0, v235\ns_cbranch_vccnz REDO_%=\nREJOIN_%=:\n"
"s_barrier\nxW lgkmcnt(12)\nxM v[0:3],v[144:147],a[64:67],0\nds_write_b128 v234, v[64:67] offset:16384\n"
"xM v[8:11],v[144:147],a[68:71],0\nv_add_u32 v208, 896, v248\nv_add_u32 v209, 0, v248\n"
"xM v[16:19],v[144:147],a[72:75],0\nds_write_b128 v234, v[72:75] offset:20480\nxM v[24:27],v[144:147],a[76:79],0\nxW lgkmcnt(12)\nv_add_u32 v210, 8064, v248\nv_add_u32 v211, 7168, v248\n"
"xM v[4:7],v[148:151],a[64:67],0\nds_write_b128 v234, v[80:83] offset:24576\nxM v[12:15],v[148:151],a[68:71],0\nv_mov_b32 v245, v234\nv_accvgpr_read_b32 v200, a196\n"
"xM v[20:23],v[148:151],a[72:75],0\nds_write_b128 v234, v[88:91] offset:28672\nxM v[28:31],v[148:151],a[76:79],0\nxW lgkmcnt(4)\nv_accvgpr_read_b32 v201, a197\nv_accvgpr_read_b32 v202, a198\n"
"xM v[0:3],v[152:155],a[80:83],v[0:3]\nv_accvgpr_read_b32 v203, a199\nv_accvgpr_read_b32 v204, a200\n"
"xM v[8:11],v[152:155],a[84:87],v[8:11]\nv_accvgpr_read_b32 v205, a201\nv_accvgpr_read_b32 v206, a202\n"
"xM v[16:19],v[152:155],a[88:91],v[16:19]\nxW vmcnt(63) lgkmcnt(0)\ns_barrier\n"
"xM v[24:27],v[152:155],a[92:95],v[24:27]\nxT v[172:173],v193 offset:24576\nxR a[0:3],v199 offset:16384\n"
"xM v[4:7],v[156:159],a[80:83],v[4:7]\nxT v[174:175],v193 offset:28672\nxR a[4:7],v199 offset:17408\n"
"xM v[12:15],v[156:159],a[84:87],v[12:15]\nxR a[8:11],v199 offset:18432\nv_accvgpr_read_b32 v207, a203\n"
"xM v[20:23],v[156:159],a[88:91],v[20:23]\nxR a[12:15],v199 offset:19456\nv_accvgpr_read_b32 v184, a208\n"
"xM v[28:31],v[156:159],a[92:95],v[28:31]\nxR a[16:19],v199 offset:20480\nv_accvgpr_read_b32 v185, a209\n"
"xM v[0:3],v[160:163],a[96:99],v[0:3]\nxR a[20:23],v199 offset:21504\nv_accvgpr_read_b32 v186, a210\n"
"xM v[8:11],v[160:163],a[100:103],v[8:11]\nxR a[24:27],v199 offset:22528\nv_accvgpr_read_b32 v187, a211\n"
"xM v[16:19],v[160:163],a[104:107],v[16:19]\nxR a[28:31],v199 offset:23552\nv_accvgpr_read_b32 v188, a212\n"
"xM v[24:27],v[160:163],a[108:111],v[24:27]\nxR a[32:35],v199 offset:24576\nv_accvgpr_read_b32 v189, a213\n"
"xM v[4:7],v[164:167],a[96:99],v[4:7]\nxW vmcnt(8) lgkmcnt(8)\ns_barrier\n"
"xM v[12:15],v[164:167],a[100:103],v[12:15]\nxA m0,s44,0\nxT v[176:177],v192 offset:32768\nxT v[178:179],v192 offset:36864\n"
"xM v[20:23],v[164:167],a[104:107],v[20:23]\nxA s39,s42,512\nxT v[180:181],v193 offset:32768\nxT v[182:183],v193 offset:36864\n"
"xM v[28:31],v[164:167],a[108:111],v[28:31]\nxL v208,s[24:27],s39 offen offset:0 lds\nxM v[0:3],v[168:171],a[112:115],v[0:3]\nxT v[144:145],v192 offset:40960\nxT v[146:147],v192 offset:45056\n"
"xM v[8:11],v[168:171],a[116:119],v[8:11]\nxR a[36:39],v199 offset:25600\nv_accvgpr_read_b32 v190, a214\n"
"xM v[16:19],v[168:171],a[120:123],v[16:19]\nxL v209,s[24:27],s39 offen offset:1024 lds\nxM v[24:27],v[168:171],a[124:127],v[24:27]\nxT v[148:149],v193 offset:40960\nxR a[40:43],v199 offset:26624\n"
"xM v[4:7],v[172:175],a[112:115],v[4:7]\nxT v[150:151],v193 offset:45056\nxR a[44:47],v199 offset:27648\n"
"xM v[12:15],v[172:175],a[116:119],v[12:15]\nxL v210,s[24:27],s39 offen offset:2048 lds\nxM v[20:23],v[172:175],a[120:123],v[20:23]\nxT v[152:153],v192 offset:49152\nxR a[48:51],v199 offset:28672\n"
"xM v[28:31],v[172:175],a[124:127],v[28:31]\nxW lgkmcnt(9)\nxR a[52:55],v199 offset:29696\nv_accvgpr_read_b32 v191, a215\n"
"xM v[0:3],v[176:179],a[0:3],v[0:3]\nxL v211,s[24:27],s39 offen offset:3072 lds\nxM v[8:11],v[176:179],a[4:7],v[8:11]\nxA m0,s44,16384\nxT v[154:155],v192 offset:53248\nxR a[56:59],v199 offset:30720\n"
"xM v[16:19],v[176:179],a[8:11],v[16:19]\nxA s39,s42,74240\nxT v[156:157],v193 offset:49152\nxR a[60:63],v199 offset:31744\n"
"xM v[24:27],v[176:179],a[12:15],v[24:27]\nxL v208,s[24:27],s39 offen offset:0 lds\nxM v[4:7],v[180:183],a[0:3],v[4:7]\nxT v[158:159],v193 offset:53248\nv_accvgpr_read_b32 v246, a216\n"
"xM v[12:15],v[180:183],a[4:7],v[12:15]\nv_accvgpr_read_b32 v247, a217\nv_lshrrev_b32 v230, 4, v255\n"
"xM v[20:23],v[180:183],a[8:11],v[20:23]\nxL v209,s[24:27],s39 offen offset:1024 lds\nxM v[28:31],v[180:183],a[12:15],v[28:31]\nxW lgkmcnt(5)\nxT v[160:161],v192 offset:57344\nv_and_b32 v231, 15, v255\n"
"xM v[0:3],v[144:147],a[16:19],v[0:3]\nxT v[162:163],v192 offset:61440\nv_bfe_u32 v232, v255, 2, 2\n"
"xM v[8:11],v[144:147],a[20:23],v[8:11]\nxL v210,s[24:27],s39 offen offset:2048 lds\nxM v[16:19],v[144:147],a[24:27],v[16:19]\nxT v[164:165],v193 offset:57344\nv_and_b32 v233, 3, v255\n"
"xM v[24:27],v[144:147],a[28:31],v[24:27]\nv_bfe_u32 v234, v255, 2, 3\nv_lshlrev_b32 v224, 4, v255\n"
"xM v[4:7],v[148:151],a[16:19],v[4:7]\nxL v211,s[24:27],s39 offen offset:3072 lds\nxM v[12:15],v[148:151],a[20:23],v[12:15]\nxT v[166:167],v193 offset:61440\nv_lshrrev_b32 v235, 2, v234\n"
"xM v[20:23],v[148:151],a[24:27],v[20:23]\nv_lshlrev_b32 v235, 1, v235\nv_xor_b32 v235, v235, v233\n"
"xM v[28:31],v[148:151],a[28:31],v[28:31]\nxW lgkmcnt(3)\nv_lshlrev_b32 v235, 4, v235\nv_lshrrev_b32 v237, 2, v255\n"
"xM v[0:3],v[152:155],a[32:35],v[0:3]\nv_mul_u32_u24 v218, 9216, v237\nv_add_u32 v218, v218, v235\n"
"xM v[8:11],v[152:155],a[36:39],v[8:11]\nv_lshrrev_b32 v237, 5, v255\nv_lshl_add_u32 v235, v237, 6, v235\n"
"xM v[16:19],v[152:155],a[40:43],v[16:19]\nv_mul_u32_u24 v217, 9216, v234\nv_add_u32 v217, v217, v235\n"
"xM v[24:27],v[152:155],a[44:47],v[24:27]\nv_mul_u32_u24 v219, 1152, v234\nv_add_u32 v219, v219, v235\n"
"xM v[4:7],v[156:159],a[32:35],v[4:7]\nxW vmcnt(38) lgkmcnt(0)\ns_barrier\n"
"xM v[12:15],v[156:159],a[36:39],v[12:15]\nxA m0,s44,32768\nxT v[168:169],v194 offset:32768\nxT v[170:171],v194 offset:36864\n"
"xM v[20:23],v[156:159],a[40:43],v[20:23]\nxA s39,s42,147968\nxT v[172:173],v195 offset:32768\nxT v[174:175],v195 offset:36864\n"
"xM v[28:31],v[156:159],a[44:47],v[28:31]\nxL v208,s[24:27],s39 offen offset:0 lds\nxM v[0:3],v[160:163],a[48:51],v[0:3]\nxT v[176:177],v194 offset:40960\nxT v[178:179],v194 offset:45056\n"
"xM v[8:11],v[160:163],a[52:55],v[8:11]\nxT v[180:181],v195 offset:40960\nxT v[182:183],v195 offset:45056\n"
"xM v[16:19],v[160:163],a[56:59],v[16:19]\nxL v209,s[24:27],s39 offen offset:1024 lds\nxM v[24:27],v[160:163],a[60:63],v[24:27]\nxT v[144:145],v194 offset:49152\nxT v[146:147],v194 offset:53248\n"
"xM v[4:7],v[164:167],a[48:51],v[4:7]\nxT v[148:149],v195 offset:49152\nxM v[12:15],v[164:167],a[52:55],v[12:15]\nxL v210,s[24:27],s39 offen offset:2048 lds\n"
"xM v[20:23],v[164:167],a[56:59],v[20:23]\nxT v[150:151],v195 offset:53248\nxM v[28:31],v[164:167],a[60:63],v[28:31]\nxW lgkmcnt(8)\n"
"xM v[32:35],v[168:171],a[64:67],0\nxL v211,s[24:27],s39 offen offset:3072 lds\nxM v[40:43],v[168:171],a[68:71],0\nxA m0,s44,49152\nxT v[152:153],v194 offset:57344\n"
"xM v[48:51],v[168:171],a[72:75],0\nxA s39,s42,221696\nxT v[154:155],v194 offset:61440\n"
"xM v[56:59],v[168:171],a[76:79],0\nxL v208,s[24:27],s39 offen offset:0 lds\nxM v[36:39],v[172:175],a[64:67],0\nxT v[156:157],v195 offset:57344\n"
"xM v[44:47],v[172:175],a[68:71],0\nxM v[52:55],v[172:175],a[72:75],0\nxW vmcnt(13)\ns_mov_b64 exec, 1\nbuffer_store_dwordx2 v[242:243], off, s[96:99], s60 sc1\ns_mov_b64 exec, -1\n"
"xM v[60:63],v[172:175],a[76:79],0\nxW lgkmcnt(3)\nxL v209,s[24:27],s39 offen offset:1024 lds\n"
"xM v[32:35],v[176:179],a[80:83],v[32:35]\nxT v[158:159],v195 offset:61440\nxM v[40:43],v[176:179],a[84:87],v[40:43]\n"
"xM v[48:51],v[176:179],a[88:91],v[48:51]\nxL v210,s[24:27],s39 offen offset:2048 lds\nxM v[56:59],v[176:179],a[92:95],v[56:59]\n"
"xM v[36:39],v[180:183],a[80:83],v[36:39]\nxM v[44:47],v[180:183],a[84:87],v[44:47]\nxL v211,s[24:27],s39 offen offset:3072 lds\n"
"xM v[52:55],v[180:183],a[88:91],v[52:55]\nxM v[60:63],v[180:183],a[92:95],v[60:63]\nxM v[32:35],v[144:147],a[96:99],v[32:35]\n"
"xM v[40:43],v[144:147],a[100:103],v[40:43]\nxM v[48:51],v[144:147],a[104:107],v[48:51]\nxM v[56:59],v[144:147],a[108:111],v[56:59]\n"
"xM v[36:39],v[148:151],a[96:99],v[36:39]\nxW vmcnt(45) lgkmcnt(0)\ns_barrier\n"
"xM v[44:47],v[148:151],a[100:103],v[44:47]\nxA m0,s44,98304\nxT v[160:161],v196 offset:0\nxT v[162:163],v196 offset:4096\n"
"xM v[52:55],v[148:151],a[104:107],v[52:55]\nxA s39,s42,768\nxT v[164:165],v197 offset:0\nxT v[166:167],v197 offset:4096\n"
"xM v[60:63],v[148:151],a[108:111],v[60:63]\nxL v208,s[24:27],s39 offen offset:0 lds\nxM v[32:35],v[152:155],a[112:115],v[32:35]\nxT v[168:169],v196 offset:8192\nxT v[170:171],v196 offset:12288\n"
"xM v[40:43],v[152:155],a[116:119],v[40:43]\nxT v[172:173],v197 offset:8192\nxT v[174:175],v197 offset:12288\n"
"xM v[48:51],v[152:155],a[120:123],v[48:51]\nxL v209,s[24:27],s39 offen offset:1024 lds\nxM v[56:59],v[152:155],a[124:127],v[56:59]\nxT v[176:177],v196 offset:16384\nxT v[178:179],v196 offset:20480\n"
"xM v[36:39],v[156:159],a[112:115],v[36:39]\nxT v[180:181],v197 offset:16384\nxM v[44:47],v[156:159],a[116:119],v[44:47]\nxL v210,s[24:27],s39 offen offset:2048 lds\n"
"xM v[52:55],v[156:159],a[120:123],v[52:55]\nxT v[182:183],v197 offset:20480\nxM v[60:63],v[156:159],a[124:127],v[60:63]\nxW lgkmcnt(8)\n"
"xM v[32:35],v[160:163],a[0:3],v[32:35]\nxL v211,s[24:27],s39 offen offset:3072 lds\nxM v[40:43],v[160:163],a[4:7],v[40:43]\nxA m0,s44,114688\nxT v[144:145],v196 offset:24576\n"
"xM v[48:51],v[160:163],a[8:11],v[48:51]\nxA s39,s42,74496\nxT v[146:147],v196 offset:28672\n"
"xM v[56:59],v[160:163],a[12:15],v[56:59]\nxL v208,s[24:27],s39 offen offset:0 lds\nxM v[36:39],v[164:167],a[0:3],v[36:39]\nxT v[148:149],v197 offset:24576\n"
"xM v[44:47],v[164:167],a[4:7],v[44:47]\nxM v[52:55],v[164:167],a[8:11],v[52:55]\nxL v209,s[24:27],s39 offen offset:1024 lds\n"
"xM v[60:63],v[164:167],a[12:15],v[60:63]\nxW lgkmcnt(3)\nxT v[150:151],v197 offset:28672\n"
"xM v[32:35],v[168:171],a[16:19],v[32:35]\nxM v[40:43],v[168:171],a[20:23],v[40:43]\nxL v210,s[24:27],s39 offen offset:2048 lds\n"
"xM v[48:51],v[168:171],a[24:27],v[48:51]\nxM v[56:59],v[168:171],a[28:31],v[56:59]\nxM v[36:39],v[172:175],a[16:19],v[36:39]\nxL v211,s[24:27],s39 offen offset:3072 lds\n"
"xM v[44:47],v[172:175],a[20:23],v[44:47]\nxM v[52:55],v[172:175],a[24:27],v[52:55]\nxM v[60:63],v[172:175],a[28:31],v[60:63]\n"
"xM v[32:35],v[176:179],a[32:35],v[32:35]\nxM v[40:43],v[176:179],a[36:39],v[40:43]\nxM v[48:51],v[176:179],a[40:43],v[48:51]\n"
"xM v[56:59],v[176:179],a[44:47],v[56:59]\nxM v[36:39],v[180:183],a[32:35],v[36:39]\nxW vmcnt(17) lgkmcnt(0)\ns_barrier\n"
"xM v[44:47],v[180:183],a[36:39],v[44:47]\nxA m0,s44,131072\nxT v[152:153],v192 offset:0\nxT v[154:155],v192 offset:4096\n"
"xM v[52:55],v[180:183],a[40:43],v[52:55]\nxA s39,s42,148224\nxT v[156:157],v193 offset:0\nxT v[158:159],v193 offset:4096\n"
"xM v[60:63],v[180:183],a[44:47],v[60:63]\nxL v208,s[24:27],s39 offen offset:0 lds\nxM v[32:35],v[144:147],a[48:51],v[32:35]\nxT v[160:161],v192 offset:8192\nxT v[162:163],v192 offset:12288\n"
"xM v[40:43],v[144:147],a[52:55],v[40:43]\nxT v[164:165],v193 offset:8192\nxT v[166:167],v193 offset:12288\n"
"xM v[48:51],v[144:147],a[56:59],v[48:51]\nxL v209,s[24:27],s39 offen offset:1024 lds\nxM v[56:59],v[144:147],a[60:63],v[56:59]\nxT v[168:169],v192 offset:16384\nxT v[170:171],v192 offset:20480\n"
"xM v[36:39],v[148:151],a[48:51],v[36:39]\nxT v[172:173],v193 offset:16384\nxM v[44:47],v[148:151],a[52:55],v[44:47]\nxL v210,s[24:27],s39 offen offset:2048 lds\n"
"xM v[52:55],v[148:151],a[56:59],v[52:55]\nxT v[174:175],v193 offset:20480\nxM v[60:63],v[148:151],a[60:63],v[60:63]\nxW lgkmcnt(8)\n"
"xM v[64:67],v[152:155],a[64:67],0\nxL v211,s[24:27],s39 offen offset:3072 lds\nxM v[72:75],v[152:155],a[68:71],0\nxA m0,s44,147456\nxT v[176:177],v192 offset:24576\n"
"xM v[80:83],v[152:155],a[72:75],0\nxA s39,s42,221952\nxT v[178:179],v192 offset:28672\n"
"xM v[88:91],v[152:155],a[76:79],0\nxL v208,s[24:27],s39 offen offset:0 lds\nxM v[68:71],v[156:159],a[64:67],0\nxT v[180:181],v193 offset:24576\n"
"xM v[76:79],v[156:159],a[68:71],0\nxM v[84:87],v[156:159],a[72:75],0\nxL v209,s[24:27],s39 offen offset:1024 lds\n"
"xM v[92:95],v[156:159],a[76:79],0\nxW lgkmcnt(3)\nxT v[182:183],v193 offset:28672\n"
"xM v[64:67],v[160:163],a[80:83],v[64:67]\nxM v[72:75],v[160:163],a[84:87],v[72:75]\nxL v210,s[24:27],s39 offen offset:2048 lds\n"
"xM v[80:83],v[160:163],a[88:91],v[80:83]\nxM v[88:91],v[160:163],a[92:95],v[88:91]\nxM v[68:71],v[164:167],a[80:83],v[68:71]\nbuffer_load_dwordx2 v[236:237], v238, s[96:99], s61 offen sc1\n"
"xM v[76:79],v[164:167],a[84:87],v[76:79]\nxL v211,s[24:27],s39 offen offset:3072 lds\nxM v[84:87],v[164:167],a[88:91],v[84:87]\n"
"xM v[92:95],v[164:167],a[92:95],v[92:95]\nxM v[64:67],v[168:171],a[96:99],v[64:67]\nxM v[72:75],v[168:171],a[100:103],v[72:75]\n"
"xM v[80:83],v[168:171],a[104:107],v[80:83]\nxM v[88:91],v[168:171],a[108:111],v[88:91]\nxM v[68:71],v[172:175],a[96:99],v[68:71]\nxW vmcnt(17) lgkmcnt(0)\ns_barrier\n"
"xM v[76:79],v[172:175],a[100:103],v[76:79]\nxA m0,s44,0\nxT v[144:145],v192 offset:32768\nxT v[146:147],v192 offset:36864\n"
"xM v[84:87],v[172:175],a[104:107],v[84:87]\nxA s39,s42,1920\nxT v[148:149],v193 offset:32768\nxT v[150:151],v193 offset:36864\n"
"xM v[92:95],v[172:175],a[108:111],v[92:95]\nxL v244,s[24:27],s39 offen lds\nxM v[64:67],v[176:179],a[112:115],v[64:67]\nxA m0,s44,1024\nxT v[152:153],v192 offset:40960\nxT v[154:155],v192 offset:45056\n"
"xM v[72:75],v[176:179],a[116:119],v[72:75]\nxA s39,s42,11136\nxT v[156:157],v193 offset:40960\nxT v[158:159],v193 offset:45056\n"
"xM v[80:83],v[176:179],a[120:123],v[80:83]\nxL v244,s[24:27],s39 offen lds\nxM v[88:91],v[176:179],a[124:127],v[88:91]\nxA m0,s44,2048\nxT v[160:161],v192 offset:49152\nxT v[162:163],v192 offset:53248\n"
"xM v[68:71],v[180:183],a[112:115],v[68:71]\nxA s39,s42,20352\nxT v[164:165],v193 offset:49152\n"
"xM v[76:79],v[180:183],a[116:119],v[76:79]\nxL v244,s[24:27],s39 offen lds\nxM v[84:87],v[180:183],a[120:123],v[84:87]\nxA m0,s44,3072\nxT v[166:167],v193 offset:53248\n"
"xM v[92:95],v[180:183],a[124:127],v[92:95]\nxA s39,s42,29568\nxW lgkmcnt(10)\n"
"xM v[64:67],v[144:147],a[0:3],v[64:67]\nxL v244,s[24:27],s39 offen lds\nxM v[72:75],v[144:147],a[4:7],v[72:75]\nxA m0,s44,16384\nxT v[168:169],v192 offset:57344\n"
"xM v[80:83],v[144:147],a[8:11],v[80:83]\nxA s39,s42,149376\nxT v[170:171],v192 offset:61440\n"
"xM v[88:91],v[144:147],a[12:15],v[88:91]\nxW lgkmcnt(4)\nxL v244,s[24:27],s39 offen lds\n"
"xM v[68:71],v[148:151],a[0:3],v[68:71]\nxA m0,s44,17408\nxT v[172:173],v193 offset:57344\n"
"xM v[76:79],v[148:151],a[4:7],v[76:79]\nxA s39,s42,158592\nxM v[84:87],v[148:151],a[8:11],v[84:87]\nxL v244,s[24:27],s39 offen lds\n"
"xM v[92:95],v[148:151],a[12:15],v[92:95]\nxA m0,s44,18432\nxT v[174:175],v193 offset:61440\n"
"xM v[64:67],v[152:155],a[16:19],v[64:67]\nxA s39,s42,167808\nxM v[72:75],v[152:155],a[20:23],v[72:75]\nxL v244,s[24:27],s39 offen lds\n"
"xM v[80:83],v[152:155],a[24:27],v[80:83]\nxA m0,s44,19456\nxM v[88:91],v[152:155],a[28:31],v[88:91]\nxA s39,s42,177024\n"
"xM v[68:71],v[156:159],a[16:19],v[68:71]\nxL v244,s[24:27],s39 offen lds\nxM v[76:79],v[156:159],a[20:23],v[76:79]\n"
"xM v[84:87],v[156:159],a[24:27],v[84:87]\nTCH2_1_%=:\ns_cbranch_execz TDONE2_%=\n"
"xM v[92:95],v[156:159],a[28:31],v[92:95]\nxM v[64:67],v[160:163],a[32:35],v[64:67]\nxM v[72:75],v[160:163],a[36:39],v[72:75]\n"
"xM v[80:83],v[160:163],a[40:43],v[80:83]\nxM v[88:91],v[160:163],a[44:47],v[88:91]\nxW lgkmcnt(0)\n"
"xM v[68:71],v[164:167],a[32:35],v[68:71]\nxW vmcnt(9) lgkmcnt(0)\nv_cmp_ne_u64 vcc, s[68:69], v[236:237]\ns_and_b64 vcc, vcc, s[100:101]\ns_and_b32 s38, vcc_lo, 61440\ns_cbranch_scc1 SPINZ_%=\n"
"RETZ_%=:\ns_barrier\nxM v[76:79],v[164:167],a[36:39],v[76:79]\nxA m0,s34,65536\nxT v[176:177],v194 offset:32768\nxT v[178:179],v194 offset:36864\n"
"xM v[84:87],v[164:167],a[40:43],v[84:87]\nxA s39,s48,0\nxT v[180:181],v195 offset:32768\nxT v[182:183],v195 offset:36864\n"
"xM v[92:95],v[164:167],a[44:47],v[92:95]\nxL v245,s[96:99],s39 offen sc1 lds\nxM v[64:67],v[168:171],a[48:51],v[64:67]\nxA m0,s34,69632\nxT v[144:145],v194 offset:40960\nxT v[146:147],v194 offset:45056\n"
"xM v[72:75],v[168:171],a[52:55],v[72:75]\nxA s39,s48,4096\nxT v[148:149],v195 offset:40960\nxT v[150:151],v195 offset:45056\n"
"xM v[80:83],v[168:171],a[56:59],v[80:83]\nxL v245,s[96:99],s39 offen sc1 lds\nxM v[88:91],v[168:171],a[60:63],v[88:91]\nxA m0,s34,73728\nxT v[152:153],v194 offset:49152\nxT v[154:155],v194 offset:53248\n"
"xM v[68:71],v[172:175],a[48:51],v[68:71]\nxA s39,s48,8192\nxT v[156:157],v195 offset:49152\n"
"xM v[76:79],v[172:175],a[52:55],v[76:79]\nxL v245,s[96:99],s39 offen sc1 lds\nxM v[84:87],v[172:175],a[56:59],v[84:87]\nxA m0,s34,77824\nxT v[158:159],v195 offset:53248\n"
"xM v[92:95],v[172:175],a[60:63],v[92:95]\nxA s39,s48,12288\nxW lgkmcnt(10)\n"
"xM v[96:99],v[176:179],a[64:67],0\nxL v245,s[96:99],s39 offen sc1 lds\nxM v[104:107],v[176:179],a[68:71],0\nxA m0,s34,81920\nxT v[160:161],v194 offset:57344\n"
"xM v[112:115],v[176:179],a[72:75],0\nxA s39,s48,16384\nxT v[162:163],v194 offset:61440\n"
"xM v[120:123],v[176:179],a[76:79],0\nxW lgkmcnt(4)\nxL v245,s[96:99],s39 offen sc1 lds\n"
"xM v[100:103],v[180:183],a[64:67],0\nxA m0,s34,86016\nxT v[164:165],v195 offset:57344\n"
"xM v[108:111],v[180:183],a[68:71],0\nxA s39,s48,20480\nxM v[116:119],v[180:183],a[72:75],0\nxL v245,s[96:99],s39 offen sc1 lds\n"
"xM v[124:127],v[180:183],a[76:79],0\nxA m0,s34,90112\nxT v[166:167],v195 offset:61440\n"
"xM v[96:99],v[144:147],a[80:83],v[96:99]\nxA s39,s48,24576\nxM v[104:107],v[144:147],a[84:87],v[104:107]\nxL v245,s[96:99],s39 offen sc1 lds\n"
"xM v[112:115],v[144:147],a[88:91],v[112:115]\nxA m0,s34,94208\nxM v[120:123],v[144:147],a[92:95],v[120:123]\nxA s39,s48,28672\n"
"xM v[100:103],v[148:151],a[80:83],v[100:103]\nxL v245,s[96:99],s39 offen sc1 lds\nxM v[108:111],v[148:151],a[84:87],v[108:111]\nxA m0,s44,32768\n"
"xM v[116:119],v[148:151],a[88:91],v[116:119]\nxA s39,s45,0\nxM v[124:127],v[148:151],a[92:95],v[124:127]\nxL v208,s[24:27],s39 offen offset:0 lds\n"
"xM v[96:99],v[152:155],a[96:99],v[96:99]\nxM v[104:107],v[152:155],a[100:103],v[104:107]\nxM v[112:115],v[152:155],a[104:107],v[112:115]\nxL v209,s[24:27],s39 offen offset:1024 lds\n"
"xM v[120:123],v[152:155],a[108:111],v[120:123]\nxW lgkmcnt(0)\nxM v[100:103],v[156:159],a[96:99],v[100:103]\nxW vmcnt(18) lgkmcnt(0)\ns_barrier\n"
"xM v[108:111],v[156:159],a[100:103],v[108:111]\nxL v210,s[24:27],s39 offen offset:2048 lds\nxM v[116:119],v[156:159],a[104:107],v[116:119]\nxT v[168:169],v196 offset:0\nxT v[170:171],v196 offset:4096\n"
"xM v[124:127],v[156:159],a[108:111],v[124:127]\nxT v[172:173],v197 offset:0\nxT v[174:175],v197 offset:4096\n"
"xM v[96:99],v[160:163],a[112:115],v[96:99]\nxL v211,s[24:27],s39 offen offset:3072 lds\nxM v[104:107],v[160:163],a[116:119],v[104:107]\nxA m0,s44,49152\nxT v[176:177],v196 offset:8192\nxT v[178:179],v196 offset:12288\n"
"xM v[112:115],v[160:163],a[120:123],v[112:115]\nxA s39,s45,73728\nxT v[180:181],v197 offset:8192\nxT v[182:183],v197 offset:12288\n"
"xM v[120:123],v[160:163],a[124:127],v[120:123]\nxL v208,s[24:27],s39 offen offset:0 lds\nxM v[100:103],v[164:167],a[112:115],v[100:103]\nxT v[144:145],v196 offset:16384\nxT v[146:147],v196 offset:20480\n"
"xM v[108:111],v[164:167],a[116:119],v[108:111]\nxT v[148:149],v197 offset:16384\nxM v[116:119],v[164:167],a[120:123],v[116:119]\nxL v209,s[24:27],s39 offen offset:1024 lds\n"
"xM v[124:127],v[164:167],a[124:127],v[124:127]\nxW lgkmcnt(7)\nxT v[150:151],v197 offset:20480\n"
"xM v[96:99],v[168:171],a[0:3],v[96:99]\nxT v[152:153],v196 offset:24576\nxM v[104:107],v[168:171],a[4:7],v[104:107]\nxL v210,s[24:27],s39 offen offset:2048 lds\n"
"xM v[112:115],v[168:171],a[8:11],v[112:115]\nxT v[154:155],v196 offset:28672\nxM v[120:123],v[168:171],a[12:15],v[120:123]\n"
"xM v[100:103],v[172:175],a[0:3],v[100:103]\nxL v211,s[24:27],s39 offen offset:3072 lds\nxM v[108:111],v[172:175],a[4:7],v[108:111]\nxA m0,s44,98304\nxT v[156:157],v197 offset:24576\n"
"xM v[116:119],v[172:175],a[8:11],v[116:119]\nxA s39,s45,147456\nxT v[158:159],v197 offset:28672\n"
"xM v[124:127],v[172:175],a[12:15],v[124:127]\nxW lgkmcnt(4)\nxL v208,s[24:27],s39 offen offset:0 lds\n"
"xM v[96:99],v[176:179],a[16:19],v[96:99]\nxM v[104:107],v[176:179],a[20:23],v[104:107]\nxM v[112:115],v[176:179],a[24:27],v[112:115]\nxL v209,s[24:27],s39 offen offset:1024 lds\n"
"xM v[120:123],v[176:179],a[28:31],v[120:123]\nxM v[100:103],v[180:183],a[16:19],v[100:103]\nxM v[108:111],v[180:183],a[20:23],v[108:111]\nxL v210,s[24:27],s39 offen offset:2048 lds\n"
"xM v[116:119],v[180:183],a[24:27],v[116:119]\nxM v[124:127],v[180:183],a[28:31],v[124:127]\nxM v[96:99],v[144:147],a[32:35],v[96:99]\nxL v211,s[24:27],s39 offen offset:3072 lds\n"
"xM v[104:107],v[144:147],a[36:39],v[104:107]\nxA m0,s44,114688\nxM v[112:115],v[144:147],a[40:43],v[112:115]\nxA s39,s45,221184\n"
"xM v[120:123],v[144:147],a[44:47],v[120:123]\nxL v208,s[24:27],s39 offen offset:0 lds\nxM v[100:103],v[148:151],a[32:35],v[100:103]\nxW vmcnt(21) lgkmcnt(0)\ns_barrier\n"
"xM v[108:111],v[148:151],a[36:39],v[108:111]\nxT v[160:161],v198 offset:0\nxT v[162:163],v198 offset:2048\n"
"xM v[116:119],v[148:151],a[40:43],v[116:119]\nxL v209,s[24:27],s39 offen offset:1024 lds\nxM v[124:127],v[148:151],a[44:47],v[124:127]\nxT v[164:165],v198 offset:4096\nxT v[166:167],v198 offset:6144\n"
"xM v[96:99],v[152:155],a[48:51],v[96:99]\nxT v[168:169],v198 offset:8192\nxT v[170:171],v198 offset:10240\n"
"xM v[104:107],v[152:155],a[52:55],v[104:107]\nxL v210,s[24:27],s39 offen offset:2048 lds\nxM v[112:115],v[152:155],a[56:59],v[112:115]\nxT v[172:173],v198 offset:12288\nxT v[174:175],v198 offset:14336\n"
"xM v[120:123],v[152:155],a[60:63],v[120:123]\nxT v[176:177],v198 offset:16384\nxT v[178:179],v198 offset:18432\n"
"xM v[100:103],v[156:159],a[48:51],v[100:103]\nxL v211,s[24:27],s39 offen offset:3072 lds\nxM v[108:111],v[156:159],a[52:55],v[108:111]\nxA m0,s44,131072\nxT v[180:181],v198 offset:20480\n"
"xM v[116:119],v[156:159],a[56:59],v[116:119]\nxA s39,s45,256\nxT v[182:183],v198 offset:22528\n"
"xM v[124:127],v[156:159],a[60:63],v[124:127]\nxW lgkmcnt(8)\nxL v208,s[24:27],s39 offen offset:0 lds\n"
"xM v[128:131],v[160:163],a[64:67],0\nxT v[144:145],v198 offset:24576\nxM v[132:135],v[160:163],a[68:71],0\n"
"xM v[136:139],v[160:163],a[72:75],0\nxL v209,s[24:27],s39 offen offset:1024 lds\nxM v[140:143],v[160:163],a[76:79],0\nxT v[146:147],v198 offset:26624\n"
"xM v[128:131],v[164:167],a[80:83],v[128:131]\nxT v[148:149],v198 offset:28672\nxM v[132:135],v[164:167],a[84:87],v[132:135]\nxL v210,s[24:27],s39 offen offset:2048 lds\n"
"xM v[136:139],v[164:167],a[88:91],v[136:139]\nxT v[150:151],v198 offset:30720\nxM v[140:143],v[164:167],a[92:95],v[140:143]\nxW lgkmcnt(4)\n"
"xM v[128:131],v[168:171],a[96:99],v[128:131]\nxL v211,s[24:27],s39 offen offset:3072 lds\nxM v[132:135],v[168:171],a[100:103],v[132:135]\nxA m0,s44,147456\n"
"xM v[136:139],v[168:171],a[104:107],v[136:139]\nxA s39,s45,73984\nxM v[140:143],v[168:171],a[108:111],v[140:143]\nxL v208,s[24:27],s39 offen offset:0 lds\n"
"xM v[128:131],v[172:175],a[112:115],v[128:131]\nxM v[132:135],v[172:175],a[116:119],v[132:135]\nxM v[136:139],v[172:175],a[120:123],v[136:139]\nxL v209,s[24:27],s39 offen offset:1024 lds\n"
"xM v[140:143],v[172:175],a[124:127],v[140:143]\nxM v[128:131],v[176:179],a[0:3],v[128:131]\nxM v[132:135],v[176:179],a[4:7],v[132:135]\nxL v210,s[24:27],s39 offen offset:2048 lds\n"
"xM v[136:139],v[176:179],a[8:11],v[136:139]\nxM v[140:143],v[176:179],a[12:15],v[140:143]\nxM v[128:131],v[180:183],a[16:19],v[128:131]\nxW vmcnt(15) lgkmcnt(0)\ns_barrier\n"
"xM v[132:135],v[180:183],a[20:23],v[132:135]\nxL v211,s[24:27],s39 offen offset:3072 lds\nxM v[136:139],v[180:183],a[24:27],v[136:139]\nxA m0,s44,0\nxT v[152:153],v192 offset:32768\nxT v[154:155],v192 offset:36864\n"
"xM v[140:143],v[180:183],a[28:31],v[140:143]\nxA s39,s45,147712\nxT v[156:157],v193 offset:32768\nxT v[158:159],v193 offset:36864\n"
"xM v[128:131],v[144:147],a[32:35],v[128:131]\nxL v208,s[24:27],s39 offen offset:0 lds\nxM v[132:135],v[144:147],a[36:39],v[132:135]\nxT v[160:161],v192 offset:40960\nxT v[162:163],v192 offset:45056\n"
"xM v[136:139],v[144:147],a[40:43],v[136:139]\nxT v[164:165],v193 offset:40960\nxR a[128:131],v199 offset:0\n"
"xM v[140:143],v[144:147],a[44:47],v[140:143]\nxL v209,s[24:27],s39 offen offset:1024 lds\nxM v[128:131],v[148:151],a[48:51],v[128:131]\nxT v[166:167],v193 offset:45056\nxR a[132:135],v199 offset:1024\n"
"xM v[132:135],v[148:151],a[52:55],v[132:135]\nxR a[136:139],v199 offset:2048\nxT v[168:169],v192 offset:49152\n"
"xM v[136:139],v[148:151],a[56:59],v[136:139]\nxL v210,s[24:27],s39 offen offset:2048 lds\nxR a[140:143],v199 offset:3072\n"
"xM v[140:143],v[148:151],a[60:63],v[140:143]\nxW lgkmcnt(5)\nxR a[144:147],v199 offset:4096\nxT v[170:171],v192 offset:53248\n"
"xM v[0:3],v[152:155],a[128:131],v[0:3]\nxW lgkmcnt(5)\nxR a[148:151],v199 offset:5120\nxT v[172:173],v193 offset:49152\n"
"xM v[8:11],v[152:155],a[132:135],v[8:11]\nxW lgkmcnt(4)\nxL v211,s[24:27],s39 offen offset:3072 lds\n"
"xM v[16:19],v[152:155],a[136:139],v[16:19]\nxA m0,s44,16384\nxR a[152:155],v199 offset:6144\nxT v[174:175],v193 offset:53248\n"
"xM v[24:27],v[152:155],a[140:143],v[24:27]\nxA s39,s45,221440\nxR a[156:159],v199 offset:7168\nxT v[176:177],v192 offset:57344\n"
"xM v[4:7],v[156:159],a[128:131],v[4:7]\nxL v208,s[24:27],s39 offen offset:0 lds\nxM v[12:15],v[156:159],a[132:135],v[12:15]\nxR a[160:163],v199 offset:8192\nxT v[178:179],v192 offset:61440\n"
"xM v[20:23],v[156:159],a[136:139],v[20:23]\nxR a[164:167],v199 offset:9216\nxT v[180:181],v193 offset:57344\n"
"xM v[28:31],v[156:159],a[140:143],v[28:31]\nxW lgkmcnt(10)\nxL v209,s[24:27],s39 offen offset:1024 lds\n"
"xM v[0:3],v[160:163],a[144:147],v[0:3]\nxW lgkmcnt(8)\nxR a[168:171],v199 offset:10240\nxT v[182:183],v193 offset:61440\n"
"xM v[8:11],v[160:163],a[148:151],v[8:11]\nxW lgkmcnt(9)\nxR a[172:175],v199 offset:11264\n"
"xM v[16:19],v[160:163],a[152:155],v[16:19]\nxW lgkmcnt(8)\nxL v210,s[24:27],s39 offen offset:2048 lds\n"
"xM v[24:27],v[160:163],a[156:159],v[24:27]\nxR a[176:179],v199 offset:12288\nxM v[4:7],v[164:167],a[144:147],v[4:7]\nxR a[180:183],v199 offset:13312\n"
"xM v[12:15],v[164:167],a[148:151],v[12:15]\nxL v211,s[24:27],s39 offen offset:3072 lds\nxM v[20:23],v[164:167],a[152:155],v[20:23]\nxR a[184:187],v199 offset:14336\n"
"xM v[28:31],v[164:167],a[156:159],v[28:31]\nxW lgkmcnt(6)\nxR a[188:191],v199 offset:15360\n"
"xM v[0:3],v[168:171],a[160:163],v[0:3]\nxR a[192:195],v199 offset:16384\nxM v[8:11],v[168:171],a[164:167],v[8:11]\nxW lgkmcnt(5)\nxR a[196:199],v199 offset:17408\n"
"xM v[16:19],v[168:171],a[168:171],v[16:19]\nxR a[200:203],v199 offset:18432\nxM v[24:27],v[168:171],a[172:175],v[24:27]\nxR a[204:207],v199 offset:19456\n"
"xM v[4:7],v[172:175],a[160:163],v[4:7]\nxW vmcnt(16) lgkmcnt(9)\ns_barrier\n"
"xM v[12:15],v[172:175],a[164:167],v[12:15]\nxA m0,s44,32768\nxT v[144:145],v194 offset:32768\nxT v[146:147],v194 offset:36864\n"
"xM v[20:23],v[172:175],a[168:171],v[20:23]\nxA s39,s45,512\nxT v[148:149],v195 offset:32768\nxT v[150:151],v195 offset:36864\n"
"xM v[28:31],v[172:175],a[172:175],v[28:31]\nxW lgkmcnt(8)\nxL v208,s[24:27],s39 offen offset:0 lds\n"
"xM v[0:3],v[176:179],a[176:179],v[0:3]\nxT v[152:153],v194 offset:40960\nxT v[154:155],v194 offset:45056\n"
"xM v[8:11],v[176:179],a[180:183],v[8:11]\nxR a[208:211],v199 offset:20480\nxT v[156:157],v195 offset:40960\n"
"xM v[16:19],v[176:179],a[184:187],v[16:19]\nxL v209,s[24:27],s39 offen offset:1024 lds\nxM v[24:27],v[176:179],a[188:191],v[24:27]\nxR a[212:215],v199 offset:21504\nxT v[158:159],v195 offset:45056\n"
"xM v[4:7],v[180:183],a[176:179],v[4:7]\nxR a[216:219],v199 offset:22528\nxT v[160:161],v194 offset:49152\n"
"xM v[12:15],v[180:183],a[180:183],v[12:15]\nxL v210,s[24:27],s39 offen offset:2048 lds\nxM v[20:23],v[180:183],a[184:187],v[20:23]\nxR a[220:223],v199 offset:23552\nxT v[162:163],v194 offset:53248\n"
"xM v[28:31],v[180:183],a[188:191],v[28:31]\nxW lgkmcnt(10)\nxR a[224:227],v199 offset:24576\nxT v[164:165],v195 offset:49152\n"
"xM v[0:3],v[144:147],a[192:195],v[0:3]\nxL v211,s[24:27],s39 offen offset:3072 lds\nxM v[8:11],v[144:147],a[196:199],v[8:11]\nxA m0,s44,49152\nxR a[228:231],v199 offset:25600\nxT v[166:167],v195 offset:53248\n"
"xM v[16:19],v[144:147],a[200:203],v[16:19]\nxA s39,s45,74240\nxR a[232:235],v199 offset:26624\nxT v[168:169],v194 offset:57344\n"
"xM v[24:27],v[144:147],a[204:207],v[24:27]\nxL v208,s[24:27],s39 offen offset:0 lds\nxM v[4:7],v[148:151],a[192:195],v[4:7]\nxR a[236:239],v199 offset:27648\nxT v[170:171],v194 offset:61440\n"
"xM v[12:15],v[148:151],a[196:199],v[12:15]\nxR a[240:243],v199 offset:28672\nxT v[172:173],v195 offset:57344\n"
"xM v[20:23],v[148:151],a[200:203],v[20:23]\nxL v209,s[24:27],s39 offen offset:1024 lds\nxM v[28:31],v[148:151],a[204:207],v[28:31]\nxW lgkmcnt(8)\nxR a[244:247],v199 offset:29696\nxT v[174:175],v195 offset:61440\n"
"xM v[0:3],v[152:155],a[208:211],v[0:3]\nxR a[248:251],v199 offset:30720\nxM v[8:11],v[152:155],a[212:215],v[8:11]\nxL v210,s[24:27],s39 offen offset:2048 lds\n"
"xM v[16:19],v[152:155],a[216:219],v[16:19]\nxR a[252:255],v199 offset:31744\nxM v[24:27],v[152:155],a[220:223],v[24:27]\n"
"xM v[4:7],v[156:159],a[208:211],v[4:7]\nxL v211,s[24:27],s39 offen offset:3072 lds\nxM v[12:15],v[156:159],a[212:215],v[12:15]\n"
"xM v[20:23],v[156:159],a[216:219],v[20:23]\nxM v[28:31],v[156:159],a[220:223],v[28:31]\nxM v[0:3],v[160:163],a[224:227],v[0:3]\nxW lgkmcnt(1)\n"
"xM v[8:11],v[160:163],a[228:231],v[8:11]\nxM v[16:19],v[160:163],a[232:235],v[16:19]\nxM v[24:27],v[160:163],a[236:239],v[24:27]\n"
"xM v[4:7],v[164:167],a[224:227],v[4:7]\nxW vmcnt(16) lgkmcnt(2)\ns_barrier\n"
"xM v[12:15],v[164:167],a[228:231],v[12:15]\nxA m0,s44,98304\nxT v[176:177],v196 offset:0\nxT v[178:179],v196 offset:4096\n"
"xM v[20:23],v[164:167],a[232:235],v[20:23]\nxA s39,s45,147968\nxT v[180:181],v197 offset:0\nxT v[182:183],v197 offset:4096\n"
"xM v[28:31],v[164:167],a[236:239],v[28:31]\nxL v208,s[24:27],s39 offen offset:0 lds\nxM v[0:3],v[168:171],a[240:243],v[0:3]\nxT v[144:145],v196 offset:8192\nxT v[146:147],v196 offset:12288\n"
"xM v[8:11],v[168:171],a[244:247],v[8:11]\nxT v[148:149],v197 offset:8192\nxT v[150:151],v197 offset:12288\n"
"xM v[16:19],v[168:171],a[248:251],v[16:19]\nxW lgkmcnt(8)\nxL v209,s[24:27],s39 offen offset:1024 lds\n"
"xM v[24:27],v[168:171],a[252:255],v[24:27]\nxT v[152:153],v196 offset:16384\nxT v[154:155],v196 offset:20480\n"
"xM v[4:7],v[172:175],a[240:243],v[4:7]\nxT v[156:157],v197 offset:16384\nxM v[12:15],v[172:175],a[244:247],v[12:15]\nxL v210,s[24:27],s39 offen offset:2048 lds\n"
"xM v[20:23],v[172:175],a[248:251],v[20:23]\nxT v[158:159],v197 offset:20480\nxM v[28:31],v[172:175],a[252:255],v[28:31]\nxW lgkmcnt(8)\n"
"xM v[32:35],v[176:179],a[128:131],v[32:35]\nxL v211,s[24:27],s39 offen offset:3072 lds\nxM v[40:43],v[176:179],a[132:135],v[40:43]\nxA m0,s44,114688\nxT v[160:161],v196 offset:24576\n"
"xM v[48:51],v[176:179],a[136:139],v[48:51]\nxA s39,s45,221696\nxT v[162:163],v196 offset:28672\n"
"xM v[56:59],v[176:179],a[140:143],v[56:59]\nxL v208,s[24:27],s39 offen offset:0 lds\nxM v[36:39],v[180:183],a[128:131],v[36:39]\nxC v0,v0,v1\nxC v1,v2,v3\n"
"xM v[44:47],v[180:183],a[132:135],v[44:47]\nxC v2,v4,v5\nxC v3,v6,v7\n"
"xM v[52:55],v[180:183],a[136:139],v[52:55]\nxS v[0:3],v249,s[96:99],s46 offen sc1\nxM v[60:63],v[180:183],a[140:143],v[60:63]\nxW lgkmcnt(2)\nxL v209,s[24:27],s39 offen offset:1024 lds\n"
"xM v[32:35],v[144:147],a[144:147],v[32:35]\nxC v8,v8,v9\nxC v9,v10,v11\n"
"xM v[40:43],v[144:147],a[148:151],v[40:43]\nxC v10,v12,v13\nxC v11,v14,v15\n"
"xM v[48:51],v[144:147],a[152:155],v[48:51]\nxL v210,s[24:27],s39 offen offset:2048 lds\nxM v[56:59],v[144:147],a[156:159],v[56:59]\nxT v[164:165],v197 offset:24576\nxC v16,v16,v17\n"
"xM v[36:39],v[148:151],a[144:147],v[36:39]\nxT v[166:167],v197 offset:28672\nxC v17,v18,v19\n"
"xM v[44:47],v[148:151],a[148:151],v[44:47]\nxL v211,s[24:27],s39 offen offset:3072 lds\nxM v[52:55],v[148:151],a[152:155],v[52:55]\nxC v18,v20,v21\nxC v19,v22,v23\n"
"xM v[60:63],v[148:151],a[156:159],v[60:63]\nxC v24,v24,v25\nxC v25,v26,v27\n"
"xM v[32:35],v[152:155],a[160:163],v[32:35]\nxC v26,v28,v29\nxC v27,v30,v31\n"
"xM v[40:43],v[152:155],a[164:167],v[40:43]\nxM v[48:51],v[152:155],a[168:171],v[48:51]\nxM v[56:59],v[152:155],a[172:175],v[56:59]\n"
"xM v[36:39],v[156:159],a[160:163],v[36:39]\nxW vmcnt(17) lgkmcnt(0)\ns_barrier\n"
"xM v[44:47],v[156:159],a[164:167],v[44:47]\nxA m0,s44,131072\nxT v[168:169],v192 offset:0\nxT v[170:171],v192 offset:4096\n"
"xM v[52:55],v[156:159],a[168:171],v[52:55]\nxA s39,s45,768\nxT v[172:173],v193 offset:0\nxT v[174:175],v193 offset:4096\n"
"xM v[60:63],v[156:159],a[172:175],v[60:63]\nxL v208,s[24:27],s39 offen offset:0 lds\nxM v[32:35],v[160:163],a[176:179],v[32:35]\nxT v[176:177],v192 offset:8192\nxT v[178:179],v192 offset:12288\n"
"xM v[40:43],v[160:163],a[180:183],v[40:43]\nxT v[180:181],v193 offset:8192\nxT v[182:183],v193 offset:12288\n"
"xM v[48:51],v[160:163],a[184:187],v[48:51]\nxL v209,s[24:27],s39 offen offset:1024 lds\nxM v[56:59],v[160:163],a[188:191],v[56:59]\nxT v[144:145],v192 offset:16384\nxT v[146:147],v192 offset:20480\n"
"xM v[36:39],v[164:167],a[176:179],v[36:39]\nxT v[148:149],v193 offset:16384\nxM v[44:47],v[164:167],a[180:183],v[44:47]\nxL v210,s[24:27],s39 offen offset:2048 lds\n"
"xM v[52:55],v[164:167],a[184:187],v[52:55]\nxS v[8:11],v250,s[96:99],s46 offen sc1\nxM v[60:63],v[164:167],a[188:191],v[60:63]\nxW lgkmcnt(7)\nxT v[150:151],v193 offset:20480\n"
"xM v[32:35],v[168:171],a[192:195],v[32:35]\nxL v211,s[24:27],s39 offen offset:3072 lds\nxM v[40:43],v[168:171],a[196:199],v[40:43]\nxA m0,s44,147456\nxT v[152:153],v192 offset:24576\n"
"xM v[48:51],v[168:171],a[200:203],v[48:51]\nxA s39,s45,74496\nxT v[154:155],v192 offset:28672\n"
"xM v[56:59],v[168:171],a[204:207],v[56:59]\nxL v208,s[24:27],s39 offen offset:0 lds\nxM v[36:39],v[172:175],a[192:195],v[36:39]\nxT v[156:157],v193 offset:24576\n"
"xM v[44:47],v[172:175],a[196:199],v[44:47]\nxM v[52:55],v[172:175],a[200:203],v[52:55]\nxL v209,s[24:27],s39 offen offset:1024 lds\n"
"xM v[60:63],v[172:175],a[204:207],v[60:63]\nxW lgkmcnt(3)\nxT v[158:159],v193 offset:28672\n"
"xM v[32:35],v[176:179],a[208:211],v[32:35]\nxM v[40:43],v[176:179],a[212:215],v[40:43]\nxL v210,s[24:27],s39 offen offset:2048 lds\n"
"xM v[48:51],v[176:179],a[216:219],v[48:51]\nxM v[56:59],v[176:179],a[220:223],v[56:59]\nxM v[36:39],v[180:183],a[208:211],v[36:39]\nTCH2_2_%=:\ns_cbranch_execz TDONE2_%=\nxL v211,s[24:27],s39 offen offset:3072 lds\n"
"xM v[44:47],v[180:183],a[212:215],v[44:47]\nxM v[52:55],v[180:183],a[216:219],v[52:55]\nxM v[60:63],v[180:183],a[220:223],v[60:63]\n"
"xM v[32:35],v[144:147],a[224:227],v[32:35]\nxM v[40:43],v[144:147],a[228:231],v[40:43]\nxM v[48:51],v[144:147],a[232:235],v[48:51]\n"
"xM v[56:59],v[144:147],a[236:239],v[56:59]\nxM v[36:39],v[148:151],a[224:227],v[36:39]\nxW vmcnt(18) lgkmcnt(0)\ns_barrier\n"
"xM v[44:47],v[148:151],a[228:231],v[44:47]\nxA m0,s44,0\nxT v[160:161],v192 offset:32768\nxT v[162:163],v192 offset:36864\n"
"xM v[52:55],v[148:151],a[232:235],v[52:55]\nxA s39,s45,148224\nxS v[16:19],v251,s[96:99],s46 offen sc1\n"
"xM v[60:63],v[148:151],a[236:239],v[60:63]\nxL v208,s[24:27],s39 offen offset:0 lds\nxM v[32:35],v[152:155],a[240:243],v[32:35]\nxT v[164:165],v193 offset:32768\nxT v[166:167],v193 offset:36864\n"
"xM v[40:43],v[152:155],a[244:247],v[40:43]\nxT v[168:169],v192 offset:40960\nxT v[170:171],v192 offset:45056\n"
"xM v[48:51],v[152:155],a[248:251],v[48:51]\nxL v209,s[24:27],s39 offen offset:1024 lds\nxM v[56:59],v[152:155],a[252:255],v[56:59]\nxT v[172:173],v193 offset:40960\nxT v[174:175],v193 offset:45056\n"
"xM v[36:39],v[156:159],a[240:243],v[36:39]\nxT v[176:177],v192 offset:49152\nxT v[178:179],v192 offset:53248\n"
"xM v[44:47],v[156:159],a[244:247],v[44:47]\nxL v210,s[24:27],s39 offen offset:2048 lds\nxM v[52:55],v[156:159],a[248:251],v[52:55]\nxT v[180:181],v193 offset:49152\nxT v[182:183],v193 offset:53248\n"
"xM v[60:63],v[156:159],a[252:255],v[60:63]\nxW lgkmcnt(10)\nxM v[64:67],v[160:163],a[128:131],v[64:67]\nxL v211,s[24:27],s39 offen offset:3072 lds\n"
"xM v[72:75],v[160:163],a[132:135],v[72:75]\nxA m0,s44,16384\nxT v[144:145],v192 offset:57344\n"
"xM v[80:83],v[160:163],a[136:139],v[80:83]\nxA s39,s45,221952\nxT v[146:147],v192 offset:61440\n"
"xM v[88:91],v[160:163],a[140:143],v[88:91]\nxW lgkmcnt(6)\nxL v208,s[24:27],s39 offen offset:0 lds\n"
"xM v[68:71],v[164:167],a[128:131],v[68:71]\nxC v32,v32,v33\nxC v33,v34,v35\n"
"xM v[76:79],v[164:167],a[132:135],v[76:79]\nxC v34,v36,v37\nxC v35,v38,v39\n"
"xM v[84:87],v[164:167],a[136:139],v[84:87]\nxL v209,s[24:27],s39 offen offset:1024 lds\nxM v[92:95],v[164:167],a[140:143],v[92:95]\nxC v40,v40,v41\nxC v41,v42,v43\n"
"xM v[64:67],v[168:171],a[144:147],v[64:67]\nxC v42,v44,v45\nxC v43,v46,v47\n"
"xM v[72:75],v[168:171],a[148:151],v[72:75]\nxL v210,s[24:27],s39 offen offset:2048 lds\nxM v[80:83],v[168:171],a[152:155],v[80:83]\nxT v[148:149],v193 offset:57344\nxC v48,v48,v49\n"
"xM v[88:91],v[168:171],a[156:159],v[88:91]\nxC v49,v50,v51\nxC v50,v52,v53\n"
"xM v[68:71],v[172:175],a[144:147],v[68:71]\nxL v211,s[24:27],s39 offen offset:3072 lds\nxM v[76:79],v[172:175],a[148:151],v[76:79]\nxT v[150:151],v193 offset:61440\nxC v51,v54,v55\n"
"xM v[84:87],v[172:175],a[152:155],v[84:87]\nxW lgkmcnt(2)\nxS v[24:27],v252,s[96:99],s46 offen sc1\n"
"xM v[92:95],v[172:175],a[156:159],v[92:95]\nxA s46,s46,2048\nxC v56,v56,v57\nxC v57,v58,v59\n"
"xM v[64:67],v[176:179],a[160:163],v[64:67]\nxC v58,v60,v61\nxC v59,v62,v63\n"
"xM v[72:75],v[176:179],a[164:167],v[72:75]\nxM v[80:83],v[176:179],a[168:171],v[80:83]\nxM v[88:91],v[176:179],a[172:175],v[88:91]\n"
"xM v[68:71],v[180:183],a[160:163],v[68:71]\nxW vmcnt(19) lgkmcnt(0)\ns_barrier\n"
"xM v[76:79],v[180:183],a[164:167],v[76:79]\nxA m0,s44,32768\nxT v[152:153],v194 offset:32768\nxT v[154:155],v194 offset:36864\n"
"xM v[84:87],v[180:183],a[168:171],v[84:87]\nxA s39,s45,1920\nxT v[156:157],v195 offset:32768\nxT v[158:159],v195 offset:36864\n"
"xM v[92:95],v[180:183],a[172:175],v[92:95]\nxL v244,s[24:27],s39 offen lds\nxM v[64:67],v[144:147],a[176:179],v[64:67]\nxA m0,s44,33792\nxT v[160:161],v194 offset:40960\nxT v[162:163],v194 offset:45056\n"
"xM v[72:75],v[144:147],a[180:183],v[72:75]\nxA s39,s45,11136\nxT v[164:165],v195 offset:40960\nxT v[166:167],v195 offset:45056\n"
"xM v[80:83],v[144:147],a[184:187],v[80:83]\nxL v244,s[24:27],s39 offen lds\nxM v[88:91],v[144:147],a[188:191],v[88:91]\nxA m0,s44,34816\nxT v[168:169],v194 offset:49152\nxT v[170:171],v194 offset:53248\n"
"xM v[68:71],v[148:151],a[176:179],v[68:71]\nxA s39,s45,20352\nxT v[172:173],v195 offset:49152\n"
"xM v[76:79],v[148:151],a[180:183],v[76:79]\nxL v244,s[24:27],s39 offen lds\nxM v[84:87],v[148:151],a[184:187],v[84:87]\nxA m0,s44,35840\nxT v[174:175],v195 offset:53248\n"
"xM v[92:95],v[148:151],a[188:191],v[92:95]\nxA s39,s45,29568\nxW lgkmcnt(10)\n"
"xM v[64:67],v[152:155],a[192:195],v[64:67]\nxL v244,s[24:27],s39 offen lds\nxM v[72:75],v[152:155],a[196:199],v[72:75]\nxA m0,s44,49152\nxT v[176:177],v194 offset:57344\n"
"xM v[80:83],v[152:155],a[200:203],v[80:83]\nxA s39,s45,149376\nxT v[178:179],v194 offset:61440\n"
"xM v[88:91],v[152:155],a[204:207],v[88:91]\nxW lgkmcnt(4)\nxL v244,s[24:27],s39 offen lds\n"
"xM v[68:71],v[156:159],a[192:195],v[68:71]\nxT v[180:181],v195 offset:57344\nxM v[76:79],v[156:159],a[196:199],v[76:79]\nxA m0,s44,50176\n"
"xM v[84:87],v[156:159],a[200:203],v[84:87]\nxA s39,s45,158592\nxS v[32:35],v249,s[96:99],s46 offen sc1\n"
"xM v[92:95],v[156:159],a[204:207],v[92:95]\nxL v244,s[24:27],s39 offen lds\nxM v[64:67],v[160:163],a[208:211],v[64:67]\nxA m0,s44,51200\nxT v[182:183],v195 offset:61440\n"
"xM v[72:75],v[160:163],a[212:215],v[72:75]\nxA s39,s45,167808\nxM v[80:83],v[160:163],a[216:219],v[80:83]\nxL v244,s[24:27],s39 offen lds\n"
"xM v[88:91],v[160:163],a[220:223],v[88:91]\nxA m0,s44,52224\nxM v[68:71],v[164:167],a[208:211],v[68:71]\nxA s39,s45,177024\n"
"xM v[76:79],v[164:167],a[212:215],v[76:79]\nxL v244,s[24:27],s39 offen lds\nxM v[84:87],v[164:167],a[216:219],v[84:87]\n"
"xM v[92:95],v[164:167],a[220:223],v[92:95]\nxA m0,s34,65536\nxM v[64:67],v[168:171],a[224:227],v[64:67]\nxA s39,s34,1053184\nxS v[40:43],v250,s[96:99],s46 offen sc1\n"
"xM v[72:75],v[168:171],a[228:231],v[72:75]\nxL v224,s[96:99],s39 offen sc1 lds\nxM v[80:83],v[168:171],a[232:235],v[80:83]\n"
"xM v[88:91],v[168:171],a[236:239],v[88:91]\nxW lgkmcnt(0)\nxM v[68:71],v[172:175],a[224:227],v[68:71]\nxW vmcnt(21) lgkmcnt(0)\ns_barrier\n"
"xM v[76:79],v[172:175],a[228:231],v[76:79]\nxA m0,s34,69632\nxT v[144:145],v196 offset:0\nxT v[146:147],v196 offset:4096\n"
"xM v[84:87],v[172:175],a[232:235],v[84:87]\nxA s39,s34,1057280\nxT v[148:149],v197 offset:0\nxT v[150:151],v197 offset:4096\n"
"xM v[92:95],v[172:175],a[236:239],v[92:95]\nxL v224,s[96:99],s39 offen sc1 lds\nxM v[64:67],v[176:179],a[240:243],v[64:67]\nxT v[152:153],v196 offset:8192\nxT v[154:155],v196 offset:12288\n"
"xM v[72:75],v[176:179],a[244:247],v[72:75]\nxT v[156:157],v197 offset:8192\nxT v[158:159],v197 offset:12288\n"
"xM v[80:83],v[176:179],a[248:251],v[80:83]\nxS v[48:51],v251,s[96:99],s46 offen sc1\nxM v[88:91],v[176:179],a[252:255],v[88:91]\nxT v[160:161],v196 offset:16384\nxT v[162:163],v196 offset:20480\n"
"xM v[68:71],v[180:183],a[240:243],v[68:71]\nxT v[164:165],v197 offset:16384\nxM v[76:79],v[180:183],a[244:247],v[76:79]\n"
"xM v[84:87],v[180:183],a[248:251],v[84:87]\nxT v[166:167],v197 offset:20480\nxM v[92:95],v[180:183],a[252:255],v[92:95]\nxW lgkmcnt(8)\n"
"xM v[96:99],v[144:147],a[128:131],v[96:99]\nxT v[168:169],v196 offset:24576\nxM v[104:107],v[144:147],a[132:135],v[104:107]\n"
"xM v[112:115],v[144:147],a[136:139],v[112:115]\nxT v[170:171],v196 offset:28672\nxM v[120:123],v[144:147],a[140:143],v[120:123]\nxC v64,v64,v65\nxC v65,v66,v67\n"
"xM v[100:103],v[148:151],a[128:131],v[100:103]\nxS v[56:59],v252,s[96:99],s46 offen sc1\nxM v[108:111],v[148:151],a[132:135],v[108:111]\nxA s46,s46,2048\nxC v66,v68,v69\nxC v67,v70,v71\n"
"xM v[116:119],v[148:151],a[136:139],v[116:119]\nxC v72,v72,v73\nxC v73,v74,v75\n"
"xM v[124:127],v[148:151],a[140:143],v[124:127]\nxW lgkmcnt(2)\nxC v74,v76,v77\nxC v75,v78,v79\n"
"xM v[96:99],v[152:155],a[144:147],v[96:99]\nxC v80,v80,v81\nxC v81,v82,v83\n"
"xM v[104:107],v[152:155],a[148:151],v[104:107]\nxC v82,v84,v85\nxC v83,v86,v87\n"
"xM v[112:115],v[152:155],a[152:155],v[112:115]\nxT v[172:173],v197 offset:24576\nxC v88,v88,v89\n"
"xM v[120:123],v[152:155],a[156:159],v[120:123]\nxC v89,v90,v91\nxC v90,v92,v93\n"
"xM v[100:103],v[156:159],a[144:147],v[100:103]\nxT v[174:175],v197 offset:28672\nxC v91,v94,v95\n"
"xM v[108:111],v[156:159],a[148:151],v[108:111]\nxM v[116:119],v[156:159],a[152:155],v[116:119]\nxS v[64:67],v249,s[96:99],s46 offen sc1\n"
"xM v[124:127],v[156:159],a[156:159],v[124:127]\nxM v[96:99],v[160:163],a[160:163],v[96:99]\nxM v[104:107],v[160:163],a[164:167],v[104:107]\n"
"xM v[112:115],v[160:163],a[168:171],v[112:115]\nxM v[120:123],v[160:163],a[172:175],v[120:123]\nxM v[100:103],v[164:167],a[160:163],v[100:103]\nxW vmcnt(16) lgkmcnt(0)\ns_barrier\n"
"xM v[108:111],v[164:167],a[164:167],v[108:111]\nxT v[176:177],v192 offset:0\nxT v[178:179],v192 offset:4096\n"
"xM v[116:119],v[164:167],a[168:171],v[116:119]\nxT v[180:181],v193 offset:0\nxT v[182:183],v193 offset:4096\n"
"xM v[124:127],v[164:167],a[172:175],v[124:127]\nxT v[144:145],v192 offset:8192\nxT v[146:147],v192 offset:12288\n"
"xM v[96:99],v[168:171],a[176:179],v[96:99]\nxS v[72:75],v250,s[96:99],s46 offen sc1\nxM v[104:107],v[168:171],a[180:183],v[104:107]\nxT v[148:149],v193 offset:8192\nxT v[150:151],v193 offset:12288\n"
"xM v[112:115],v[168:171],a[184:187],v[112:115]\nxT v[152:153],v192 offset:16384\nxT v[154:155],v192 offset:20480\n"
"xM v[120:123],v[168:171],a[188:191],v[120:123]\nxM v[100:103],v[172:175],a[176:179],v[100:103]\nxT v[156:157],v193 offset:16384\n"
"xM v[108:111],v[172:175],a[180:183],v[108:111]\nxM v[116:119],v[172:175],a[184:187],v[116:119]\nxT v[158:159],v193 offset:20480\n"
"xM v[124:127],v[172:175],a[188:191],v[124:127]\nxW lgkmcnt(6)\nxM v[96:99],v[176:179],a[192:195],v[96:99]\nxT v[160:161],v192 offset:24576\n"
"xM v[104:107],v[176:179],a[196:199],v[104:107]\nxM v[112:115],v[176:179],a[200:203],v[112:115]\nxS v[80:83],v251,s[96:99],s46 offen sc1\n"
"xM v[120:123],v[176:179],a[204:207],v[120:123]\nxT v[162:163],v192 offset:28672\nxM v[100:103],v[180:183],a[192:195],v[100:103]\nxT v[164:165],v193 offset:24576\n"
"xM v[108:111],v[180:183],a[196:199],v[108:111]\nxM v[116:119],v[180:183],a[200:203],v[116:119]\nxT v[166:167],v193 offset:28672\n"
"xM v[124:127],v[180:183],a[204:207],v[124:127]\nxM v[96:99],v[144:147],a[208:211],v[96:99]\nxM v[104:107],v[144:147],a[212:215],v[104:107]\n"
"xM v[112:115],v[144:147],a[216:219],v[112:115]\nxM v[120:123],v[144:147],a[220:223],v[120:123]\nxW lgkmcnt(2)\n"
"xM v[100:103],v[148:151],a[208:211],v[100:103]\nxS v[88:91],v252,s[96:99],s46 offen sc1\nxM v[108:111],v[148:151],a[212:215],v[108:111]\nxA s46,s46,2048\n"
"xM v[116:119],v[148:151],a[216:219],v[116:119]\nxM v[124:127],v[148:151],a[220:223],v[124:127]\nxM v[96:99],v[152:155],a[224:227],v[96:99]\n"
"xM v[104:107],v[152:155],a[228:231],v[104:107]\nxM v[112:115],v[152:155],a[232:235],v[112:115]\nxM v[120:123],v[152:155],a[236:239],v[120:123]\n"
"xM v[100:103],v[156:159],a[224:227],v[100:103]\nxW vmcnt(9) lgkmcnt(0)\ns_barrier\n"
"xM v[108:111],v[156:159],a[228:231],v[108:111]\nxT v[168:169],v198 offset:32768\nxT v[170:171],v198 offset:34816\n"
"xM v[116:119],v[156:159],a[232:235],v[116:119]\nxT v[172:173],v198 offset:36864\nxT v[174:175],v198 offset:38912\n"
"xM v[124:127],v[156:159],a[236:239],v[124:127]\nxT v[176:177],v198 offset:40960\nxT v[178:179],v198 offset:43008\n"
"xM v[96:99],v[160:163],a[240:243],v[96:99]\nxT v[180:181],v198 offset:45056\nxT v[182:183],v198 offset:47104\n"
"xM v[104:107],v[160:163],a[244:247],v[104:107]\nxT v[144:145],v198 offset:49152\nxM v[112:115],v[160:163],a[248:251],v[112:115]\nxT v[146:147],v198 offset:51200\n"
"xM v[120:123],v[160:163],a[252:255],v[120:123]\nxM v[100:103],v[164:167],a[240:243],v[100:103]\nxT v[148:149],v198 offset:53248\n"
"xM v[108:111],v[164:167],a[244:247],v[108:111]\nxM v[116:119],v[164:167],a[248:251],v[116:119]\nxT v[150:151],v198 offset:55296\n"
"xM v[124:127],v[164:167],a[252:255],v[124:127]\nxW lgkmcnt(6)\nxM v[128:131],v[168:171],a[128:131],v[128:131]\nxT v[152:153],v198 offset:57344\n"
"xM v[132:135],v[168:171],a[132:135],v[132:135]\nxM v[136:139],v[168:171],a[136:139],v[136:139]\nxT v[154:155],v198 offset:59392\n"
"xM v[140:143],v[168:171],a[140:143],v[140:143]\nxC v96,v96,v97\nxC v97,v98,v99\n"
"xM v[128:131],v[172:175],a[144:147],v[128:131]\nxC v98,v100,v101\nxC v99,v102,v103\n"
"xM v[132:135],v[172:175],a[148:151],v[132:135]\nxC v104,v104,v105\nxC v105,v106,v107\n"
"xM v[136:139],v[172:175],a[152:155],v[136:139]\nxS v[96:99],v249,s[96:99],s46 offen sc1\nxM v[140:143],v[172:175],a[156:159],v[140:143]\nxC v106,v108,v109\nxC v107,v110,v111\n"
"xM v[128:131],v[176:179],a[160:163],v[128:131]\nxC v112,v112,v113\nxC v113,v114,v115\n"
"xM v[132:135],v[176:179],a[164:167],v[132:135]\nxC v114,v116,v117\nxC v115,v118,v119\n"
"xM v[136:139],v[176:179],a[168:171],v[136:139]\nxW vmcnt(5)\ns_mov_b64 exec, 1\nbuffer_store_dwordx2 v[242:243], off, s[96:99], s50 sc1\ns_mov_b64 exec, -1\n"
"xM v[140:143],v[176:179],a[172:175],v[140:143]\nxA s50,s50,256\nxT v[156:157],v198 offset:61440\nxC v120,v120,v121\nxW lgkmcnt(7)\n"
"xM v[128:131],v[180:183],a[176:179],v[128:131]\nxT v[158:159],v198 offset:63488\nxC v121,v122,v123\n"
"xM v[132:135],v[180:183],a[180:183],v[132:135]\nxC v122,v124,v125\nxC v123,v126,v127\n"
"xM v[136:139],v[180:183],a[184:187],v[136:139]\nxM v[140:143],v[180:183],a[188:191],v[140:143]\nxW lgkmcnt(2)\n"
"xM v[128:131],v[144:147],a[192:195],v[128:131]\nxS v[104:107],v250,s[96:99],s46 offen sc1\nxM v[132:135],v[144:147],a[196:199],v[132:135]\n"
"xM v[136:139],v[144:147],a[200:203],v[136:139]\nxM v[140:143],v[144:147],a[204:207],v[140:143]\nxM v[128:131],v[148:151],a[208:211],v[128:131]\n"
"xM v[132:135],v[148:151],a[212:215],v[132:135]\nxM v[136:139],v[148:151],a[216:219],v[136:139]\nxM v[140:143],v[148:151],a[220:223],v[140:143]\n"
"xM v[128:131],v[152:155],a[224:227],v[128:131]\nxM v[132:135],v[152:155],a[228:231],v[132:135]\nxM v[136:139],v[152:155],a[232:235],v[136:139]\nxS v[112:115],v251,s[96:99],s46 offen sc1\n"
"xM v[140:143],v[152:155],a[236:239],v[140:143]\nxW lgkmcnt(0)\nxS v[120:123],v252,s[96:99],s46 offen sc1\n"
"xM v[128:131],v[156:159],a[240:243],v[128:131]\nxA s46,s46,2048\nxM v[132:135],v[156:159],a[244:247],v[132:135]\ns_sub_u32 s46, s46, s37\n"
"xM v[136:139],v[156:159],a[248:251],v[136:139]\nxM v[140:143],v[156:159],a[252:255],v[140:143]\ns_nop 7\nxC v128,v128,v129\nxC v129,v130,v131\nxC v130,v132,v133\n"
"xC v131,v134,v135\ns_nop 0\nv_permlane16_swap_b32 v128, v130\ns_nop 0\nv_permlane16_swap_b32 v129, v131\nxS v[128:131],v253,s[96:99],s46 offen sc1\n"
"xA s46,s46,589824\nxC v136,v136,v137\nxC v137,v138,v139\nxC v138,v140,v141\nxC v139,v142,v143\ns_nop 0\n"
"v_permlane16_swap_b32 v136, v138\ns_nop 0\nv_permlane16_swap_b32 v137, v139\nxS v[136:139],v253,s[96:99],s46 offen sc1\nxA s46,s46,589824\ns_branch SKIP2_%=\n"
"SPINZ_%=:\ns_cmp_lg_u32 %[wid], 0\ns_cbranch_scc1 RETZ_%=\ns_mov_b32 s52, 200000\nSPLZ_%=:\nbuffer_load_dwordx2 v[236:237], v238, s[96:99], s61 offen sc1\n"
"xW vmcnt(0)\nv_cmp_ne_u64 vcc, s[68:69], v[236:237]\ns_and_b32 s38, vcc_lo, 61440\ns_cbranch_scc0 RETZ_%=\ns_sleep 4\ns_sub_u32 s52, s52, 1\n"
"s_cmp_lg_u32 s52, 0\ns_cbranch_scc1 SPLZ_%=\ns_branch RETZ_%=\nSPINM2_%=:\ns_cmp_lg_u32 %[wid], 0\ns_cbranch_scc1 RETM2_%=\n"
"s_mov_b32 s52, 200000\nSPLM2_%=:\nbuffer_load_dwordx2 v[246:247], v245, s[96:99], s53 offen sc1\nxW vmcnt(0)\nv_cmp_ne_u64 vcc, s[68:69], v[246:247]\ns_cmp_lg_u64 vcc, 0\n"
"s_cbranch_scc0 RETM2_%=\ns_sleep 4\ns_sub_u32 s52, s52, 1\ns_cmp_lg_u32 s52, 0\ns_cbranch_scc1 SPLM2_%=\ns_branch RETM2_%=\n"
"BUMP_%=:\nv_lshlrev_b32 v180, 3, v255\ns_mov_b32 s52, 200000\nBUMPL_%=:\ns_mov_b32 s39, 1204224\ns_mov_b32 s38, 1208320\n"
"buffer_load_dwordx2 a[64:65], v180, s[96:99], s39 offen offset:0 sc1\nbuffer_load_dwordx2 a[66:67], v180, s[96:99], s39 offen offset:512 sc1\nbuffer_load_dwordx2 a[68:69], v180, s[96:99], s39 offen offset:1024 sc1\nbuffer_load_dwordx2 a[70:71], v180, s[96:99], s39 offen offset:1536 sc1\nbuffer_load_dwordx2 a[72:73], v180, s[96:99], s39 offen offset:2048 sc1\nbuffer_load_dwordx2 a[74:75], v180, s[96:99], s39 offen offset:2560 sc1\n"
"buffer_load_dwordx2 a[76:77], v180, s[96:99], s39 offen offset:3072 sc1\nbuffer_load_dwordx2 a[78:79], v180, s[96:99], s39 offen offset:3584 sc1\nbuffer_load_dwordx2 a[80:81], v180, s[96:99], s38 offen offset:0 sc1\nbuffer_load_dwordx2 a[82:83], v180, s[96:99], s38 offen offset:512 sc1\nbuffer_load_dwordx2 a[84:85], v180, s[96:99], s38 offen offset:1024 sc1\nbuffer_load_dwordx2 a[86:87], v180, s[96:99], s38 offen offset:1536 sc1\n"
"buffer_load_dwordx2 a[88:89], v180, s[96:99], s38 offen offset:2048 sc1\nbuffer_load_dwordx2 a[90:91], v180, s[96:99], s38 offen offset:2560 sc1\nbuffer_load_dwordx2 a[92:93], v180, s[96:99], s38 offen offset:3072 sc1\nbuffer_load_dwordx2 a[94:95], v180, s[96:99], s38 offen offset:3584 sc1\nxW vmcnt(0)\ns_mov_b64 s[60:61], 0\n"
"v_accvgpr_read_b32 v182, a64\nv_accvgpr_read_b32 v183, a65\nv_cmp_ne_u64 vcc, s[68:69], v[182:183]\ns_or_b64 s[60:61], s[60:61], vcc\nv_accvgpr_read_b32 v182, a66\nv_accvgpr_read_b32 v183, a67\n"
"v_cmp_ne_u64 vcc, s[68:69], v[182:183]\ns_or_b64 s[60:61], s[60:61], vcc\nv_accvgpr_read_b32 v182, a68\nv_accvgpr_read_b32 v183, a69\nv_cmp_ne_u64 vcc, s[68:69], v[182:183]\ns_or_b64 s[60:61], s[60:61], vcc\n"
"v_accvgpr_read_b32 v182, a70\nv_accvgpr_read_b32 v183, a71\nv_cmp_ne_u64 vcc, s[68:69], v[182:183]\ns_or_b64 s[60:61], s[60:61], vcc\nv_accvgpr_read_b32 v182, a72\nv_accvgpr_read_b32 v183, a73\n"
"v_cmp_ne_u64 vcc, s[68:69], v[182:183]\ns_or_b64 s[60:61], s[60:61], vcc\nv_accvgpr_read_b32 v182, a74\nv_accvgpr_read_b32 v183, a75\nv_cmp_ne_u64 vcc, s[68:69], v[182:183]\ns_or_b64 s[60:61], s[60:61], vcc\n"
"v_accvgpr_read_b32 v182, a76\nv_accvgpr_read_b32 v183, a77\nv_cmp_ne_u64 vcc, s[68:69], v[182:183]\ns_or_b64 s[60:61], s[60:61], vcc\nv_accvgpr_read_b32 v182, a78\nv_accvgpr_read_b32 v183, a79\n"
"v_cmp_ne_u64 vcc, s[68:69], v[182:183]\ns_or_b64 s[60:61], s[60:61], vcc\nv_accvgpr_read_b32 v182, a80\nv_accvgpr_read_b32 v183, a81\nv_cmp_ne_u64 vcc, s[68:69], v[182:183]\ns_or_b64 s[60:61], s[60:61], vcc\n"
"v_accvgpr_read_b32 v182, a82\nv_accvgpr_read_b32 v183, a83\nv_cmp_ne_u64 vcc, s[68:69], v[182:183]\ns_or_b64 s[60:61], s[60:61], vcc\nv_accvgpr_read_b32 v182, a84\nv_accvgpr_read_b32 v183, a85\n"
"v_cmp_ne_u64 vcc, s[68:69], v[182:183]\ns_or_b64 s[60:61], s[60:61], vcc\nv_accvgpr_read_b32 v182, a86\nv_accvgpr_read_b32 v183, a87\nv_cmp_ne_u64 vcc, s[68:69], v[182:183]\ns_or_b64 s[60:61], s[60:61], vcc\n"
"v_accvgpr_read_b32 v182, a88\nv_accvgpr_read_b32 v183, a89\nv_cmp_ne_u64 vcc, s[68:69], v[182:183]\ns_or_b64 s[60:61], s[60:61], vcc\nv_accvgpr_read_b32 v182, a90\nv_accvgpr_read_b32 v183, a91\n"
"v_cmp_ne_u64 vcc, s[68:69], v[182:183]\ns_or_b64 s[60:61], s[60:61], vcc\nv_accvgpr_read_b32 v182, a92\nv_accvgpr_read_b32 v183, a93\nv_cmp_ne_u64 vcc, s[68:69], v[182:183]\ns_or_b64 s[60:61], s[60:61], vcc\n"
"v_accvgpr_read_b32 v182, a94\nv_accvgpr_read_b32 v183, a95\nv_cmp_ne_u64 vcc, s[68:69], v[182:183]\ns_or_b64 s[60:61], s[60:61], vcc\ns_cbranch_scc0 BUMPD_%=\ns_sleep 8\n"
"s_sub_u32 s52, s52, 1\ns_cmp_lg_u32 s52, 0\ns_cbranch_scc1 BUMPL_%=\nBUMPD_%=:\ns_mov_b32 s39, 1277952\ns_mov_b64 exec, 1\n"
"buffer_store_dwordx2 v[242:243], off, s[96:99], s39 sc1\ns_mov_b64 exec, -1\ns_branch BUMPR_%=\nSPINA1_%=:\ns_cmp_lg_u32 %[wid], 0\ns_cbranch_scc1 RETA1_%=\n"
"s_mov_b32 s52, 200000\nSPLA1_%=:\ns_mov_b32 exec_hi, 0\nbuffer_load_dwordx2 v[236:237], v238, s[96:99], s51 offen sc1\ns_mov_b64 exec, -1\nxW vmcnt(0)\n"
"v_cmp_ne_u64 vcc, s[68:69], v[236:237]\ns_and_b32 s39, vcc_lo, 0xffffffff\ns_and_b32 s39, s39, s54\ns_cbranch_scc0 RETA1_%=\ns_sleep 4\ns_sub_u32 s52, s52, 1\n"
"s_cmp_lg_u32 s52, 0\ns_cbranch_scc1 SPLA1_%=\ns_branch RETA1_%=\nSPINA2_%=:\ns_cmp_lg_u32 %[wid], 0\ns_cbranch_scc1 RETA2_%=\n"
"s_mov_b32 s52, 200000\nSPLA2_%=:\ns_mov_b32 exec_lo, 0\nbuffer_load_dwordx2 v[236:237], v238, s[96:99], s51 offen sc1\ns_mov_b64 exec, -1\nxW vmcnt(0)\n"
"v_cmp_ne_u64 vcc, s[68:69], v[236:237]\ns_and_b32 s39, vcc_hi, 0xffffffff\ns_and_b32 s39, s39, s54\ns_cbranch_scc0 RETA2_%=\ns_sleep 4\ns_sub_u32 s52, s52, 1\n"
"s_cmp_lg_u32 s52, 0\ns_cbranch_scc1 SPLA2_%=\ns_branch RETA2_%=\nSPINH_%=:\ns_cmp_lg_u32 %[wid], 0\ns_cbranch_scc1 RETH_%=\n"
"s_mov_b32 s52, 200000\nSPLH_%=:\nbuffer_load_dwordx2 v[236:237], v238, s[96:99], s51 offen sc1\nTCH2_3_%=:\ns_cbranch_execz TDONE2_%=\nxW vmcnt(0)\n"
"v_cmp_ne_u64 vcc, s[68:69], v[236:237]\ns_andn2_b64 s[60:61], vcc, s[54:55]\ns_cbranch_scc0 RETH_%=\ns_sleep 4\ns_sub_u32 s52, s52, 1\ns_cmp_lg_u32 s52, 0\n"
"s_cbranch_scc1 SPLH_%=\ns_branch RETH_%=\nSPINY0_%=:\ns_cmp_lg_u32 %[wid], 0\ns_cbranch_scc1 RETY0_%=\ns_mov_b32 s52, 200000\n"
"SPLY0_%=:\nbuffer_load_dwordx2 v[236:237], v238, s[96:99], s37 offen sc1\nxW vmcnt(0)\nv_cmp_ne_u64 vcc, s[68:69], v[236:237]\ns_and_b32 s38, vcc_lo, 15\ns_cbranch_scc0 RETY0_%=\n"
"s_sleep 4\ns_sub_u32 s52, s52, 1\ns_cmp_lg_u32 s52, 0\ns_cbranch_scc1 SPLY0_%=\ns_branch RETY0_%=\nSPINY1_%=:\n"
"s_cmp_lg_u32 %[wid], 0\ns_cbranch_scc1 RETY1_%=\ns_mov_b32 s52, 200000\nSPLY1_%=:\nbuffer_load_dwordx2 v[236:237], v238, s[96:99], s37 offen sc1\nxW vmcnt(0)\n"
"v_cmp_ne_u64 vcc, s[68:69], v[236:237]\ns_and_b32 s38, vcc_lo, 240\ns_cbranch_scc0 RETY1_%=\ns_sleep 4\ns_sub_u32 s52, s52, 1\ns_cmp_lg_u32 s52, 0\n"
"s_cbranch_scc1 SPLY1_%=\ns_branch RETY1_%=\nSPINY2_%=:\ns_cmp_lg_u32 %[wid], 0\ns_cbranch_scc1 RETY2_%=\ns_mov_b32 s52, 200000\n"
"SPLY2_%=:\nbuffer_load_dwordx2 v[236:237], v238, s[96:99], s37 offen sc1\nxW vmcnt(0)\nv_cmp_ne_u64 vcc, s[68:69], v[236:237]\ns_and_b32 s38, vcc_lo, 3840\ns_cbranch_scc0 RETY2_%=\n"
"s_sleep 4\ns_sub_u32 s52, s52, 1\ns_cmp_lg_u32 s52, 0\ns_cbranch_scc1 SPLY2_%=\ns_branch RETY2_%=\nSPINB1_%=:\n"
"s_cmp_lg_u32 %[wid], 0\ns_cbranch_scc1 RETB1_%=\ns_mov_b32 s52, 200000\nSPLB1_%=:\ns_mov_b32 exec_hi, 0\nbuffer_load_dwordx2 v[236:237], v238, s[96:99], s51 offen sc1\n"
"s_mov_b64 exec, -1\nxW vmcnt(0)\nv_cmp_ne_u64 vcc, s[68:69], v[236:237]\ns_and_b32 s39, vcc_lo, 0xffffffff\ns_and_b32 s39, s39, s54\ns_cbranch_scc0 RETB1_%=\n"
"s_sleep 4\ns_sub_u32 s52, s52, 1\ns_cmp_lg_u32 s52, 0\ns_cbranch_scc1 SPLB1_%=\ns_branch RETB1_%=\nSPINB2_%=:\n"
"s_cmp_lg_u32 %[wid], 0\ns_cbranch_scc1 RETB2_%=\ns_mov_b32 s52, 200000\nSPLB2_%=:\ns_mov_b32 exec_lo, 0\nbuffer_load_dwordx2 v[236:237], v238, s[96:99], s51 offen sc1\n"
"s_mov_b64 exec, -1\nxW vmcnt(0)\nv_cmp_ne_u64 vcc, s[68:69], v[236:237]\ns_and_b32 s39, vcc_hi, 0xffffffff\ns_and_b32 s39, s39, s54\ns_cbranch_scc0 RETB2_%=\n"
"s_sleep 4\ns_sub_u32 s52, s52, 1\ns_cmp_lg_u32 s52, 0\ns_cbranch_scc1 SPLB2_%=\ns_branch RETB2_%=\nSKIP2_%=:\n"
"xA m0,s35,160256\nxA s39,s35,1060096\nxL v224,s[96:99],s39 offen sc1 lds\ns_mov_b32 exec_hi, 0\nbuffer_load_dwordx2 v[236:237], v238, s[96:99], s51 offen sc1\ns_mov_b64 exec, -1\n"
"v_lshlrev_b32 v148, 16, v184\nv_and_b32 v149, 0xffff0000, v184\nv_lshlrev_b32 v150, 16, v185\nv_and_b32 v151, 0xffff0000, v185\nv_lshlrev_b32 v152, 16, v186\nv_and_b32 v153, 0xffff0000, v186\n"
"v_lshlrev_b32 v154, 16, v187\nv_and_b32 v155, 0xffff0000, v187\nv_lshlrev_b32 v156, 16, v188\nv_and_b32 v157, 0xffff0000, v188\nv_lshlrev_b32 v158, 16, v189\nv_and_b32 v159, 0xffff0000, v189\n"
"v_lshlrev_b32 v160, 16, v190\nv_and_b32 v161, 0xffff0000, v190\nv_lshlrev_b32 v162, 16, v191\nv_and_b32 v163, 0xffff0000, v191\nv_lshlrev_b32 v164, 16, v246\nv_and_b32 v165, 0xffff0000, v246\n"
"v_lshlrev_b32 v166, 16, v247\nv_and_b32 v167, 0xffff0000, v247\ns_cmp_lt_u32 %[wid], 2\ns_cselect_b32 s24, s16, s20\ns_cselect_b32 s25, s17, s21\ns_mov_b32 s26, s18\n"
"s_mov_b32 s27, s19\ns_and_b32 s45, %[wid], 1\ns_lshr_b32 s44, %[wid], 1\ns_mul_i32 s35, s45, 442368\ns_xor_b32 s39, s43, 1\ns_lshl_b32 s39, s39, 9\n"
"xA s35,s35,s39\ns_mul_i32 s39, s71, 331776\nxA s56,s86,s39\ns_addc_u32 s57, s87, 0\ns_and_b32 s57, s57, 0xffff\ns_mov_b32 s58, 331776\n"
"s_mov_b32 s59, 0x20000\ns_mul_i32 s34, %[wid], 27648\ns_mul_i32 s39, s43, 36864\nxA s34,s34,s39\ns_mul_i32 s38, s43, 331776\nxA s40,s34,221184\n"
"s_cmp_ge_u32 %[wid], 3\ns_cselect_b32 s39, s38, 0\ns_sub_u32 s40, s40, s39\nxA s41,s34,230400\ns_cmp_ge_u32 %[wid], 3\ns_cselect_b32 s39, s38, 0\n"
"s_sub_u32 s41, s41, s39\nxA s42,s34,239616\ns_cmp_ge_u32 %[wid], 2\ns_cselect_b32 s39, s38, 0\ns_sub_u32 s42, s42, s39\ns_lshl_b32 s46, s70, 1\n"
"xA s46,s46,s44\ns_mul_i32 s46, s46, 1179648\ns_mul_i32 s39, s71, 9216\nxA s46,s46,s39\ns_lshl_b32 s38, s45, 9\nxA s46,s46,s38\n"
"xA s46,s46,45613056\ns_lshl_b32 s47, s70, 2\nxA s47,s47,%[wid]\ns_mul_i32 s47, s47, 36864\nxA s47,s47,5505024\nv_and_b32 v235, 1, v230\n"
"v_lshrrev_b32 v190, 1, v230\nv_lshl_add_u32 v191, v235, 2, v232\nv_lshlrev_b32 v191, 6, v191\nv_lshl_add_u32 v191, v233, 3, v191\nv_lshl_add_u32 v191, v235, 5, v191\nv_lshl_add_u32 v208, v190, 10, v191\n"
"v_add_u32 v208, s38, v208\nv_xor_b32 v209, 32, v208\ns_lshl_b32 s39, s45, 5\nv_lshl_add_u32 v214, v190, 9, v191\nv_xor_b32 v214, s39, v214\nv_add_u32 v210, 61440, v208\n"
"v_add_u32 v211, 61440, v209\nv_add_u32 v215, 61440, v214\nv_add_u32 v212, 122880, v208\nv_add_u32 v213, 122880, v209\nv_add_u32 v216, 122880, v214\nv_lshrrev_b32 v235, 3, v231\n"
"v_mul_u32_u24 v235, 147456, v235\nv_lshl_add_u32 v235, v230, 7, v235\nv_and_b32 v190, 7, v255\nv_lshl_add_u32 v220, v190, 4, v235\nv_add_u32 v221, 294912, v220\nv_add_u32 v222, 589824, v220\n"
"v_add_u32 v223, 884736, v220\ns_mul_i32 s39, s44, 12288\nxA s39,s39,0\nv_add_u32 v225, s39, v224\ns_lshl_b32 s39, s44, 12\nxA s39,s39,0\n"
"v_add_u32 v228, s39, v224\ns_lshl_b32 s38, s45, 13\nv_add_u32 v226, s38, v228\ns_lshl_b32 s38, s45, 3\nxA s38,s38,65536\nv_add_u32 v227, s38, v228\n"
"v_add_u32 v229, 65536, v228\ns_lshr_b32 s38, s71, 1\ns_mul_i32 s39, s38, 663552\nxA s56,s86,s39\ns_addc_u32 s57, s87, 0\ns_and_b32 s57, s57, 0xffff\n"
"s_mov_b32 s58, 663552\ns_mov_b32 s59, 0x20000\ns_lshl_b32 s46, s70, 1\nxA s46,s46,s44\ns_mul_i32 s46, s46, 589824\ns_mul_i32 s39, s38, 9216\n"
"xA s46,s46,s39\ns_lshl_b32 s39, s45, 9\nxA s46,s46,s39\ns_mul_i32 s39, s43, 5120\nxA s46,s46,s39\nxA s46,s46,45613056\n"
"s_xor_b32 s52, s43, 0\ns_mul_i32 s34, %[wid], 27648\ns_mul_i32 s39, s52, 368640\nxA s34,s34,s39\ns_mul_i32 s39, s43, 640\nxA s34,s34,s39\n"
"s_mul_i32 s38, s52, 331776\nxA s40,s34,221184\ns_cmp_ge_u32 %[wid], 3\ns_cselect_b32 s39, s38, 0\ns_sub_u32 s40, s40, s39\nxA s41,s34,230400\n"
"s_cmp_ge_u32 %[wid], 3\ns_cselect_b32 s39, s38, 0\ns_sub_u32 s41, s41, s39\nxA s42,s34,239616\ns_cmp_ge_u32 %[wid], 2\ns_cselect_b32 s39, s38, 0\n"
"s_sub_u32 s42, s42, s39\ns_xor_b32 s52, s43, 1\ns_mul_i32 s28, %[wid], 27648\ns_mul_i32 s39, s52, 368640\nxA s28,s28,s39\ns_mul_i32 s39, s43, 640\n"
"xA s28,s28,s39\ns_mul_i32 s38, s52, 331776\nxA s29,s28,221184\ns_cmp_ge_u32 %[wid], 3\ns_cselect_b32 s39, s38, 0\ns_sub_u32 s29, s29, s39\n"
"xA s30,s28,230400\ns_cmp_ge_u32 %[wid], 3\ns_cselect_b32 s39, s38, 0\ns_sub_u32 s30, s30, s39\nxA s31,s28,239616\ns_cmp_ge_u32 %[wid], 2\n"
"s_cselect_b32 s39, s38, 0\ns_sub_u32 s31, s31, s39\nv_lshrrev_b32 v235, 3, v231\nv_mul_u32_u24 v235, 73728, v235\nv_lshl_add_u32 v235, v230, 7, v235\nv_and_b32 v190, 7, v255\n"
"v_lshl_add_u32 v220, v190, 4, v235\nv_add_u32 v221, 147456, v220\nv_add_u32 v222, 294912, v220\nv_add_u32 v223, 442368, v220\nv_lshrrev_b32 v235, 3, v231\nv_mul_u32_u24 v235, 73728, v235\n"
"v_lshl_add_u32 v235, v190, 4, v235\nv_lshrrev_b32 v190, 1, v230\nv_lshl_add_u32 v235, v190, 7, v235\nv_and_b32 v190, 1, v230\nv_mul_u32_u24 v191, 147456, v190\nv_add_u32 v235, v235, v191\n"
"v_lshrrev_b32 v234, 2, v234\nv_xor_b32 v234, v234, v233\nv_lshrrev_b32 v191, 2, v255\nv_mul_u32_u24 v191, 1152, v191\nv_lshl_add_u32 v232, v234, 4, v191\nv_xor_b32 v190, s45, v190\n"
"v_lshlrev_b32 v190, 1, v190\nv_add_u32 v190, v190, v233\nv_and_b32 v191, 1, v233\nv_lshlrev_b32 v191, 2, v191\nv_lshrrev_b32 v233, 1, v233\nv_add_u32 v191, v191, v233\n"
"v_sub_u32 v191, v191, v190\nv_lshlrev_b32 v233, 3, v191\nv_lshlrev_b32 v191, 3, v255\nv_sub_u32 v229, v229, v191\ns_lshl_b32 s39, s45, 9\nv_add_u32 v227, s39, v229\n"
"v_lshlrev_b32 v234, 7, v255\nv_cmp_gt_u32 vcc, 32, v255\nv_and_b32 v176, 7, v255\nv_lshlrev_b32 v176, 3, v176\nv_cndmask_b32 v168, 0, 1.0, vcc\nv_add_u32 v176, 159744, v176\n"
"v_lshrrev_b32 v175, 5, v255\nv_lshlrev_b32 v175, 11, v175\nv_bfe_u32 v171, v255, 3, 1\nv_lshl_add_u32 v175, v171, 8, v175\nv_and_b32 v171, 7, v255\nv_lshl_add_u32 v175, v171, 4, v175\n"
"v_bfe_u32 v171, v255, 4, 1\nv_lshl_add_u32 v175, v171, 3, v175\ns_lshl_b32 s37, s70, 3\ns_lshr_b32 s39, s71, 1\nxA s37,s37,s39\ns_mul_i32 s37, s37, 18432\n"
"s_lshl_b32 s39, %[wid], 9\nxA s37,s37,s39\ns_and_b32 s39, s71, 1\ns_lshl_b32 s39, s39, 7\nxA s37,s37,s39\nxA s37,s37,5505024\n"
"xW vmcnt(2)\ns_mov_b64 exec, 1\nbuffer_store_dwordx2 v[242:243], off, s[96:99], s50 sc1\ns_mov_b64 exec, -1\ns_mov_b64 exec, 0\ns_cmp_eq_u32 %[wid], 1\n"
"s_cbranch_scc1 TCH1_1_%=\ns_cmp_eq_u32 %[wid], 2\ns_cbranch_scc1 TCH1_2_%=\ns_cmp_eq_u32 %[wid], 3\ns_cbranch_scc1 TCH1_3_%=\nTDONE1_%=:\n"
"s_mov_b64 exec, -1\ns_lshl_b32 s39, %[bid], 2\nxA s39,s39,%[wid]\ns_cmp_eq_u32 s39, 1\ns_cbranch_scc1 BUMP_%=\nBUMPR_%=:\n"
"xW vmcnt(1)\nv_cmp_ne_u64 vcc, s[68:69], v[236:237]\ns_and_b64 vcc, vcc, s[100:101]\ns_and_b32 s39, vcc_lo, 0xffffffff\ns_and_b32 s39, s39, s54\ns_cbranch_scc1 SPINA1_%=\n"
"RETA1_%=:\ns_barrier\nxA s38,s49,0\nxL a[128:131],v239,s[96:99],s38 offen sc1\nxA s38,s49,36864\nxL a[132:135],v239,s[96:99],s38 offen sc1\n"
"xA s38,s49,1024\nxL a[136:139],v239,s[96:99],s38 offen sc1\nxA s38,s49,37888\nxL a[140:143],v239,s[96:99],s38 offen sc1\ns_mov_b32 exec_lo, 0\nbuffer_load_dwordx2 v[236:237], v238, s[96:99], s51 offen sc1\n"
"s_mov_b64 exec, -1\nxA s38,s49,2048\nxL a[144:147],v239,s[96:99],s38 offen sc1\nxA s38,s49,38912\nxL a[148:151],v239,s[96:99],s38 offen sc1\nxA s38,s49,3072\n"
"xL a[152:155],v239,s[96:99],s38 offen sc1\nxA s38,s49,39936\nxL a[156:159],v239,s[96:99],s38 offen sc1\nxW vmcnt(4)\nv_cmp_ne_u64 vcc, s[68:69], v[236:237]\ns_and_b64 vcc, vcc, s[100:101]\n"
"s_and_b32 s39, vcc_hi, 0xffffffff\ns_and_b32 s39, s39, s54\ns_cbranch_scc1 SPINA2_%=\nRETA2_%=:\ns_barrier\nxA s38,s49,4096\n"
"xL a[160:163],v239,s[96:99],s38 offen sc1\nxA s38,s49,40960\nxL a[164:167],v239,s[96:99],s38 offen sc1\nxA s38,s49,5120\nxL a[168:171],v239,s[96:99],s38 offen sc1\nxA s38,s49,41984\n"
"xL a[172:175],v239,s[96:99],s38 offen sc1\nxA s38,s49,6144\nxL a[176:179],v239,s[96:99],s38 offen sc1\nxA s38,s49,43008\nxL a[180:183],v239,s[96:99],s38 offen sc1\nxA s38,s49,7168\n"
"xL a[184:187],v239,s[96:99],s38 offen sc1\nxA s38,s49,44032\nxL a[188:191],v239,s[96:99],s38 offen sc1\nxA s38,s49,8192\nxL a[192:195],v239,s[96:99],s38 offen sc1\nxA s38,s49,45056\n"
"xL a[196:199],v239,s[96:99],s38 offen sc1\ns_lshl_b32 s51, s70, 9\nxA s51,s51,1196032\ns_lshl_b32 s39, s71, 2\ns_lshl_b64 s[54:55], 15, s39\nxA s39,s39,%[wid]\n"
"s_lshl_b32 s39, s39, 3\nxA s50,s51,s39\nv_add_u32 v171, 65536, v241\nxR v[4:7],v171 offset:0\nxR v[12:15],v171 offset:512\nxR v[20:23],v171 offset:1024\n"
"xR v[28:31],v171 offset:1536\nxR v[36:39],v171 offset:2048\nxR v[44:47],v171 offset:2304\nxR v[52:55],v171 offset:2816\nxR v[60:63],v171 offset:3328\nxR v[68:71],v171 offset:3840\n"
"xR v[76:79],v171 offset:4352\nxR v[84:87],v171 offset:4608\nxR v[92:95],v171 offset:5120\nxR v[100:103],v171 offset:5632\nxR v[108:111],v171 offset:6144\nxR v[116:119],v171 offset:6656\n"
"xW vmcnt(10)\nxM v[128:131],v[200:203],a[128:131],0\nxM v[128:131],v[204:207],a[136:139],v[128:131]\n"
"xM v[132:135],v[200:203],a[144:147],0\nxM v[132:135],v[204:207],a[152:155],v[132:135]\nxM v[128:131],v[200:203],a[132:135],v[128:131]\n"
"xM v[128:131],v[204:207],a[140:143],v[128:131]\nxM v[132:135],v[200:203],a[148:151],v[132:135]\nxM v[132:135],v[204:207],a[156:159],v[132:135]\nxW vmcnt(0)\n"
"xM v[136:139],v[200:203],a[160:163],0\nxM v[136:139],v[204:207],a[168:171],v[136:139]\nxM v[140:143],v[200:203],a[176:179],0\n"
"xM v[140:143],v[204:207],a[184:187],v[140:143]\nxM v[144:147],v[200:203],a[192:195],0\nxM v[136:139],v[200:203],a[164:167],v[136:139]\n"
"xM v[136:139],v[204:207],a[172:175],v[136:139]\nxM v[140:143],v[200:203],a[180:183],v[140:143]\nxM v[140:143],v[204:207],a[188:191],v[140:143]\n"
"xM v[144:147],v[200:203],a[196:199],v[144:147]\ns_nop 7\nxW lgkmcnt(14)\nv_pk_fma_f32 v[128:129], v[4:5], v[128:129], v[148:149]\nv_pk_fma_f32 v[130:131], v[6:7], v[130:131], v[150:151]\nxW lgkmcnt(13)\n"
"v_pk_fma_f32 v[132:133], v[12:13], v[132:133], v[152:153]\nv_pk_fma_f32 v[134:135], v[14:15], v[134:135], v[154:155]\nxW lgkmcnt(12)\nv_pk_fma_f32 v[136:137], v[20:21], v[136:137], v[156:157]\nv_pk_fma_f32 v[138:139], v[22:23], v[138:139], v[158:159]\nxW lgkmcnt(11)\n"
"v_pk_fma_f32 v[140:141], v[28:29], v[140:141], v[160:161]\nv_pk_fma_f32 v[142:143], v[30:31], v[142:143], v[162:163]\nxW lgkmcnt(10)\nv_pk_fma_f32 v[144:145], v[36:37], v[144:145], v[164:165]\nv_pk_fma_f32 v[146:147], v[38:39], v[146:147], v[166:167]\nv_pk_mul_f32 v[144:145], v[144:145], v[168:169] op_sel:[0,0] op_sel_hi:[1,0]\n"
"v_pk_mul_f32 v[146:147], v[146:147], v[168:169] op_sel:[0,0] op_sel_hi:[1,0]\nv_pk_add_f32 v[178:179], v[128:129], v[130:131]\nv_pk_add_f32 v[178:179], v[178:179], v[132:133]\nv_pk_add_f32 v[178:179], v[178:179], v[134:135]\nv_pk_add_f32 v[178:179], v[178:179], v[136:137]\nv_pk_add_f32 v[178:179], v[178:179], v[138:139]\n"
"v_pk_add_f32 v[178:179], v[178:179], v[140:141]\nv_pk_add_f32 v[178:179], v[178:179], v[142:143]\nv_pk_add_f32 v[178:179], v[178:179], v[144:145]\nv_pk_add_f32 v[178:179], v[178:179], v[146:147]\nv_add_f32 v169, v178, v179\ns_nop 1\n"
"v_add_f32_dpp v169, v169, v169 row_ror:8 row_mask:0xf bank_mask:0xf\nv_mov_b32 v171, v169\ns_nop 1\nv_permlane16_swap_b32 v169, v171\nv_add_f32 v169, v169, v171\nv_mov_b32 v171, v169\n"
"s_nop 1\nv_permlane32_swap_b32 v169, v171\nv_add_f32 v169, v169, v171\nxU v169,0x3be38e39,v169\nv_pk_add_f32 v[148:149], v[128:129], v[168:169] op_sel:[0,1] op_sel_hi:[1,1] neg_lo:[0,1] neg_hi:[0,1]\nv_pk_add_f32 v[150:151], v[130:131], v[168:169] op_sel:[0,1] op_sel_hi:[1,1] neg_lo:[0,1] neg_hi:[0,1]\n"
"v_pk_add_f32 v[152:153], v[132:133], v[168:169] op_sel:[0,1] op_sel_hi:[1,1] neg_lo:[0,1] neg_hi:[0,1]\nv_pk_add_f32 v[154:155], v[134:135], v[168:169] op_sel:[0,1] op_sel_hi:[1,1] neg_lo:[0,1] neg_hi:[0,1]\nv_pk_add_f32 v[156:157], v[136:137], v[168:169] op_sel:[0,1] op_sel_hi:[1,1] neg_lo:[0,1] neg_hi:[0,1]\nv_pk_add_f32 v[158:159], v[138:139], v[168:169] op_sel:[0,1] op_sel_hi:[1,1] neg_lo:[0,1] neg_hi:[0,1]\nv_pk_add_f32 v[160:161], v[140:141], v[168:169] op_sel:[0,1] op_sel_hi:[1,1] neg_lo:[0,1] neg_hi:[0,1]\nv_pk_add_f32 v[162:163], v[142:143], v[168:169] op_sel:[0,1] op_sel_hi:[1,1] neg_lo:[0,1] neg_hi:[0,1]\n"
"v_pk_add_f32 v[164:165], v[144:145], v[168:169] op_sel:[0,1] op_sel_hi:[1,1] neg_lo:[0,1] neg_hi:[0,1]\nv_pk_add_f32 v[166:167], v[146:147], v[168:169] op_sel:[0,1] op_sel_hi:[1,1] neg_lo:[0,1] neg_hi:[0,1]\nv_pk_mul_f32 v[164:165], v[164:165], v[168:169] op_sel:[0,0] op_sel_hi:[1,0]\nv_pk_mul_f32 v[166:167], v[166:167], v[168:169] op_sel:[0,0] op_sel_hi:[1,0]\nv_pk_mul_f32 v[178:179], v[148:149], v[148:149]\nv_pk_fma_f32 v[178:179], v[150:151], v[150:151], v[178:179]\n"
"v_pk_fma_f32 v[178:179], v[152:153], v[152:153], v[178:179]\nv_pk_fma_f32 v[178:179], v[154:155], v[154:155], v[178:179]\nv_pk_fma_f32 v[178:179], v[156:157], v[156:157], v[178:179]\nv_pk_fma_f32 v[178:179], v[158:159], v[158:159], v[178:179]\nv_pk_fma_f32 v[178:179], v[160:161], v[160:161], v[178:179]\nv_pk_fma_f32 v[178:179], v[162:163], v[162:163], v[178:179]\n"
"v_pk_fma_f32 v[178:179], v[164:165], v[164:165], v[178:179]\nv_pk_fma_f32 v[178:179], v[166:167], v[166:167], v[178:179]\nv_add_f32 v170, v178, v179\ns_nop 1\nv_add_f32_dpp v170, v170, v170 row_ror:8 row_mask:0xf bank_mask:0xf\nv_mov_b32 v171, v170\n"
"s_nop 1\nv_permlane16_swap_b32 v170, v171\nv_add_f32 v170, v170, v171\nv_mov_b32 v171, v170\ns_nop 1\nv_permlane32_swap_b32 v170, v171\n"
"v_add_f32 v170, v170, v171\nv_mov_b32 v148, v169\nv_mov_b32 v149, v170\ns_lshl_b32 s39, %[wid], 6\nv_add_u32 v171, s39, v176\nds_write_b64 v171, v[148:149]\n"
"xC v244,v128,v129\nxC v245,v130,v131\nxC v246,v132,v133\nxC v247,v134,v135\nxC v248,v136,v137\nxC v249,v138,v139\n"
"xC v250,v140,v141\nxC v251,v142,v143\nxC v252,v144,v145\nxC v253,v146,v147\nxW lgkmcnt(0)\ns_barrier\n"
"ds_read_b64 v[148:149], v176 offset:0\nds_read_b64 v[150:151], v176 offset:64\nds_read_b64 v[152:153], v176 offset:128\nds_read_b64 v[154:155], v176 offset:192\nxW lgkmcnt(0)\nv_add_f32 v172, v148, v150\n"
"v_add_f32 v171, v152, v154\nv_add_f32 v172, v172, v171\nxU v172,0x3e800000,v172\nv_add_f32 v170, v149, v151\nv_add_f32 v171, v153, v155\nv_add_f32 v170, v170, v171\n"
"v_mov_b32 v169, 0\nv_sub_f32 v171, v148, v172\nv_fmac_f32 v169, v171, v171\nv_sub_f32 v171, v150, v172\nv_fmac_f32 v169, v171, v171\nv_sub_f32 v171, v152, v172\n"
"v_fmac_f32 v169, v171, v171\nv_sub_f32 v171, v154, v172\nv_fmac_f32 v169, v171, v171\nv_fmac_f32 v170, 0x43100000, v169\nv_mov_b32 v171, 0x3727c5ac\nv_fmac_f32 v171, 0x3ae38e39, v170\n"
"v_rsq_f32 v173, v171\ns_nop 0\nxU v174,v172,v173\nv_pk_fma_f32 v[128:129], v[128:129], v[172:173], v[174:175] op_sel:[0,1,0] op_sel_hi:[1,1,0] neg_lo:[0,0,1] neg_hi:[0,0,1]\nv_pk_fma_f32 v[128:129], v[128:129], v[84:85], v[44:45]\nv_pk_fma_f32 v[130:131], v[130:131], v[172:173], v[174:175] op_sel:[0,1,0] op_sel_hi:[1,1,0] neg_lo:[0,0,1] neg_hi:[0,0,1]\n"
"v_pk_fma_f32 v[130:131], v[130:131], v[86:87], v[46:47]\nxC v148,v128,v129\nxC v149,v130,v131\nbuffer_store_dwordx2 v[148:149], v175, s[96:99], s37 offen sc1\nxA s37,s37,4096\nv_pk_fma_f32 v[132:133], v[132:133], v[172:173], v[174:175] op_sel:[0,1,0] op_sel_hi:[1,1,0] neg_lo:[0,0,1] neg_hi:[0,0,1]\n"
"v_pk_fma_f32 v[132:133], v[132:133], v[92:93], v[52:53]\nv_pk_fma_f32 v[134:135], v[134:135], v[172:173], v[174:175] op_sel:[0,1,0] op_sel_hi:[1,1,0] neg_lo:[0,0,1] neg_hi:[0,0,1]\nv_pk_fma_f32 v[134:135], v[134:135], v[94:95], v[54:55]\nxC v150,v132,v133\nxC v151,v134,v135\nbuffer_store_dwordx2 v[150:151], v175, s[96:99], s37 offen sc1\n"
"xA s37,s37,4096\nv_pk_fma_f32 v[136:137], v[136:137], v[172:173], v[174:175] op_sel:[0,1,0] op_sel_hi:[1,1,0] neg_lo:[0,0,1] neg_hi:[0,0,1]\nv_pk_fma_f32 v[136:137], v[136:137], v[100:101], v[60:61]\nv_pk_fma_f32 v[138:139], v[138:139], v[172:173], v[174:175] op_sel:[0,1,0] op_sel_hi:[1,1,0] neg_lo:[0,0,1] neg_hi:[0,0,1]\nv_pk_fma_f32 v[138:139], v[138:139], v[102:103], v[62:63]\nxC v152,v136,v137\n"
"xC v153,v138,v139\nbuffer_store_dwordx2 v[152:153], v175, s[96:99], s37 offen sc1\nxA s37,s37,4096\nv_pk_fma_f32 v[140:141], v[140:141], v[172:173], v[174:175] op_sel:[0,1,0] op_sel_hi:[1,1,0] neg_lo:[0,0,1] neg_hi:[0,0,1]\nv_pk_fma_f32 v[140:141], v[140:141], v[108:109], v[68:69]\nv_pk_fma_f32 v[142:143], v[142:143], v[172:173], v[174:175] op_sel:[0,1,0] op_sel_hi:[1,1,0] neg_lo:[0,0,1] neg_hi:[0,0,1]\n"
"v_pk_fma_f32 v[142:143], v[142:143], v[110:111], v[70:71]\nxC v154,v140,v141\nxC v155,v142,v143\nbuffer_store_dwordx2 v[154:155], v175, s[96:99], s37 offen sc1\nxA s37,s37,4096\nv_pk_fma_f32 v[144:145], v[144:145], v[172:173], v[174:175] op_sel:[0,1,0] op_sel_hi:[1,1,0] neg_lo:[0,0,1] neg_hi:[0,0,1]\n"
"v_pk_fma_f32 v[144:145], v[144:145], v[116:117], v[76:77]\nv_pk_fma_f32 v[146:147], v[146:147], v[172:173], v[174:175] op_sel:[0,1,0] op_sel_hi:[1,1,0] neg_lo:[0,0,1] neg_hi:[0,0,1]\nv_pk_fma_f32 v[146:147], v[146:147], v[118:119], v[78:79]\nxC v156,v144,v145\nxC v157,v146,v147\ns_mov_b32 exec_hi, 0\n"
"buffer_store_dwordx2 v[156:157], v175, s[96:99], s37 offen sc1\ns_mov_b32 exec_hi, -1\nxW vmcnt(0)\ns_barrier\ns_cmp_lg_u32 %[wid], 1\ns_cbranch_scc1 HSG_%=\n"
"v_lshlrev_b32 v171, 3, v255\ns_sub_u32 s50, s50, 8\ns_mov_b64 exec, 15\nbuffer_store_dwordx2 v[242:243], v171, s[96:99], s50 offen sc1\ns_mov_b64 exec, -1\nHSG_%=:\n"
"xA m0,s32,73728\nxA s39,s33,0\nxL v217,s[16:19],s39 offen lds\nxA m0,s32,74752\nxA s39,s33,73728\nxL v217,s[16:19],s39 offen lds\n"
"xA m0,s32,75776\nxA s39,s33,147456\nxL v217,s[16:19],s39 offen lds\nxA m0,s32,86016\nxA s39,s33,0\nxL v217,s[20:23],s39 offen lds\n"
"xA m0,s32,87040\nxA s39,s33,73728\nxL v217,s[20:23],s39 offen lds\nxA m0,s32,88064\nxA s39,s33,147456\nxL v217,s[20:23],s39 offen lds\n"
"xA m0,s32,98304\nxA s39,s33,884736\nxL v217,s[16:19],s39 offen lds\nxA m0,s32,99328\nxA s39,s33,958464\nxL v217,s[16:19],s39 offen lds\n"
"xA m0,s32,100352\nxA s39,s33,1032192\nxL v217,s[16:19],s39 offen lds\nxA m0,s32,110592\nxA s39,s33,884736\nxL v217,s[20:23],s39 offen lds\n"
"xA m0,s32,111616\nxA s39,s33,958464\nxL v217,s[20:23],s39 offen lds\nxA m0,s32,112640\nxA s39,s33,1032192\nxL v217,s[20:23],s39 offen lds\n"
"xA m0,s32,122880\nxA s39,s33,1769472\nxL v217,s[16:19],s39 offen lds\nxA m0,s32,123904\nxA s39,s33,1843200\nxL v217,s[16:19],s39 offen lds\n"
"xA m0,s32,124928\nxA s39,s33,1916928\nxL v217,s[16:19],s39 offen lds\nbuffer_load_dwordx2 v[236:237], v238, s[96:99], s51 offen sc1\nxW vmcnt(0)\nv_cmp_ne_u64 vcc, s[68:69], v[236:237]\n"
"s_and_b64 vcc, vcc, s[100:101]\ns_andn2_b64 s[60:61], vcc, s[54:55]\ns_cbranch_scc1 SPINH_%=\nRETH_%=:\ns_barrier\nxA m0,s36,0\n"
"xA s39,s47,0\nxL v224,s[96:99],s39 offen sc1 lds\nxA m0,s36,3072\nxA s39,s47,18432\nxL v224,s[96:99],s39 offen sc1 lds\nxA m0,s36,1024\n"
"xA s39,s47,1024\nxL v224,s[96:99],s39 offen sc1 lds\nxA m0,s36,4096\nxA s39,s47,19456\nxL v224,s[96:99],s39 offen sc1 lds\nxA m0,s36,2048\n"
"xA s39,s47,2048\nxL v224,s[96:99],s39 offen sc1 lds\nxA m0,s36,5120\nxA s39,s47,20480\nxL v224,s[96:99],s39 offen sc1 lds\nxA m0,s36,24576\n"
"xA s39,s47,3072\nxL v224,s[96:99],s39 offen sc1 lds\nxA m0,s36,27648\nxA s39,s47,21504\nxL v224,s[96:99],s39 offen sc1 lds\nxA m0,s36,25600\n"
"xA s39,s47,4096\nxL v224,s[96:99],s39 offen sc1 lds\nxA m0,s36,28672\nxA s39,s47,22528\nxL v224,s[96:99],s39 offen sc1 lds\nxA m0,s36,26624\n"
"xA s39,s47,5120\nxL v224,s[96:99],s39 offen sc1 lds\nTCH1_1_%=:\ns_cbranch_execz TC1_1_1_%=\nxA m0,s36,29696\nxA s39,s47,23552\n"
"xL v224,s[96:99],s39 offen sc1 lds\ns_lshl_b32 s49, s70, 4\nxA s49,s49,s71\ns_mul_i32 s39, s49, 9216\nxA s12,s92,s39\ns_addc_u32 s13, s93, 0\n"
"s_and_b32 s13, s13, 0xffff\ns_mov_b32 s14, 9216\ns_mov_b32 s15, 0x20000\ns_mul_i32 s49, s49, 73728\nxA s49,s49,45613056\ns_lshl_b32 s51, s70, 11\n"
"xA s51,s51,1163264\ns_lshr_b32 s39, %[wid], 1\ns_lshl_b32 s39, s39, 10\nxA s50,s51,s39\ns_lshl_b32 s39, s71, 4\nxA s50,s50,s39\n"
"s_and_b32 s39, %[wid], 1\ns_lshl_b32 s39, s39, 3\nxA s50,s50,s39\ns_lshr_b32 s39, s71, 3\ns_lshl_b32 s39, s39, 10\nxA s51,s51,s39\n"
"s_lshl_b32 s39, s71, 1\ns_lshl_b32 s54, 3, s39\ns_not_b32 s54, s54\ns_xor_b32 s52, s43, s44\ns_mul_i32 s48, s45, 55296\ns_mul_i32 s39, s52, 368640\n"
"xA s48,s48,s39\ns_lshl_b32 s39, s43, 6\nxA s39,s39,512\nxA s48,s48,s39\ns_and_b32 s52, s52, s45\ns_mul_i32 s38, s52, 331776\n"
"xA s60,s48,239616\ns_sub_u32 s60, s60, s38\nxA s61,s48,258048\ns_sub_u32 s61, s61, s38\ns_lshl_b32 s52, s70, 4\nxA s52,s52,s71\n"
"s_mul_i32 s62, s52, 73728\nxA s62,s62,83361792\ns_lshl_b32 s53, s52, 7\ns_lshl_b32 s39, %[wid], 3\nxA s53,s53,s39\nxA s53,s53,1245184\n"
"s_xor_b32 s52, s52, 1\ns_mul_i32 s63, s52, 73728\nxA s63,s63,83361792\ns_lshl_b32 s37, s52, 7\nxA s37,s37,1245184\ns_lshl_b32 s39, s45, 8\n"
"s_mul_i32 s43, s43, 4608\nxA s43,s43,s39\ns_lshl_b32 s45, %[wid], 10\nxA s63,s63,s45\nxW vmcnt(6)\ns_barrier\n"
"xR a[0:3],v225 offset:0\nxR a[16:19],v225 offset:1024\nxR a[32:35],v225 offset:2048\nxR a[4:7],v225 offset:3072\nxT v[160:161],v210 offset:12288\nxT v[162:163],v210 offset:14336\n"
"xT v[164:165],v210 offset:16384\nxT v[166:167],v210 offset:18432\nxT v[168:169],v210 offset:20480\nxT v[170:171],v210 offset:22528\nxR a[20:23],v225 offset:4096\nxR a[36:39],v225 offset:5120\n"
"xR a[8:11],v225 offset:6144\nxR a[24:27],v225 offset:7168\nxR a[40:43],v225 offset:8192\nxR a[12:15],v225 offset:9216\nxR a[28:31],v225 offset:10240\nxR a[44:47],v225 offset:11264\n"
"xT v[172:173],v211 offset:12288\nxT v[174:175],v211 offset:14336\nxT v[176:177],v211 offset:16384\nxT v[178:179],v211 offset:18432\nxT v[180:181],v211 offset:20480\nxT v[182:183],v211 offset:22528\n"
"xW lgkmcnt(15)\nxM v[32:35],v[160:163],a[0:3],0\nxA m0,s36,49152\nxT v[184:185],v210 offset:24576\nxT v[186:187],v210 offset:26624\n"
"xM v[32:35],v[164:167],a[16:19],v[32:35]\nxA s39,s47,6144\nxW lgkmcnt(15)\n"
"xM v[32:35],v[168:171],a[32:35],v[32:35]\nxL v224,s[96:99],s39 offen sc1 lds\nxM v[40:43],v[160:163],a[4:7],0\nxW lgkmcnt(14)\nxT v[188:189],v210 offset:28672\n"
"xM v[40:43],v[164:167],a[20:23],v[40:43]\nxA m0,s36,52224\nxT v[190:191],v210 offset:30720\n"
"xM v[40:43],v[168:171],a[36:39],v[40:43]\nxA s39,s47,24576\nxW lgkmcnt(15)\n"
"xM v[48:51],v[160:163],a[8:11],0\nxW lgkmcnt(14)\nxL v224,s[96:99],s39 offen sc1 lds\n"
"xM v[48:51],v[164:167],a[24:27],v[48:51]\nxW lgkmcnt(4)\nxT v[192:193],v210 offset:32768\n"
"xM v[48:51],v[168:171],a[40:43],v[48:51]\nxA m0,s36,50176\nxT v[194:195],v210 offset:34816\n"
"xM v[56:59],v[160:163],a[12:15],0\nxA s39,s47,7168\nxM v[56:59],v[164:167],a[28:31],v[56:59]\nxL v224,s[96:99],s39 offen sc1 lds\n"
"xM v[56:59],v[168:171],a[44:47],v[56:59]\nxT v[196:197],v211 offset:24576\nxM v[36:39],v[172:175],a[0:3],0\nxA m0,s36,53248\nxT v[198:199],v211 offset:26624\n"
"xM v[36:39],v[176:179],a[16:19],v[36:39]\nxA s39,s47,25600\nxM v[36:39],v[180:183],a[32:35],v[36:39]\nxL v224,s[96:99],s39 offen sc1 lds\n"
"xM v[44:47],v[172:175],a[4:7],0\nxT v[160:161],v211 offset:28672\nxM v[44:47],v[176:179],a[20:23],v[44:47]\nxA m0,s36,51200\nxT v[162:163],v211 offset:30720\n"
"xM v[44:47],v[180:183],a[36:39],v[44:47]\nxA s39,s47,8192\nxM v[52:55],v[172:175],a[8:11],0\nxL v224,s[96:99],s39 offen sc1 lds\n"
"xM v[52:55],v[176:179],a[24:27],v[52:55]\nxT v[164:165],v211 offset:32768\nxM v[52:55],v[180:183],a[40:43],v[52:55]\nxA m0,s36,54272\nxT v[166:167],v211 offset:34816\n"
"xM v[60:63],v[172:175],a[12:15],0\nxA s39,s47,26624\nxM v[60:63],v[176:179],a[28:31],v[60:63]\nxL v224,s[96:99],s39 offen sc1 lds\n"
"xM v[60:63],v[180:183],a[44:47],v[60:63]\nxW lgkmcnt(3)\nxM v[64:67],v[184:187],a[0:3],0\nxA m0,s32,135168\n"
"xM v[64:67],v[188:191],a[16:19],v[64:67]\nxA s39,s33,1769472\nxM v[64:67],v[192:195],a[32:35],v[64:67]\nxL v217,s[20:23],s39 offen lds\n"
"xM v[72:75],v[184:187],a[4:7],0\nxM v[72:75],v[188:191],a[20:23],v[72:75]\nxW vmcnt(7)\ns_barrier\n"
"xM v[72:75],v[192:195],a[36:39],v[72:75]\nxA m0,s32,136192\nxR a[48:51],v225 offset:24576\nxT v[168:169],v210 offset:36864\n"
"xM v[80:83],v[184:187],a[8:11],0\nxA s39,s33,1843200\nxR a[64:67],v225 offset:25600\nxT v[170:171],v210 offset:38912\n"
"xM v[80:83],v[188:191],a[24:27],v[80:83]\nxL v217,s[20:23],s39 offen lds\nxM v[80:83],v[192:195],a[40:43],v[80:83]\nxR a[80:83],v225 offset:26624\nxT v[172:173],v210 offset:40960\n"
"xM v[88:91],v[184:187],a[12:15],0\nxA m0,s32,137216\nxR a[52:55],v225 offset:27648\nxT v[174:175],v210 offset:43008\n"
"xM v[88:91],v[188:191],a[28:31],v[88:91]\nxA s39,s33,1916928\nxR a[68:71],v225 offset:28672\nxT v[176:177],v210 offset:45056\n"
"xM v[88:91],v[192:195],a[44:47],v[88:91]\nxL v217,s[20:23],s39 offen lds\nxM v[68:71],v[196:199],a[0:3],0\nxW lgkmcnt(10)\nxR a[84:87],v225 offset:29696\nxT v[178:179],v210 offset:47104\n"
"xM v[68:71],v[160:163],a[16:19],v[68:71]\nxA m0,s36,0\nxR a[56:59],v225 offset:30720\nxT v[180:181],v211 offset:36864\n"
"xM v[68:71],v[164:167],a[32:35],v[68:71]\nxA s39,s47,9216\nxR a[72:75],v225 offset:31744\nxT v[182:183],v211 offset:38912\n"
"xM v[76:79],v[196:199],a[4:7],0\nxL v224,s[96:99],s39 offen sc1 lds\nxM v[76:79],v[160:163],a[20:23],v[76:79]\nxR a[88:91],v225 offset:32768\nxT v[184:185],v211 offset:40960\n"
"xM v[76:79],v[164:167],a[36:39],v[76:79]\nxA m0,s36,3072\nxR a[60:63],v225 offset:33792\nxT v[186:187],v211 offset:43008\n"
"xM v[84:87],v[196:199],a[8:11],0\nxA s39,s47,27648\nxR a[76:79],v225 offset:34816\nxT v[188:189],v211 offset:45056\n"
"xM v[84:87],v[160:163],a[24:27],v[84:87]\nxL v224,s[96:99],s39 offen sc1 lds\nxM v[84:87],v[164:167],a[40:43],v[84:87]\nxR a[92:95],v225 offset:35840\nxT v[190:191],v211 offset:47104\n"
"xM v[92:95],v[196:199],a[12:15],0\nxA m0,s36,1024\nxM v[92:95],v[160:163],a[28:31],v[92:95]\nxA s39,s47,10240\nxT v[192:193],v210 offset:49152\n"
"xM v[92:95],v[164:167],a[44:47],v[92:95]\nxW lgkmcnt(9)\nxL v224,s[96:99],s39 offen sc1 lds\n"
"xM v[32:35],v[168:171],a[48:51],v[32:35]\nxT v[194:195],v210 offset:51200\nxM v[32:35],v[172:175],a[64:67],v[32:35]\nxA m0,s36,4096\n"
"xM v[32:35],v[176:179],a[80:83],v[32:35]\nxA s39,s47,28672\nxT v[196:197],v210 offset:53248\n"
"xM v[40:43],v[168:171],a[52:55],v[40:43]\nxL v224,s[96:99],s39 offen sc1 lds\nxM v[40:43],v[172:175],a[68:71],v[40:43]\nxT v[198:199],v210 offset:55296\n"
"xM v[40:43],v[176:179],a[84:87],v[40:43]\nxA m0,s36,2048\nxM v[48:51],v[168:171],a[56:59],v[48:51]\nxA s39,s47,11264\nxT v[160:161],v210 offset:57344\n"
"xM v[48:51],v[172:175],a[72:75],v[48:51]\nxW lgkmcnt(4)\nxL v224,s[96:99],s39 offen sc1 lds\n"
"xM v[48:51],v[176:179],a[88:91],v[48:51]\nxT v[162:163],v210 offset:59392\nxM v[56:59],v[168:171],a[60:63],v[56:59]\nxA m0,s36,5120\n"
"xM v[56:59],v[172:175],a[76:79],v[56:59]\nxA s39,s47,29696\nxT v[164:165],v211 offset:49152\n"
"xM v[56:59],v[176:179],a[92:95],v[56:59]\nxL v224,s[96:99],s39 offen sc1 lds\nxM v[36:39],v[180:183],a[48:51],v[36:39]\nxT v[166:167],v211 offset:51200\n"
"xM v[36:39],v[184:187],a[64:67],v[36:39]\nxA m0,s32,147456\nxM v[36:39],v[188:191],a[80:83],v[36:39]\nxA s39,s33,2654208\nxT v[168:169],v211 offset:53248\n"
"xM v[44:47],v[180:183],a[52:55],v[44:47]\nxL v217,s[16:19],s39 offen lds\nxM v[44:47],v[184:187],a[68:71],v[44:47]\nxT v[170:171],v211 offset:55296\n"
"xM v[44:47],v[188:191],a[84:87],v[44:47]\nxA m0,s32,148480\nxM v[52:55],v[180:183],a[56:59],v[52:55]\nxA s39,s33,2727936\nxT v[172:173],v211 offset:57344\n"
"xM v[52:55],v[184:187],a[72:75],v[52:55]\nxL v217,s[16:19],s39 offen lds\nxM v[52:55],v[188:191],a[88:91],v[52:55]\nxT v[174:175],v211 offset:59392\n"
"xM v[60:63],v[180:183],a[60:63],v[60:63]\nxA m0,s32,149504\nxM v[60:63],v[184:187],a[76:79],v[60:63]\nxA s39,s33,2801664\n"
"xM v[60:63],v[188:191],a[92:95],v[60:63]\nxW lgkmcnt(3)\nxL v217,s[16:19],s39 offen lds\n"
"xM v[64:67],v[192:195],a[48:51],v[64:67]\nxM v[64:67],v[196:199],a[64:67],v[64:67]\nxA m0,s32,73728\n"
"xM v[64:67],v[160:163],a[80:83],v[64:67]\nxA s39,s33,2654208\nxM v[72:75],v[192:195],a[52:55],v[72:75]\nxL v217,s[20:23],s39 offen lds\n"
"xM v[72:75],v[196:199],a[68:71],v[72:75]\nxW vmcnt(10)\ns_barrier\n"
"xM v[72:75],v[160:163],a[84:87],v[72:75]\nxA m0,s32,74752\nxR a[96:99],v225 offset:49152\nxT v[176:177],v212 offset:0\n"
"xM v[80:83],v[192:195],a[56:59],v[80:83]\nxA s39,s33,2727936\nxR a[112:115],v225 offset:50176\nxT v[178:179],v212 offset:2048\n"
"xM v[80:83],v[196:199],a[72:75],v[80:83]\nxL v217,s[20:23],s39 offen lds\nxM v[80:83],v[160:163],a[88:91],v[80:83]\nxR a[128:131],v225 offset:51200\nxT v[180:181],v212 offset:4096\n"
"xM v[88:91],v[192:195],a[60:63],v[88:91]\nxA m0,s32,75776\nxR a[100:103],v225 offset:52224\nxT v[182:183],v212 offset:6144\n"
"xM v[88:91],v[196:199],a[76:79],v[88:91]\nxA s39,s33,2801664\nxR a[116:119],v225 offset:53248\nxT v[184:185],v212 offset:8192\n"
"xM v[88:91],v[160:163],a[92:95],v[88:91]\nxL v217,s[20:23],s39 offen lds\nxM v[68:71],v[164:167],a[48:51],v[68:71]\nxW lgkmcnt(10)\nxR a[132:135],v225 offset:54272\nxT v[186:187],v212 offset:10240\n"
"xM v[68:71],v[168:171],a[64:67],v[68:71]\nxA m0,s36,24576\nxR a[104:107],v225 offset:55296\nxT v[188:189],v213 offset:0\n"
"xM v[68:71],v[172:175],a[80:83],v[68:71]\nxA s39,s47,12288\nxR a[120:123],v225 offset:56320\nxT v[190:191],v213 offset:2048\n"
"xM v[76:79],v[164:167],a[52:55],v[76:79]\nxL v224,s[96:99],s39 offen sc1 lds\nxM v[76:79],v[168:171],a[68:71],v[76:79]\nxR a[136:139],v225 offset:57344\nxT v[192:193],v213 offset:4096\n"
"xM v[76:79],v[172:175],a[84:87],v[76:79]\nxA m0,s36,27648\nxR a[108:111],v225 offset:58368\nxT v[194:195],v213 offset:6144\n"
"xM v[84:87],v[164:167],a[56:59],v[84:87]\nxA s39,s47,30720\nxR a[124:127],v225 offset:59392\nxT v[196:197],v213 offset:8192\n"
"xM v[84:87],v[168:171],a[72:75],v[84:87]\nxL v224,s[96:99],s39 offen sc1 lds\nxM v[84:87],v[172:175],a[88:91],v[84:87]\nxR a[140:143],v225 offset:60416\nxT v[198:199],v213 offset:10240\n"
"xM v[92:95],v[164:167],a[60:63],v[92:95]\nxA m0,s36,25600\nxM v[92:95],v[168:171],a[76:79],v[92:95]\nxA s39,s47,13312\nxT v[160:161],v212 offset:12288\n"
"xM v[92:95],v[172:175],a[92:95],v[92:95]\nxW lgkmcnt(9)\nxL v224,s[96:99],s39 offen sc1 lds\n"
"xM v[32:35],v[176:179],a[96:99],v[32:35]\nxT v[162:163],v212 offset:14336\nxM v[32:35],v[180:183],a[112:115],v[32:35]\nxA m0,s36,28672\n"
"xM v[32:35],v[184:187],a[128:131],v[32:35]\nxA s39,s47,31744\nxT v[164:165],v212 offset:16384\n"
"xM v[40:43],v[176:179],a[100:103],v[40:43]\nxL v224,s[96:99],s39 offen sc1 lds\nxM v[40:43],v[180:183],a[116:119],v[40:43]\nxT v[166:167],v212 offset:18432\n"
"xM v[40:43],v[184:187],a[132:135],v[40:43]\nxA m0,s36,26624\nxM v[48:51],v[176:179],a[104:107],v[48:51]\nxA s39,s47,14336\nxT v[168:169],v212 offset:20480\n"
"xM v[48:51],v[180:183],a[120:123],v[48:51]\nxW lgkmcnt(4)\nxL v224,s[96:99],s39 offen sc1 lds\n"
"xM v[48:51],v[184:187],a[136:139],v[48:51]\nxT v[170:171],v212 offset:22528\nxM v[56:59],v[176:179],a[108:111],v[56:59]\nxA m0,s36,29696\n"
"xM v[56:59],v[180:183],a[124:127],v[56:59]\nxA s39,s47,32768\nxT v[172:173],v213 offset:12288\n"
"xM v[56:59],v[184:187],a[140:143],v[56:59]\nxL v224,s[96:99],s39 offen sc1 lds\nxM v[36:39],v[188:191],a[96:99],v[36:39]\nxT v[174:175],v213 offset:14336\n"
"xM v[36:39],v[192:195],a[112:115],v[36:39]\nxA m0,s32,86016\nxM v[36:39],v[196:199],a[128:131],v[36:39]\nxA s39,s33,3538944\nxT v[176:177],v213 offset:16384\n"
"xM v[44:47],v[188:191],a[100:103],v[44:47]\nxL v217,s[16:19],s39 offen lds\nxM v[44:47],v[192:195],a[116:119],v[44:47]\nxT v[178:179],v213 offset:18432\n"
"xM v[44:47],v[196:199],a[132:135],v[44:47]\nxA m0,s32,87040\nxM v[52:55],v[188:191],a[104:107],v[52:55]\nxA s39,s33,3612672\nxT v[180:181],v213 offset:20480\n"
"xM v[52:55],v[192:195],a[120:123],v[52:55]\nxL v217,s[16:19],s39 offen lds\nxM v[52:55],v[196:199],a[136:139],v[52:55]\nxT v[182:183],v213 offset:22528\n"
"xM v[60:63],v[188:191],a[108:111],v[60:63]\nxA m0,s32,88064\nxM v[60:63],v[192:195],a[124:127],v[60:63]\nxA s39,s33,3686400\n"
"xM v[60:63],v[196:199],a[140:143],v[60:63]\nxW lgkmcnt(3)\nxL v217,s[16:19],s39 offen lds\n"
"xM v[64:67],v[160:163],a[96:99],v[64:67]\nxM v[64:67],v[164:167],a[112:115],v[64:67]\nxA m0,s32,98304\n"
"xM v[64:67],v[168:171],a[128:131],v[64:67]\nxA s39,s33,3538944\nxM v[72:75],v[160:163],a[100:103],v[72:75]\nxL v217,s[20:23],s39 offen lds\n"
"xM v[72:75],v[164:167],a[116:119],v[72:75]\nxW vmcnt(10)\ns_barrier\n"
"xM v[72:75],v[168:171],a[132:135],v[72:75]\nxA m0,s32,99328\nxR a[144:147],v225 offset:0\nxT v[184:185],v212 offset:24576\n"
"xM v[80:83],v[160:163],a[104:107],v[80:83]\nxA s39,s33,3612672\nxR a[160:163],v225 offset:1024\nxT v[186:187],v212 offset:26624\n"
"xM v[80:83],v[164:167],a[120:123],v[80:83]\nxL v217,s[20:23],s39 offen lds\nxM v[80:83],v[168:171],a[136:139],v[80:83]\nxR a[176:179],v225 offset:2048\nxT v[188:189],v212 offset:28672\n"
"xM v[88:91],v[160:163],a[108:111],v[88:91]\nxA m0,s32,100352\nxR a[148:151],v225 offset:3072\nxT v[190:191],v212 offset:30720\n"
"xM v[88:91],v[164:167],a[124:127],v[88:91]\nxA s39,s33,3686400\nxR a[164:167],v225 offset:4096\nxT v[192:193],v212 offset:32768\n"
"xM v[88:91],v[168:171],a[140:143],v[88:91]\nxL v217,s[20:23],s39 offen lds\nxM v[68:71],v[172:175],a[96:99],v[68:71]\nxW lgkmcnt(10)\nxR a[180:183],v225 offset:5120\nxT v[194:195],v212 offset:34816\n"
"xM v[68:71],v[176:179],a[112:115],v[68:71]\nxA m0,s36,49152\nxR a[152:155],v225 offset:6144\nxT v[196:197],v213 offset:24576\n"
"xM v[68:71],v[180:183],a[128:131],v[68:71]\nxA s39,s47,15360\nxR a[168:171],v225 offset:7168\nxT v[198:199],v213 offset:26624\n"
"xM v[76:79],v[172:175],a[100:103],v[76:79]\nxL v224,s[96:99],s39 offen sc1 lds\nxM v[76:79],v[176:179],a[116:119],v[76:79]\nxR a[184:187],v225 offset:8192\nxT v[160:161],v213 offset:28672\n"
"xM v[76:79],v[180:183],a[132:135],v[76:79]\nxA m0,s36,52224\nxR a[156:159],v225 offset:9216\nxT v[162:163],v213 offset:30720\n"
"xM v[84:87],v[172:175],a[104:107],v[84:87]\nxA s39,s47,33792\nxR a[172:175],v225 offset:10240\nxT v[164:165],v213 offset:32768\n"
"xM v[84:87],v[176:179],a[120:123],v[84:87]\nxL v224,s[96:99],s39 offen sc1 lds\nxM v[84:87],v[180:183],a[136:139],v[84:87]\nxR a[188:191],v225 offset:11264\nxT v[166:167],v213 offset:34816\n"
"xM v[92:95],v[172:175],a[108:111],v[92:95]\nxA m0,s36,50176\nxM v[92:95],v[176:179],a[124:127],v[92:95]\nxA s39,s47,16384\nxT v[168:169],v210 offset:12288\n"
"xM v[92:95],v[180:183],a[140:143],v[92:95]\nxW lgkmcnt(9)\nxL v224,s[96:99],s39 offen sc1 lds\n"
"xM v[32:35],v[184:187],a[144:147],v[32:35]\nxT v[170:171],v210 offset:14336\nxM v[32:35],v[188:191],a[160:163],v[32:35]\nxA m0,s36,53248\n"
"xM v[32:35],v[192:195],a[176:179],v[32:35]\nxA s39,s47,34816\nxT v[172:173],v210 offset:16384\n"
"xM v[40:43],v[184:187],a[148:151],v[40:43]\nxL v224,s[96:99],s39 offen sc1 lds\nxM v[40:43],v[188:191],a[164:167],v[40:43]\nxT v[174:175],v210 offset:18432\n"
"xM v[40:43],v[192:195],a[180:183],v[40:43]\nxA m0,s36,51200\nxM v[48:51],v[184:187],a[152:155],v[48:51]\nxA s39,s47,17408\nxT v[176:177],v210 offset:20480\n"
"xM v[48:51],v[188:191],a[168:171],v[48:51]\nxW lgkmcnt(4)\nxL v224,s[96:99],s39 offen sc1 lds\n"
"xM v[48:51],v[192:195],a[184:187],v[48:51]\nxT v[178:179],v210 offset:22528\nxM v[56:59],v[184:187],a[156:159],v[56:59]\nxA m0,s36,54272\n"
"xM v[56:59],v[188:191],a[172:175],v[56:59]\nxA s39,s47,35840\nxT v[180:181],v211 offset:12288\n"
"xM v[56:59],v[192:195],a[188:191],v[56:59]\nxL v224,s[96:99],s39 offen sc1 lds\nxM v[36:39],v[196:199],a[144:147],v[36:39]\nxT v[182:183],v211 offset:14336\n"
"xM v[36:39],v[160:163],a[160:163],v[36:39]\nxA m0,s32,110592\nxM v[36:39],v[164:167],a[176:179],v[36:39]\nxA s39,s33,4423680\nxT v[184:185],v211 offset:16384\n"
"xM v[44:47],v[196:199],a[148:151],v[44:47]\nxL v217,s[16:19],s39 offen lds\nxM v[44:47],v[160:163],a[164:167],v[44:47]\nxT v[186:187],v211 offset:18432\n"
"xM v[44:47],v[164:167],a[180:183],v[44:47]\nxA m0,s32,111616\nxM v[52:55],v[196:199],a[152:155],v[52:55]\nxA s39,s33,4497408\nxT v[188:189],v211 offset:20480\n"
"xM v[52:55],v[160:163],a[168:171],v[52:55]\nxL v217,s[16:19],s39 offen lds\nxM v[52:55],v[164:167],a[184:187],v[52:55]\nxT v[190:191],v211 offset:22528\n"
"xM v[60:63],v[196:199],a[156:159],v[60:63]\nxA m0,s32,112640\nxM v[60:63],v[160:163],a[172:175],v[60:63]\nxA s39,s33,4571136\n"
"xM v[60:63],v[164:167],a[188:191],v[60:63]\nxW lgkmcnt(3)\nxL v217,s[16:19],s39 offen lds\n"
"xM v[64:67],v[168:171],a[144:147],v[64:67]\nxM v[64:67],v[172:175],a[160:163],v[64:67]\nxA m0,s32,122880\n"
"xM v[64:67],v[176:179],a[176:179],v[64:67]\nxA s39,s33,4423680\nxM v[72:75],v[168:171],a[148:151],v[72:75]\nxL v217,s[20:23],s39 offen lds\n"
"xM v[72:75],v[172:175],a[164:167],v[72:75]\nxW vmcnt(10)\ns_barrier\n"
"xM v[72:75],v[176:179],a[180:183],v[72:75]\nxA m0,s32,123904\nxR a[192:195],v225 offset:24576\nxT v[192:193],v210 offset:24576\n"
"xM v[80:83],v[168:171],a[152:155],v[80:83]\nxA s39,s33,4497408\nxR a[208:211],v225 offset:25600\nxT v[194:195],v210 offset:26624\n"
"xM v[80:83],v[172:175],a[168:171],v[80:83]\nxL v217,s[20:23],s39 offen lds\nxM v[80:83],v[176:179],a[184:187],v[80:83]\nTCH1_2_%=:\ns_cbranch_execz TC1_2_1_%=\nxR a[224:227],v225 offset:26624\n"
"xT v[196:197],v210 offset:28672\nxM v[88:91],v[168:171],a[156:159],v[88:91]\nxA m0,s32,124928\nxR a[196:199],v225 offset:27648\nxT v[198:199],v210 offset:30720\n"
"xM v[88:91],v[172:175],a[172:175],v[88:91]\nxA s39,s33,4571136\nxR a[212:215],v225 offset:28672\nxT v[160:161],v210 offset:32768\n"
"xM v[88:91],v[176:179],a[188:191],v[88:91]\nxL v217,s[20:23],s39 offen lds\nxM v[68:71],v[180:183],a[144:147],v[68:71]\nxW lgkmcnt(10)\nxR a[228:231],v225 offset:29696\nxT v[162:163],v210 offset:34816\n"
"xM v[68:71],v[184:187],a[160:163],v[68:71]\nxA m0,s32,135168\nxR a[200:203],v225 offset:30720\nxT v[164:165],v211 offset:24576\n"
"xM v[68:71],v[188:191],a[176:179],v[68:71]\nxA s39,s33,128\nxR a[216:219],v225 offset:31744\nxT v[166:167],v211 offset:26624\n"
"xM v[76:79],v[180:183],a[148:151],v[76:79]\nxL v217,s[16:19],s39 offen lds\nxM v[76:79],v[184:187],a[164:167],v[76:79]\nxR a[232:235],v225 offset:32768\nxT v[168:169],v211 offset:28672\n"
"xM v[76:79],v[188:191],a[180:183],v[76:79]\nxA m0,s32,136192\nxR a[204:207],v225 offset:33792\nxT v[170:171],v211 offset:30720\n"
"xM v[84:87],v[180:183],a[152:155],v[84:87]\nxA s39,s33,73856\nxR a[220:223],v225 offset:34816\nxT v[172:173],v211 offset:32768\n"
"xM v[84:87],v[184:187],a[168:171],v[84:87]\nxL v217,s[16:19],s39 offen lds\nxM v[84:87],v[188:191],a[184:187],v[84:87]\nxR a[236:239],v225 offset:35840\nxT v[174:175],v211 offset:34816\n"
"xM v[92:95],v[180:183],a[156:159],v[92:95]\nxA m0,s32,137216\nxM v[92:95],v[184:187],a[172:175],v[92:95]\nxA s39,s33,147584\nxT v[176:177],v210 offset:36864\n"
"xM v[92:95],v[188:191],a[188:191],v[92:95]\nxW lgkmcnt(9)\nxL v217,s[16:19],s39 offen lds\n"
"xM v[32:35],v[192:195],a[192:195],v[32:35]\nxT v[178:179],v210 offset:38912\nxM v[32:35],v[196:199],a[208:211],v[32:35]\nxA m0,s32,147456\n"
"xM v[32:35],v[160:163],a[224:227],v[32:35]\nxA s39,s33,128\nxT v[180:181],v210 offset:40960\n"
"xM v[40:43],v[192:195],a[196:199],v[40:43]\nxL v217,s[20:23],s39 offen lds\nxM v[40:43],v[196:199],a[212:215],v[40:43]\nxT v[182:183],v210 offset:43008\n"
"xM v[40:43],v[160:163],a[228:231],v[40:43]\nxA m0,s32,148480\nxM v[48:51],v[192:195],a[200:203],v[48:51]\nxA s39,s33,73856\nxT v[184:185],v210 offset:45056\n"
"xM v[48:51],v[196:199],a[216:219],v[48:51]\nxW lgkmcnt(4)\nxL v217,s[20:23],s39 offen lds\n"
"xM v[48:51],v[160:163],a[232:235],v[48:51]\nxT v[186:187],v210 offset:47104\nxM v[56:59],v[192:195],a[204:207],v[56:59]\nxA m0,s32,149504\n"
"xM v[56:59],v[196:199],a[220:223],v[56:59]\nxA s39,s33,147584\nxT v[188:189],v211 offset:36864\n"
"xM v[56:59],v[160:163],a[236:239],v[56:59]\nxL v217,s[20:23],s39 offen lds\nxM v[36:39],v[164:167],a[192:195],v[36:39]\nxT v[190:191],v211 offset:38912\n"
"xM v[36:39],v[168:171],a[208:211],v[36:39]\nxM v[36:39],v[172:175],a[224:227],v[36:39]\nxT v[192:193],v211 offset:40960\n"
"xM v[44:47],v[164:167],a[196:199],v[44:47]\nxM v[44:47],v[168:171],a[212:215],v[44:47]\nxT v[194:195],v211 offset:43008\n"
"xM v[44:47],v[172:175],a[228:231],v[44:47]\nxM v[52:55],v[164:167],a[200:203],v[52:55]\nxT v[196:197],v211 offset:45056\n"
"xM v[52:55],v[168:171],a[216:219],v[52:55]\nxM v[52:55],v[172:175],a[232:235],v[52:55]\nxT v[198:199],v211 offset:47104\n"
"xM v[60:63],v[164:167],a[204:207],v[60:63]\nxM v[60:63],v[168:171],a[220:223],v[60:63]\nxM v[60:63],v[172:175],a[236:239],v[60:63]\nxW lgkmcnt(3)\n"
"xM v[64:67],v[176:179],a[192:195],v[64:67]\nxM v[64:67],v[180:183],a[208:211],v[64:67]\nxM v[64:67],v[184:187],a[224:227],v[64:67]\n"
"xM v[72:75],v[176:179],a[196:199],v[72:75]\nxM v[72:75],v[180:183],a[212:215],v[72:75]\nxW vmcnt(6)\ns_barrier\n"
"xM v[72:75],v[184:187],a[228:231],v[72:75]\nxA m0,s32,73728\nxR a[240:243],v225 offset:49152\nxT v[160:161],v210 offset:49152\n"
"xM v[80:83],v[176:179],a[200:203],v[80:83]\nxA s39,s33,884864\nxR v[0:3],v225 offset:50176\nxT v[162:163],v210 offset:51200\n"
"xM v[80:83],v[180:183],a[216:219],v[80:83]\nxL v217,s[16:19],s39 offen lds\nxM v[80:83],v[184:187],a[232:235],v[80:83]\nxR v[16:19],v225 offset:51200\nxT v[164:165],v210 offset:53248\n"
"xM v[88:91],v[176:179],a[204:207],v[88:91]\nxA m0,s32,74752\nxR a[244:247],v225 offset:52224\nxT v[166:167],v210 offset:55296\n"
"xM v[88:91],v[180:183],a[220:223],v[88:91]\nxA s39,s33,958592\nxR v[4:7],v225 offset:53248\nxT v[168:169],v210 offset:57344\n"
"xM v[88:91],v[184:187],a[236:239],v[88:91]\nxL v217,s[16:19],s39 offen lds\nxM v[68:71],v[188:191],a[192:195],v[68:71]\nxW lgkmcnt(10)\nxR v[20:23],v225 offset:54272\nxT v[170:171],v210 offset:59392\n"
"xM v[68:71],v[192:195],a[208:211],v[68:71]\nxA m0,s32,75776\nxR a[248:251],v225 offset:55296\nxT v[172:173],v211 offset:49152\n"
"xM v[68:71],v[196:199],a[224:227],v[68:71]\nxA s39,s33,1032320\nxR v[8:11],v225 offset:56320\nxT v[174:175],v211 offset:51200\n"
"xM v[76:79],v[188:191],a[196:199],v[76:79]\nxL v217,s[16:19],s39 offen lds\nxM v[76:79],v[192:195],a[212:215],v[76:79]\nxR v[24:27],v225 offset:57344\nxT v[176:177],v211 offset:53248\n"
"xM v[76:79],v[196:199],a[228:231],v[76:79]\nxA m0,s32,86016\nxR a[252:255],v225 offset:58368\nxT v[178:179],v211 offset:55296\n"
"xM v[84:87],v[188:191],a[200:203],v[84:87]\nxA s39,s33,884864\nxR v[12:15],v225 offset:59392\nxT v[180:181],v211 offset:57344\n"
"xM v[84:87],v[192:195],a[216:219],v[84:87]\nxL v217,s[20:23],s39 offen lds\nxM v[84:87],v[196:199],a[232:235],v[84:87]\nxR v[28:31],v225 offset:60416\nxT v[182:183],v211 offset:59392\n"
"xM v[92:95],v[188:191],a[204:207],v[92:95]\nxA m0,s32,87040\nxM v[92:95],v[192:195],a[220:223],v[92:95]\nxA s39,s33,958592\nxT v[184:185],v212 offset:0\n"
"xM v[92:95],v[196:199],a[236:239],v[92:95]\nxW lgkmcnt(9)\nxL v217,s[20:23],s39 offen lds\n"
"xM v[32:35],v[160:163],a[240:243],v[32:35]\nxT v[186:187],v212 offset:2048\nxM v[32:35],v[164:167],v[0:3],v[32:35]\nxA m0,s32,88064\n"
"xM v[32:35],v[168:171],v[16:19],v[32:35]\nxA s39,s33,1032320\nxT v[188:189],v212 offset:4096\n"
"xM v[40:43],v[160:163],a[244:247],v[40:43]\nxL v217,s[20:23],s39 offen lds\nxM v[40:43],v[164:167],v[4:7],v[40:43]\nxT v[190:191],v212 offset:6144\n"
"xM v[40:43],v[168:171],v[20:23],v[40:43]\nxM v[48:51],v[160:163],a[248:251],v[48:51]\nxT v[192:193],v212 offset:8192\n"
"xM v[48:51],v[164:167],v[8:11],v[48:51]\nxW lgkmcnt(4)\nxM v[48:51],v[168:171],v[24:27],v[48:51]\nxT v[194:195],v212 offset:10240\n"
"xM v[56:59],v[160:163],a[252:255],v[56:59]\nxM v[56:59],v[164:167],v[12:15],v[56:59]\nxT v[196:197],v213 offset:0\n"
"xM v[56:59],v[168:171],v[28:31],v[56:59]\nxM v[36:39],v[172:175],a[240:243],v[36:39]\nxT v[198:199],v213 offset:2048\n"
"xM v[36:39],v[176:179],v[0:3],v[36:39]\nxM v[36:39],v[180:183],v[16:19],v[36:39]\nxT v[160:161],v213 offset:4096\n"
"xM v[44:47],v[172:175],a[244:247],v[44:47]\nxM v[44:47],v[176:179],v[4:7],v[44:47]\nxT v[162:163],v213 offset:6144\n"
"xM v[44:47],v[180:183],v[20:23],v[44:47]\nxM v[52:55],v[172:175],a[248:251],v[52:55]\nxT v[164:165],v213 offset:8192\n"
"xM v[52:55],v[176:179],v[8:11],v[52:55]\nxM v[52:55],v[180:183],v[24:27],v[52:55]\nxT v[166:167],v213 offset:10240\n"
"xM v[60:63],v[172:175],a[252:255],v[60:63]\nxM v[60:63],v[176:179],v[12:15],v[60:63]\nxM v[60:63],v[180:183],v[28:31],v[60:63]\nxW lgkmcnt(3)\n"
"xM v[64:67],v[184:187],a[240:243],v[64:67]\nxM v[64:67],v[188:191],v[0:3],v[64:67]\nxM v[64:67],v[192:195],v[16:19],v[64:67]\n"
"xM v[72:75],v[184:187],a[244:247],v[72:75]\nxM v[72:75],v[188:191],v[4:7],v[72:75]\nxW vmcnt(6)\ns_barrier\n"
"xM v[72:75],v[192:195],v[20:23],v[72:75]\nxA m0,s32,98304\nxT v[168:169],v212 offset:12288\nxT v[170:171],v212 offset:14336\n"
"xM v[80:83],v[184:187],a[248:251],v[80:83]\nxA s39,s33,1769600\nxT v[172:173],v212 offset:16384\nxT v[174:175],v212 offset:18432\n"
"xM v[80:83],v[188:191],v[8:11],v[80:83]\nxL v217,s[16:19],s39 offen lds\nxM v[80:83],v[192:195],v[24:27],v[80:83]\nxT v[176:177],v212 offset:20480\nxT v[178:179],v212 offset:22528\n"
"xM v[88:91],v[184:187],a[252:255],v[88:91]\nxM v[88:91],v[188:191],v[12:15],v[88:91]\nxT v[180:181],v213 offset:12288\n"
"xM v[88:91],v[192:195],v[28:31],v[88:91]\nxA m0,s32,99328\nxM v[68:71],v[196:199],a[240:243],v[68:71]\nxA s39,s33,1843328\nxT v[182:183],v213 offset:14336\nxW lgkmcnt(10)\n"
"xM v[68:71],v[160:163],v[0:3],v[68:71]\nxW lgkmcnt(6)\nxL v217,s[16:19],s39 offen lds\n"
"xM v[68:71],v[164:167],v[16:19],v[68:71]\nxT v[184:185],v213 offset:16384\nxM v[76:79],v[196:199],a[244:247],v[76:79]\n"
"xM v[76:79],v[160:163],v[4:7],v[76:79]\nxT v[186:187],v213 offset:18432\nxM v[76:79],v[164:167],v[20:23],v[76:79]\nxA m0,s32,100352\n"
"xM v[84:87],v[196:199],a[248:251],v[84:87]\nxA s39,s33,1917056\nxT v[188:189],v213 offset:20480\n"
"xM v[84:87],v[160:163],v[8:11],v[84:87]\nxL v217,s[16:19],s39 offen lds\nxM v[84:87],v[164:167],v[24:27],v[84:87]\nxT v[190:191],v213 offset:22528\n"
"xM v[92:95],v[196:199],a[252:255],v[92:95]\nxM v[92:95],v[160:163],v[12:15],v[92:95]\nxT v[192:193],v212 offset:24576\n"
"xM v[92:95],v[164:167],v[28:31],v[92:95]\nxA m0,s32,110592\nxM v[96:99],v[168:171],a[0:3],0\nxA s39,s33,1769600\nxT v[194:195],v212 offset:26624\nxW lgkmcnt(10)\n"
"xM v[96:99],v[172:175],a[16:19],v[96:99]\nxW lgkmcnt(4)\nxL v217,s[20:23],s39 offen lds\n"
"xM v[96:99],v[176:179],a[32:35],v[96:99]\nxT v[196:197],v212 offset:28672\nxM v[104:107],v[168:171],a[4:7],0\nxU v64,v32,v64\nxU v65,v33,v65\n"
"xM v[104:107],v[172:175],a[20:23],v[104:107]\nxE v32,-v32\nxM v[104:107],v[176:179],a[36:39],v[104:107]\nxA m0,s32,111616\nxE v33,-v33\n"
"xM v[112:115],v[168:171],a[8:11],0\nxA s39,s33,1843328\nv_add_f32 v32, 1.0, v32\nv_add_f32 v33, 1.0, v33\n"
"xM v[112:115],v[172:175],a[24:27],v[112:115]\nxL v217,s[20:23],s39 offen lds\nxM v[112:115],v[176:179],a[40:43],v[112:115]\nv_rcp_f32 v32, v32\n"
"xM v[120:123],v[168:171],a[12:15],0\nv_rcp_f32 v33, v33\nxM v[120:123],v[172:175],a[28:31],v[120:123]\nxU v64,v64,v32\nxU v65,v65,v33\n"
"xM v[120:123],v[176:179],a[44:47],v[120:123]\nxA m0,s32,112640\nxC v64,v64,v65\nxU v66,v34,v66\n"
"xM v[100:103],v[180:183],a[0:3],0\nxA s39,s33,1917056\nxT v[198:199],v212 offset:30720\nxU v67,v35,v67\n"
"xM v[100:103],v[184:187],a[16:19],v[100:103]\nxW lgkmcnt(1)\nxL v217,s[20:23],s39 offen lds\n"
"xM v[100:103],v[188:191],a[32:35],v[100:103]\nxT v[160:161],v212 offset:32768\nxT v[162:163],v212 offset:34816\n"
"xM v[108:111],v[180:183],a[4:7],0\nxE v34,-v34\nxM v[108:111],v[184:187],a[20:23],v[108:111]\nxE v35,-v35\n"
"xM v[108:111],v[188:191],a[36:39],v[108:111]\nv_add_f32 v34, 1.0, v34\nv_add_f32 v35, 1.0, v35\n"
"xM v[116:119],v[180:183],a[8:11],0\nxT v[164:165],v213 offset:24576\nxT v[166:167],v213 offset:26624\n"
"xM v[116:119],v[184:187],a[24:27],v[116:119]\nv_rcp_f32 v34, v34\nxM v[116:119],v[188:191],a[40:43],v[116:119]\nv_rcp_f32 v35, v35\n"
"xM v[124:127],v[180:183],a[12:15],0\nxU v66,v66,v34\nxU v67,v67,v35\n"
"xM v[124:127],v[184:187],a[28:31],v[124:127]\nxT v[168:169],v213 offset:28672\nxC v65,v66,v67\n"
"xM v[124:127],v[188:191],a[44:47],v[124:127]\nxU v68,v36,v68\nxU v69,v37,v69\n"
"xM v[128:131],v[192:195],a[0:3],0\nxW lgkmcnt(3)\nxT v[170:171],v213 offset:30720\nxT v[172:173],v213 offset:32768\n"
"xM v[128:131],v[196:199],a[16:19],v[128:131]\nxE v36,-v36\nxM v[128:131],v[160:163],a[32:35],v[128:131]\nxE v37,-v37\n"
"xM v[136:139],v[192:195],a[4:7],0\nv_add_f32 v36, 1.0, v36\nv_add_f32 v37, 1.0, v37\n"
"xM v[136:139],v[196:199],a[20:23],v[136:139]\nxW vmcnt(6)\ns_barrier\n"
"xM v[136:139],v[160:163],a[36:39],v[136:139]\nxA m0,s32,122880\nxT v[174:175],v213 offset:34816\nxT v[176:177],v210 offset:12288\n"
"xM v[144:147],v[192:195],a[8:11],0\nxA s39,s33,2654336\nv_rcp_f32 v36, v36\n"
"xM v[144:147],v[196:199],a[24:27],v[144:147]\nxL v217,s[16:19],s39 offen lds\nxM v[144:147],v[160:163],a[40:43],v[144:147]\nxT v[178:179],v210 offset:14336\nxT v[180:181],v210 offset:16384\n"
"xM v[152:155],v[192:195],a[12:15],0\nv_rcp_f32 v37, v37\nxM v[152:155],v[196:199],a[28:31],v[152:155]\nxW lgkmcnt(4)\nxU v68,v68,v36\nxU v69,v69,v37\n"
"xM v[152:155],v[160:163],a[44:47],v[152:155]\nxA m0,s32,123904\nxC v66,v68,v69\nxU v70,v38,v70\n"
"xM v[132:135],v[164:167],a[0:3],0\nxA s39,s33,2728064\nxT v[182:183],v210 offset:18432\nxU v71,v39,v71\n"
"xM v[132:135],v[168:171],a[16:19],v[132:135]\nxW lgkmcnt(3)\nxL v217,s[16:19],s39 offen lds\n"
"xM v[132:135],v[172:175],a[32:35],v[132:135]\nxT v[184:185],v210 offset:20480\nxT v[186:187],v210 offset:22528\n"
"xM v[140:143],v[164:167],a[4:7],0\nxE v38,-v38\nxM v[140:143],v[168:171],a[20:23],v[140:143]\nxE v39,-v39\n"
"xM v[140:143],v[172:175],a[36:39],v[140:143]\nxA m0,s32,124928\nv_add_f32 v38, 1.0, v38\nv_add_f32 v39, 1.0, v39\n"
"xM v[148:151],v[164:167],a[8:11],0\nxA s39,s33,2801792\nxT v[188:189],v211 offset:12288\nxT v[190:191],v211 offset:14336\n"
"xM v[148:151],v[168:171],a[24:27],v[148:151]\nxL v217,s[16:19],s39 offen lds\nxM v[148:151],v[172:175],a[40:43],v[148:151]\nv_rcp_f32 v38, v38\n"
"xM v[156:159],v[164:167],a[12:15],0\nv_rcp_f32 v39, v39\nxM v[156:159],v[168:171],a[28:31],v[156:159]\nxW lgkmcnt(2)\nxT v[192:193],v211 offset:16384\nxU v70,v70,v38\n"
"xM v[156:159],v[172:175],a[44:47],v[156:159]\nxA m0,s32,135168\nxU v71,v71,v39\nxC v67,v70,v71\n"
"xM v[96:99],v[176:179],a[48:51],v[96:99]\nxA s39,s33,2654336\nxT v[194:195],v211 offset:18432\nxT v[196:197],v211 offset:20480\n"
"xM v[96:99],v[180:183],a[64:67],v[96:99]\nxL v217,s[20:23],s39 offen lds\nxM v[96:99],v[184:187],a[80:83],v[96:99]\nds_write_b128 v226, v[64:67] offset:0\n"
"xM v[104:107],v[176:179],a[52:55],v[104:107]\nxS v[64:67],v226,s[96:99],s62 offen offset:0 sc1\nxM v[104:107],v[180:183],a[68:71],v[104:107]\nxT v[198:199],v211 offset:22528\nxU v72,v40,v72\n"
"xM v[104:107],v[184:187],a[84:87],v[104:107]\nxA m0,s32,136192\nxT v[160:161],v210 offset:24576\nxU v73,v41,v73\n"
"xM v[112:115],v[176:179],a[56:59],v[112:115]\nxA s39,s33,2728064\nxE v40,-v40\n"
"xM v[112:115],v[180:183],a[72:75],v[112:115]\nxL v217,s[20:23],s39 offen lds\nxM v[112:115],v[184:187],a[88:91],v[112:115]\nxT v[162:163],v210 offset:26624\nxT v[164:165],v210 offset:28672\n"
"xM v[120:123],v[176:179],a[60:63],v[120:123]\nxE v41,-v41\nxM v[120:123],v[180:183],a[76:79],v[120:123]\nxW lgkmcnt(4)\nv_add_f32 v40, 1.0, v40\nv_add_f32 v41, 1.0, v41\n"
"xM v[120:123],v[184:187],a[92:95],v[120:123]\nxA m0,s32,137216\nv_rcp_f32 v40, v40\n"
"xM v[100:103],v[188:191],a[48:51],v[100:103]\nxA s39,s33,2801792\nxT v[166:167],v210 offset:30720\nxT v[168:169],v210 offset:32768\n"
"xM v[100:103],v[192:195],a[64:67],v[100:103]\nxW lgkmcnt(4)\nxL v217,s[20:23],s39 offen lds\n"
"xM v[100:103],v[196:199],a[80:83],v[100:103]\nv_rcp_f32 v41, v41\nxM v[108:111],v[188:191],a[52:55],v[108:111]\nxU v72,v72,v40\nxU v73,v73,v41\n"
"xM v[108:111],v[192:195],a[68:71],v[108:111]\nxT v[170:171],v210 offset:34816\nxC v72,v72,v73\n"
"xM v[108:111],v[196:199],a[84:87],v[108:111]\nxA m0,s32,24576\nxU v74,v42,v74\nxU v75,v43,v75\n"
"xM v[116:119],v[188:191],a[56:59],v[116:119]\nxA s39,s33,3539072\nxT v[172:173],v211 offset:24576\nxT v[174:175],v211 offset:26624\n"
"xM v[116:119],v[192:195],a[72:75],v[116:119]\nxL v217,s[16:19],s39 offen lds\nxM v[116:119],v[196:199],a[88:91],v[116:119]\nxE v42,-v42\n"
"xM v[124:127],v[188:191],a[60:63],v[124:127]\nxE v43,-v43\nxM v[124:127],v[192:195],a[76:79],v[124:127]\nxW lgkmcnt(3)\nxT v[176:177],v211 offset:28672\nv_add_f32 v42, 1.0, v42\n"
"xM v[124:127],v[196:199],a[92:95],v[124:127]\nxA m0,s32,25600\nxT v[178:179],v211 offset:30720\nv_add_f32 v43, 1.0, v43\n"
"xM v[128:131],v[160:163],a[48:51],v[128:131]\nxA s39,s33,3612800\nv_rcp_f32 v42, v42\n"
"xM v[128:131],v[164:167],a[64:67],v[128:131]\nxW lgkmcnt(4)\nxL v217,s[16:19],s39 offen lds\n"
"xM v[128:131],v[168:171],a[80:83],v[128:131]\nxT v[180:181],v211 offset:32768\nxT v[182:183],v211 offset:34816\n"
"xM v[136:139],v[160:163],a[52:55],v[136:139]\nv_rcp_f32 v43, v43\nxM v[136:139],v[164:167],a[68:71],v[136:139]\nxW vmcnt(9)\ns_barrier\n"
"xM v[136:139],v[168:171],a[84:87],v[136:139]\nxA m0,s32,26624\nxU v74,v74,v42\nxU v75,v75,v43\n"
"xM v[144:147],v[160:163],a[56:59],v[144:147]\nxA s39,s33,3686528\nxT v[184:185],v210 offset:36864\nxC v73,v74,v75\n"
"xM v[144:147],v[164:167],a[72:75],v[144:147]\nxL v217,s[16:19],s39 offen lds\nxM v[144:147],v[168:171],a[88:91],v[144:147]\nxT v[186:187],v210 offset:38912\nxU v76,v44,v76\n"
"xM v[152:155],v[160:163],a[60:63],v[152:155]\nxT v[188:189],v210 offset:40960\nxU v77,v45,v77\n"
"xM v[152:155],v[164:167],a[76:79],v[152:155]\nxW lgkmcnt(3)\nxE v44,-v44\n"
"xM v[152:155],v[168:171],a[92:95],v[152:155]\nxA m0,s32,36864\nxE v45,-v45\n"
"xM v[132:135],v[172:175],a[48:51],v[132:135]\nxA s39,s33,3539072\nxT v[190:191],v210 offset:43008\nv_add_f32 v44, 1.0, v44\n"
"xM v[132:135],v[176:179],a[64:67],v[132:135]\nxL v217,s[20:23],s39 offen lds\nxM v[132:135],v[180:183],a[80:83],v[132:135]\nxT v[192:193],v210 offset:45056\nv_add_f32 v45, 1.0, v45\n"
"xM v[140:143],v[172:175],a[52:55],v[140:143]\nv_rcp_f32 v44, v44\nxM v[140:143],v[176:179],a[68:71],v[140:143]\nxT v[194:195],v210 offset:47104\nxT v[196:197],v211 offset:36864\n"
"xM v[140:143],v[180:183],a[84:87],v[140:143]\nxA m0,s32,37888\nv_rcp_f32 v45, v45\n"
"xM v[148:151],v[172:175],a[56:59],v[148:151]\nxA s39,s33,3612800\nxU v76,v76,v44\nxU v77,v77,v45\n"
"xM v[148:151],v[176:179],a[72:75],v[148:151]\nxL v217,s[20:23],s39 offen lds\nxM v[148:151],v[180:183],a[88:91],v[148:151]\nxT v[198:199],v211 offset:38912\nxC v74,v76,v77\n"
"xM v[156:159],v[172:175],a[60:63],v[156:159]\nxU v78,v46,v78\nxU v79,v47,v79\n"
"xM v[156:159],v[176:179],a[76:79],v[156:159]\nxW lgkmcnt(3)\nxT v[160:161],v211 offset:40960\nxT v[162:163],v211 offset:43008\n"
"xM v[156:159],v[180:183],a[92:95],v[156:159]\nxA m0,s32,38912\nxE v46,-v46\n"
"xM v[96:99],v[184:187],a[96:99],v[96:99]\nxA s39,s33,3686528\nxE v47,-v47\n"
"xM v[96:99],v[188:191],a[112:115],v[96:99]\nxW lgkmcnt(3)\nxL v217,s[20:23],s39 offen lds\n"
"xM v[96:99],v[192:195],a[128:131],v[96:99]\nxT v[164:165],v211 offset:45056\nv_add_f32 v46, 1.0, v46\n"
"xM v[104:107],v[184:187],a[100:103],v[104:107]\nxT v[166:167],v211 offset:47104\nv_add_f32 v47, 1.0, v47\n"
"xM v[104:107],v[188:191],a[116:119],v[104:107]\nv_rcp_f32 v46, v46\nxM v[104:107],v[192:195],a[132:135],v[104:107]\nxA m0,s32,49152\nv_rcp_f32 v47, v47\n"
"xM v[112:115],v[184:187],a[104:107],v[112:115]\nxA s39,s33,4423808\nxT v[168:169],v210 offset:49152\nxU v78,v78,v46\n"
"xM v[112:115],v[188:191],a[120:123],v[112:115]\nTCH1_3_%=:\ns_cbranch_execz TC1_3_1_%=\nxL v217,s[16:19],s39 offen lds\n"
"xM v[112:115],v[192:195],a[136:139],v[112:115]\nxT v[170:171],v210 offset:51200\nxU v79,v79,v47\n"
"xM v[120:123],v[184:187],a[108:111],v[120:123]\nxT v[172:173],v210 offset:53248\nxC v75,v78,v79\n"
"xM v[120:123],v[188:191],a[124:127],v[120:123]\nxW lgkmcnt(4)\nds_write_b128 v226, v[72:75] offset:1024\n"
"xM v[120:123],v[192:195],a[140:143],v[120:123]\nxA m0,s32,50176\nxS v[72:75],v226,s[96:99],s62 offen offset:1024 sc1\n"
"xM v[100:103],v[196:199],a[96:99],v[100:103]\nxA s39,s33,4497536\nxT v[174:175],v210 offset:55296\nxU v80,v48,v80\n"
"xM v[100:103],v[160:163],a[112:115],v[100:103]\nxW lgkmcnt(5)\nxL v217,s[16:19],s39 offen lds\n"
"xM v[100:103],v[164:167],a[128:131],v[100:103]\nxT v[176:177],v210 offset:57344\nxU v81,v49,v81\n"
"xM v[108:111],v[196:199],a[100:103],v[108:111]\nxE v48,-v48\nxM v[108:111],v[160:163],a[116:119],v[108:111]\nxT v[178:179],v210 offset:59392\nxT v[180:181],v211 offset:49152\n"
"xM v[108:111],v[164:167],a[132:135],v[108:111]\nxA m0,s32,51200\nxE v49,-v49\n"
"xM v[116:119],v[196:199],a[104:107],v[116:119]\nxA s39,s33,4571264\nv_add_f32 v48, 1.0, v48\nv_add_f32 v49, 1.0, v49\n"
"xM v[116:119],v[160:163],a[120:123],v[116:119]\nxL v217,s[16:19],s39 offen lds\nxM v[116:119],v[164:167],a[136:139],v[116:119]\nxT v[182:183],v211 offset:51200\nxT v[184:185],v211 offset:53248\n"
"xM v[124:127],v[196:199],a[108:111],v[124:127]\nv_rcp_f32 v48, v48\nxM v[124:127],v[160:163],a[124:127],v[124:127]\nxW lgkmcnt(4)\nv_rcp_f32 v49, v49\n"
"xM v[124:127],v[164:167],a[140:143],v[124:127]\nxA m0,s32,61440\nxU v80,v80,v48\nxU v81,v81,v49\n"
"xM v[128:131],v[168:171],a[96:99],v[128:131]\nxA s39,s33,4423808\nxT v[186:187],v211 offset:55296\nxC v80,v80,v81\n"
"xM v[128:131],v[172:175],a[112:115],v[128:131]\nxW lgkmcnt(3)\nxL v217,s[20:23],s39 offen lds\n"
"xM v[128:131],v[176:179],a[128:131],v[128:131]\nxT v[188:189],v211 offset:57344\nxU v82,v50,v82\n"
"xM v[136:139],v[168:171],a[100:103],v[136:139]\nxT v[190:191],v211 offset:59392\nxU v83,v51,v83\n"
"xM v[136:139],v[172:175],a[116:119],v[136:139]\nxW vmcnt(11)\ns_barrier\n"
"xM v[136:139],v[176:179],a[132:135],v[136:139]\nxA m0,s32,62464\nxE v50,-v50\n"
"xM v[144:147],v[168:171],a[104:107],v[144:147]\nxA s39,s33,4497536\nxT v[192:193],v212 offset:0\nxT v[194:195],v212 offset:2048\n"
"xM v[144:147],v[172:175],a[120:123],v[144:147]\nxL v217,s[20:23],s39 offen lds\nxM v[144:147],v[176:179],a[136:139],v[144:147]\nxE v51,-v51\n"
"xM v[152:155],v[168:171],a[108:111],v[152:155]\nv_add_f32 v50, 1.0, v50\nv_add_f32 v51, 1.0, v51\n"
"xM v[152:155],v[172:175],a[124:127],v[152:155]\nxW lgkmcnt(3)\nxT v[196:197],v212 offset:4096\nxT v[198:199],v212 offset:6144\n"
"xM v[152:155],v[176:179],a[140:143],v[152:155]\nxA m0,s32,63488\nv_rcp_f32 v50, v50\n"
"xM v[132:135],v[180:183],a[96:99],v[132:135]\nxA s39,s33,4571264\nv_rcp_f32 v51, v51\n"
"xM v[132:135],v[184:187],a[112:115],v[132:135]\nxW lgkmcnt(4)\nxL v217,s[20:23],s39 offen lds\n"
"xM v[132:135],v[188:191],a[128:131],v[132:135]\nxT v[160:161],v212 offset:8192\nxU v82,v82,v50\n"
"xM v[140:143],v[180:183],a[100:103],v[140:143]\nxU v83,v83,v51\nxC v81,v82,v83\n"
"xM v[140:143],v[184:187],a[116:119],v[140:143]\nxT v[162:163],v212 offset:10240\nxU v84,v52,v84\n"
"xM v[140:143],v[188:191],a[132:135],v[140:143]\nxA m0,s32,73728\nxT v[164:165],v213 offset:0\nxU v85,v53,v85\n"
"xM v[148:151],v[180:183],a[104:107],v[148:151]\nxA s39,s33,256\nxE v52,-v52\n"
"xM v[148:151],v[184:187],a[120:123],v[148:151]\nxL v217,s[16:19],s39 offen lds\nxM v[148:151],v[188:191],a[136:139],v[148:151]\nxT v[166:167],v213 offset:2048\nxT v[168:169],v213 offset:4096\n"
"xM v[156:159],v[180:183],a[108:111],v[156:159]\nxE v53,-v53\nxM v[156:159],v[184:187],a[124:127],v[156:159]\nxW lgkmcnt(4)\nv_add_f32 v52, 1.0, v52\nv_add_f32 v53, 1.0, v53\n"
"xM v[156:159],v[188:191],a[140:143],v[156:159]\nxA m0,s32,74752\nv_rcp_f32 v52, v52\n"
"xM v[96:99],v[192:195],a[144:147],v[96:99]\nxA s39,s33,73984\nxT v[170:171],v213 offset:6144\nxT v[172:173],v213 offset:8192\n"
"xM v[96:99],v[196:199],a[160:163],v[96:99]\nxW lgkmcnt(4)\nxL v217,s[16:19],s39 offen lds\n"
"xM v[96:99],v[160:163],a[176:179],v[96:99]\nv_rcp_f32 v53, v53\nxM v[104:107],v[192:195],a[148:151],v[104:107]\nxU v84,v84,v52\nxU v85,v85,v53\n"
"xM v[104:107],v[196:199],a[164:167],v[104:107]\nxT v[174:175],v213 offset:10240\nxC v82,v84,v85\n"
"xM v[104:107],v[160:163],a[180:183],v[104:107]\nxA m0,s32,75776\nxU v86,v54,v86\nxU v87,v55,v87\n"
"xM v[112:115],v[192:195],a[152:155],v[112:115]\nxA s39,s33,147712\nxT v[176:177],v212 offset:12288\nxT v[178:179],v212 offset:14336\n"
"xM v[112:115],v[196:199],a[168:171],v[112:115]\nxL v217,s[16:19],s39 offen lds\nxM v[112:115],v[160:163],a[184:187],v[112:115]\nxE v54,-v54\n"
"xM v[120:123],v[192:195],a[156:159],v[120:123]\nxE v55,-v55\nxM v[120:123],v[196:199],a[172:175],v[120:123]\nxW lgkmcnt(3)\nxT v[180:181],v212 offset:16384\nv_add_f32 v54, 1.0, v54\n"
"xM v[120:123],v[160:163],a[188:191],v[120:123]\nxA m0,s32,147456\nxT v[182:183],v212 offset:18432\nv_add_f32 v55, 1.0, v55\n"
"xM v[100:103],v[164:167],a[144:147],v[100:103]\nxA s39,s33,256\nv_rcp_f32 v54, v54\n"
"xM v[100:103],v[168:171],a[160:163],v[100:103]\nxW lgkmcnt(4)\nxL v217,s[20:23],s39 offen lds\n"
"xM v[100:103],v[172:175],a[176:179],v[100:103]\nxT v[184:185],v212 offset:20480\nxT v[186:187],v212 offset:22528\n"
"xM v[108:111],v[164:167],a[148:151],v[108:111]\nv_rcp_f32 v55, v55\nxM v[108:111],v[168:171],a[164:167],v[108:111]\nxU v86,v86,v54\nxU v87,v87,v55\n"
"xM v[108:111],v[172:175],a[180:183],v[108:111]\nxA m0,s32,148480\nxT v[188:189],v213 offset:12288\nxC v83,v86,v87\n"
"xM v[116:119],v[164:167],a[152:155],v[116:119]\nxA s39,s33,73984\nds_write_b128 v226, v[80:83] offset:2048\n"
"xM v[116:119],v[168:171],a[168:171],v[116:119]\nxL v217,s[20:23],s39 offen lds\nxM v[116:119],v[172:175],a[184:187],v[116:119]\nxT v[190:191],v213 offset:14336\nxT v[192:193],v213 offset:16384\n"
"xM v[124:127],v[164:167],a[156:159],v[124:127]\nxS v[80:83],v226,s[96:99],s62 offen offset:2048 sc1\nxM v[124:127],v[168:171],a[172:175],v[124:127]\nxW lgkmcnt(4)\nxU v88,v56,v88\nxU v89,v57,v89\n"
"xM v[124:127],v[172:175],a[188:191],v[124:127]\nxA m0,s32,149504\nxE v56,-v56\n"
"xM v[128:131],v[176:179],a[144:147],v[128:131]\nxA s39,s33,147712\nxT v[194:195],v213 offset:18432\nxT v[196:197],v213 offset:20480\n"
"xM v[128:131],v[180:183],a[160:163],v[128:131]\nxL v217,s[20:23],s39 offen lds\nxM v[128:131],v[184:187],a[176:179],v[128:131]\nxE v57,-v57\n"
"xM v[136:139],v[176:179],a[148:151],v[136:139]\nv_add_f32 v56, 1.0, v56\nv_add_f32 v57, 1.0, v57\n"
"xM v[136:139],v[180:183],a[164:167],v[136:139]\nxW vmcnt(14)\ns_barrier\n"
"xM v[136:139],v[184:187],a[180:183],v[136:139]\nxA m0,s32,86016\nxT v[198:199],v213 offset:22528\nxT v[160:161],v208 offset:24576\n"
"xM v[144:147],v[176:179],a[152:155],v[144:147]\nxA s39,s33,884992\nv_rcp_f32 v56, v56\n"
"xM v[144:147],v[180:183],a[168:171],v[144:147]\nxL v217,s[16:19],s39 offen lds\nxM v[144:147],v[184:187],a[184:187],v[144:147]\nxT v[162:163],v208 offset:26624\nxT v[164:165],v208 offset:28672\n"
"xM v[152:155],v[176:179],a[156:159],v[152:155]\nv_rcp_f32 v57, v57\nxM v[152:155],v[180:183],a[172:175],v[152:155]\nxW lgkmcnt(4)\nxU v88,v88,v56\nxU v89,v89,v57\n"
"xM v[152:155],v[184:187],a[188:191],v[152:155]\nxA m0,s32,87040\nxC v88,v88,v89\nxU v90,v58,v90\n"
"xM v[132:135],v[188:191],a[144:147],v[132:135]\nxA s39,s33,958720\nxT v[166:167],v208 offset:30720\nxU v91,v59,v91\n"
"xM v[132:135],v[192:195],a[160:163],v[132:135]\nxW lgkmcnt(3)\nxL v217,s[16:19],s39 offen lds\n"
"xM v[132:135],v[196:199],a[176:179],v[132:135]\nxT v[168:169],v208 offset:32768\nxT v[170:171],v208 offset:34816\n"
"xM v[140:143],v[188:191],a[148:151],v[140:143]\nxE v58,-v58\nxM v[140:143],v[192:195],a[164:167],v[140:143]\nxE v59,-v59\n"
"xM v[140:143],v[196:199],a[180:183],v[140:143]\nxA m0,s32,88064\nv_add_f32 v58, 1.0, v58\nv_add_f32 v59, 1.0, v59\n"
"xM v[148:151],v[188:191],a[152:155],v[148:151]\nxA s39,s33,1032448\nxT v[172:173],v209 offset:24576\nxT v[174:175],v209 offset:26624\n"
"xM v[148:151],v[192:195],a[168:171],v[148:151]\nxL v217,s[16:19],s39 offen lds\nxM v[148:151],v[196:199],a[184:187],v[148:151]\nv_rcp_f32 v58, v58\n"
"xM v[156:159],v[188:191],a[156:159],v[156:159]\nv_rcp_f32 v59, v59\nxM v[156:159],v[192:195],a[172:175],v[156:159]\nxW lgkmcnt(2)\nxT v[176:177],v209 offset:28672\nxU v90,v90,v58\n"
"xM v[156:159],v[196:199],a[188:191],v[156:159]\nxA m0,s32,98304\nxU v91,v91,v59\nxC v89,v90,v91\n"
"xM v[96:99],v[160:163],a[192:195],v[96:99]\nxA s39,s33,884992\nxT v[178:179],v209 offset:30720\nxU v92,v60,v92\n"
"xM v[96:99],v[164:167],a[208:211],v[96:99]\nxL v217,s[20:23],s39 offen lds\nxM v[96:99],v[168:171],a[224:227],v[96:99]\nxT v[180:181],v209 offset:32768\nxU v93,v61,v93\n"
"xM v[104:107],v[160:163],a[196:199],v[104:107]\nxE v60,-v60\nxM v[104:107],v[164:167],a[212:215],v[104:107]\nxT v[182:183],v209 offset:34816\nxT v[184:185],v208 offset:36864\n"
"xM v[104:107],v[168:171],a[228:231],v[104:107]\nxA m0,s32,99328\nxE v61,-v61\n"
"xM v[112:115],v[160:163],a[200:203],v[112:115]\nxA s39,s33,958720\nv_add_f32 v60, 1.0, v60\nv_add_f32 v61, 1.0, v61\n"
"xM v[112:115],v[164:167],a[216:219],v[112:115]\nxL v217,s[20:23],s39 offen lds\nxM v[112:115],v[168:171],a[232:235],v[112:115]\nxT v[186:187],v208 offset:38912\nxT v[188:189],v208 offset:40960\n"
"xM v[120:123],v[160:163],a[204:207],v[120:123]\nv_rcp_f32 v60, v60\nxM v[120:123],v[164:167],a[220:223],v[120:123]\nxW lgkmcnt(4)\nv_rcp_f32 v61, v61\n"
"xM v[120:123],v[168:171],a[236:239],v[120:123]\nxA m0,s32,100352\nxU v92,v92,v60\nxU v93,v93,v61\n"
"xM v[100:103],v[172:175],a[192:195],v[100:103]\nxA s39,s33,1032448\nxT v[190:191],v208 offset:43008\nxC v90,v92,v93\n"
"xM v[100:103],v[176:179],a[208:211],v[100:103]\nxW lgkmcnt(3)\nxL v217,s[20:23],s39 offen lds\n"
"xM v[100:103],v[180:183],a[224:227],v[100:103]\nxT v[192:193],v208 offset:45056\nxU v94,v62,v94\n"
"xM v[108:111],v[172:175],a[196:199],v[108:111]\nxT v[194:195],v208 offset:47104\nxU v95,v63,v95\n"
"xM v[108:111],v[176:179],a[212:215],v[108:111]\nxE v62,-v62\nxM v[108:111],v[180:183],a[228:231],v[108:111]\nxA m0,s32,110592\nxE v63,-v63\n"
"xM v[116:119],v[172:175],a[200:203],v[116:119]\nxA s39,s33,1769728\nxT v[196:197],v209 offset:36864\nv_add_f32 v62, 1.0, v62\n"
"xM v[116:119],v[176:179],a[216:219],v[116:119]\nxL v217,s[16:19],s39 offen lds\nxM v[116:119],v[180:183],a[232:235],v[116:119]\nxT v[198:199],v209 offset:38912\nv_add_f32 v63, 1.0, v63\n"
"xM v[124:127],v[172:175],a[204:207],v[124:127]\nv_rcp_f32 v62, v62\nxM v[124:127],v[176:179],a[220:223],v[124:127]\nxW lgkmcnt(3)\nxT v[160:161],v209 offset:40960\nxT v[162:163],v209 offset:43008\n"
"xM v[124:127],v[180:183],a[236:239],v[124:127]\nxA m0,s32,111616\nv_rcp_f32 v63, v63\n"
"xM v[128:131],v[184:187],a[192:195],v[128:131]\nxA s39,s33,1843456\nxU v94,v94,v62\nxU v95,v95,v63\n"
"xM v[128:131],v[188:191],a[208:211],v[128:131]\nxW lgkmcnt(4)\nxL v217,s[16:19],s39 offen lds\n"
"xM v[128:131],v[192:195],a[224:227],v[128:131]\nxT v[164:165],v209 offset:45056\nxC v91,v94,v95\n"
"xM v[136:139],v[184:187],a[196:199],v[136:139]\nds_write_b128 v226, v[88:91] offset:3072\nxM v[136:139],v[188:191],a[212:215],v[136:139]\nxW vmcnt(15)\ns_barrier\n"
"xM v[136:139],v[192:195],a[228:231],v[136:139]\nxA m0,s32,112640\nxT v[166:167],v209 offset:47104\nxT v[168:169],v208 offset:49152\n"
"xM v[144:147],v[184:187],a[200:203],v[144:147]\nxA s39,s33,1917184\nxS v[88:91],v226,s[96:99],s62 offen offset:3072 sc1\n"
"xM v[144:147],v[188:191],a[216:219],v[144:147]\nxL v217,s[16:19],s39 offen lds\nxM v[144:147],v[192:195],a[232:235],v[144:147]\nxT v[170:171],v208 offset:51200\nxA s62,s62,16384\n"
"xM v[152:155],v[184:187],a[204:207],v[152:155]\nxT v[172:173],v208 offset:53248\nxT v[174:175],v208 offset:55296\n"
"xM v[152:155],v[188:191],a[220:223],v[152:155]\nxW lgkmcnt(6)\nxT v[176:177],v208 offset:57344\nxT v[178:179],v208 offset:59392\n"
"xM v[152:155],v[192:195],a[236:239],v[152:155]\nxA m0,s32,122880\nxT v[180:181],v209 offset:49152\n"
"xM v[132:135],v[196:199],a[192:195],v[132:135]\nxA s39,s33,1769728\nxT v[182:183],v209 offset:51200\n"
"xM v[132:135],v[160:163],a[208:211],v[132:135]\nxW lgkmcnt(7)\nxL v217,s[20:23],s39 offen lds\n"
"xM v[132:135],v[164:167],a[224:227],v[132:135]\nxT v[184:185],v209 offset:53248\nxM v[140:143],v[196:199],a[196:199],v[140:143]\n"
"xM v[140:143],v[160:163],a[212:215],v[140:143]\nxT v[186:187],v209 offset:55296\nxM v[140:143],v[164:167],a[228:231],v[140:143]\nxA m0,s32,123904\n"
"xM v[148:151],v[196:199],a[200:203],v[148:151]\nxA s39,s33,1843456\nxT v[188:189],v209 offset:57344\n"
"xM v[148:151],v[160:163],a[216:219],v[148:151]\nxL v217,s[20:23],s39 offen lds\nxM v[148:151],v[164:167],a[232:235],v[148:151]\nxT v[190:191],v209 offset:59392\n"
"xM v[156:159],v[196:199],a[204:207],v[156:159]\nxM v[156:159],v[160:163],a[220:223],v[156:159]\nxW lgkmcnt(3)\nxT v[192:193],v210 offset:0\n"
"xM v[156:159],v[164:167],a[236:239],v[156:159]\nxA m0,s32,124928\nxM v[96:99],v[168:171],a[240:243],v[96:99]\nxA s39,s33,1917184\nxT v[194:195],v210 offset:2048\n"
"xM v[96:99],v[172:175],v[0:3],v[96:99]\nxL v217,s[20:23],s39 offen lds\nxM v[96:99],v[176:179],v[16:19],v[96:99]\nxT v[196:197],v210 offset:4096\n"
"xM v[104:107],v[168:171],a[244:247],v[104:107]\nxM v[104:107],v[172:175],v[4:7],v[104:107]\nxT v[198:199],v210 offset:6144\n"
"xM v[104:107],v[176:179],v[20:23],v[104:107]\nxA m0,s32,135168\nxM v[112:115],v[168:171],a[248:251],v[112:115]\nxA s39,s33,2654464\nxT v[160:161],v210 offset:8192\n"
"xM v[112:115],v[172:175],v[8:11],v[112:115]\nxL v217,s[16:19],s39 offen lds\nxM v[112:115],v[176:179],v[24:27],v[112:115]\nxT v[162:163],v210 offset:10240\n"
"xM v[120:123],v[168:171],a[252:255],v[120:123]\nxM v[120:123],v[172:175],v[12:15],v[120:123]\nxT v[164:165],v211 offset:0\n"
"xM v[120:123],v[176:179],v[28:31],v[120:123]\nxA m0,s32,136192\nxM v[100:103],v[180:183],a[240:243],v[100:103]\nxA s39,s33,2728192\nxT v[166:167],v211 offset:2048\nxW lgkmcnt(10)\n"
"xM v[100:103],v[184:187],v[0:3],v[100:103]\nxW lgkmcnt(4)\nxL v217,s[16:19],s39 offen lds\n"
"xM v[100:103],v[188:191],v[16:19],v[100:103]\nxT v[168:169],v211 offset:4096\nxM v[108:111],v[180:183],a[244:247],v[108:111]\n"
"xM v[108:111],v[184:187],v[4:7],v[108:111]\nxT v[170:171],v211 offset:6144\nxM v[108:111],v[188:191],v[20:23],v[108:111]\nxA m0,s32,137216\n"
"xM v[116:119],v[180:183],a[248:251],v[116:119]\nxA s39,s33,2801920\nxT v[172:173],v211 offset:8192\n"
"xM v[116:119],v[184:187],v[8:11],v[116:119]\nxL v217,s[16:19],s39 offen lds\nxM v[116:119],v[188:191],v[24:27],v[116:119]\nxT v[174:175],v211 offset:10240\n"
"xM v[124:127],v[180:183],a[252:255],v[124:127]\nxM v[124:127],v[184:187],v[12:15],v[124:127]\nxM v[124:127],v[188:191],v[28:31],v[124:127]\n"
"xM v[128:131],v[192:195],a[240:243],v[128:131]\nxM v[128:131],v[196:199],v[0:3],v[128:131]\nxW lgkmcnt(2)\n"
"xM v[128:131],v[160:163],v[16:19],v[128:131]\nxM v[136:139],v[192:195],a[244:247],v[136:139]\nxM v[136:139],v[196:199],v[4:7],v[136:139]\nxW vmcnt(16) lgkmcnt(15)\ns_barrier\n"
"xM v[136:139],v[160:163],v[20:23],v[136:139]\nxA m0,s32,36864\nxT v[176:177],v210 offset:12288\nxT v[178:179],v210 offset:14336\n"
"xM v[144:147],v[192:195],a[248:251],v[144:147]\nxA s39,s33,2654464\nxT v[180:181],v210 offset:16384\nxT v[182:183],v210 offset:18432\n"
"xM v[144:147],v[196:199],v[8:11],v[144:147]\nxL v217,s[20:23],s39 offen lds\nxM v[144:147],v[160:163],v[24:27],v[144:147]\nxT v[184:185],v210 offset:20480\nxT v[186:187],v210 offset:22528\n"
"xM v[152:155],v[192:195],a[252:255],v[152:155]\nxM v[152:155],v[196:199],v[12:15],v[152:155]\nxT v[188:189],v211 offset:12288\n"
"xM v[152:155],v[160:163],v[28:31],v[152:155]\nxA m0,s32,37888\nxM v[132:135],v[164:167],a[240:243],v[132:135]\nxA s39,s33,2728192\nxT v[190:191],v211 offset:14336\n"
"xM v[132:135],v[168:171],v[0:3],v[132:135]\nxW lgkmcnt(6)\nxL v217,s[20:23],s39 offen lds\n"
"xM v[132:135],v[172:175],v[16:19],v[132:135]\nxT v[192:193],v211 offset:16384\nxM v[140:143],v[164:167],a[244:247],v[140:143]\n"
"xM v[140:143],v[168:171],v[4:7],v[140:143]\nxT v[194:195],v211 offset:18432\nxM v[140:143],v[172:175],v[20:23],v[140:143]\nxA m0,s32,38912\n"
"xM v[148:151],v[164:167],a[248:251],v[148:151]\nxA s39,s33,2801920\nxT v[196:197],v211 offset:20480\n"
"xM v[148:151],v[168:171],v[8:11],v[148:151]\nxL v217,s[20:23],s39 offen lds\nxM v[148:151],v[172:175],v[24:27],v[148:151]\nxT v[198:199],v211 offset:22528\n"
"xM v[156:159],v[164:167],a[252:255],v[156:159]\nxM v[156:159],v[168:171],v[12:15],v[156:159]\nxT v[160:161],v212 offset:24576\n"
"xM v[156:159],v[172:175],v[28:31],v[156:159]\nxA m0,s32,49152\nxM v[32:35],v[176:179],a[0:3],0\nxA s39,s33,3539200\nxT v[162:163],v212 offset:26624\nxW lgkmcnt(10)\n"
"xM v[32:35],v[180:183],a[16:19],v[32:35]\nxW lgkmcnt(4)\nxL v217,s[16:19],s39 offen lds\n"
"xM v[32:35],v[184:187],a[32:35],v[32:35]\nxT v[164:165],v212 offset:28672\nxM v[40:43],v[176:179],a[4:7],0\nxU v128,v96,v128\nxU v129,v97,v129\n"
"xM v[40:43],v[180:183],a[20:23],v[40:43]\nxE v96,-v96\nxM v[40:43],v[184:187],a[36:39],v[40:43]\nxA m0,s32,50176\nxE v97,-v97\n"
"xM v[48:51],v[176:179],a[8:11],0\nxA s39,s33,3612928\nv_add_f32 v96, 1.0, v96\nv_add_f32 v97, 1.0, v97\n"
"xM v[48:51],v[180:183],a[24:27],v[48:51]\nxL v217,s[16:19],s39 offen lds\nxM v[48:51],v[184:187],a[40:43],v[48:51]\nv_rcp_f32 v96, v96\n"
"xM v[56:59],v[176:179],a[12:15],0\nv_rcp_f32 v97, v97\nxM v[56:59],v[180:183],a[28:31],v[56:59]\nxU v128,v128,v96\nxU v129,v129,v97\n"
"xM v[56:59],v[184:187],a[44:47],v[56:59]\nxA m0,s32,51200\nxC v128,v128,v129\nxU v130,v98,v130\n"
"xM v[36:39],v[188:191],a[0:3],0\nxA s39,s33,3686656\nxT v[166:167],v212 offset:30720\nxU v131,v99,v131\n"
"xM v[36:39],v[192:195],a[16:19],v[36:39]\nxW lgkmcnt(1)\nxL v217,s[16:19],s39 offen lds\n"
"xM v[36:39],v[196:199],a[32:35],v[36:39]\nxT v[168:169],v212 offset:32768\nxT v[170:171],v212 offset:34816\n"
"xM v[44:47],v[188:191],a[4:7],0\nxE v98,-v98\nxM v[44:47],v[192:195],a[20:23],v[44:47]\nxE v99,-v99\n"
"xM v[44:47],v[196:199],a[36:39],v[44:47]\nv_add_f32 v98, 1.0, v98\nv_add_f32 v99, 1.0, v99\n"
"xM v[52:55],v[188:191],a[8:11],0\nxT v[172:173],v213 offset:24576\nxT v[174:175],v213 offset:26624\n"
"xM v[52:55],v[192:195],a[24:27],v[52:55]\nv_rcp_f32 v98, v98\nxM v[52:55],v[196:199],a[40:43],v[52:55]\nv_rcp_f32 v99, v99\n"
"xM v[60:63],v[188:191],a[12:15],0\nxU v130,v130,v98\nxU v131,v131,v99\n"
"xM v[60:63],v[192:195],a[28:31],v[60:63]\nxT v[176:177],v213 offset:28672\nxC v129,v130,v131\n"
"xM v[60:63],v[196:199],a[44:47],v[60:63]\nxU v132,v100,v132\nxU v133,v101,v133\n"
"xM v[64:67],v[160:163],a[0:3],0\nxW lgkmcnt(3)\nxT v[178:179],v213 offset:30720\nxT v[180:181],v213 offset:32768\n"
"xM v[64:67],v[164:167],a[16:19],v[64:67]\nTC1_1_1_%=:\ns_cbranch_execz TC1_1_2_%=\nxE v100,-v100\n"
"xM v[64:67],v[168:171],a[32:35],v[64:67]\nxE v101,-v101\nxM v[72:75],v[160:163],a[4:7],0\nv_add_f32 v100, 1.0, v100\nv_add_f32 v101, 1.0, v101\n"
"xM v[72:75],v[164:167],a[20:23],v[72:75]\nxW vmcnt(16)\ns_barrier\n"
"xM v[72:75],v[168:171],a[36:39],v[72:75]\nxA m0,s32,73728\nxT v[182:183],v213 offset:34816\nxT v[184:185],v210 offset:24576\n"
"xM v[80:83],v[160:163],a[8:11],0\nxA s39,s33,3539200\nv_rcp_f32 v100, v100\n"
"xM v[80:83],v[164:167],a[24:27],v[80:83]\nxL v217,s[20:23],s39 offen lds\nxM v[80:83],v[168:171],a[40:43],v[80:83]\nxT v[186:187],v210 offset:26624\nxT v[188:189],v210 offset:28672\n"
"xM v[88:91],v[160:163],a[12:15],0\nv_rcp_f32 v101, v101\nxM v[88:91],v[164:167],a[28:31],v[88:91]\nxW lgkmcnt(4)\nxU v132,v132,v100\nxU v133,v133,v101\n"
"xM v[88:91],v[168:171],a[44:47],v[88:91]\nxA m0,s32,74752\nxC v130,v132,v133\nxU v134,v102,v134\n"
"xM v[68:71],v[172:175],a[0:3],0\nxA s39,s33,3612928\nxT v[190:191],v210 offset:30720\nxU v135,v103,v135\n"
"xM v[68:71],v[176:179],a[16:19],v[68:71]\nxW lgkmcnt(3)\nxL v217,s[20:23],s39 offen lds\n"
"xM v[68:71],v[180:183],a[32:35],v[68:71]\nxT v[192:193],v210 offset:32768\nxT v[194:195],v210 offset:34816\n"
"xM v[76:79],v[172:175],a[4:7],0\nxE v102,-v102\nxM v[76:79],v[176:179],a[20:23],v[76:79]\nxE v103,-v103\n"
"xM v[76:79],v[180:183],a[36:39],v[76:79]\nxA m0,s32,75776\nv_add_f32 v102, 1.0, v102\nv_add_f32 v103, 1.0, v103\n"
"xM v[84:87],v[172:175],a[8:11],0\nxA s39,s33,3686656\nxT v[196:197],v211 offset:24576\nxT v[198:199],v211 offset:26624\n"
"xM v[84:87],v[176:179],a[24:27],v[84:87]\nxL v217,s[20:23],s39 offen lds\nxM v[84:87],v[180:183],a[40:43],v[84:87]\nv_rcp_f32 v102, v102\n"
"xM v[92:95],v[172:175],a[12:15],0\nv_rcp_f32 v103, v103\nxM v[92:95],v[176:179],a[28:31],v[92:95]\nxW lgkmcnt(2)\nxT v[160:161],v211 offset:28672\nxU v134,v134,v102\n"
"xM v[92:95],v[180:183],a[44:47],v[92:95]\nxA m0,s32,61440\nxU v135,v135,v103\nxC v131,v134,v135\n"
"xM v[32:35],v[184:187],a[48:51],v[32:35]\nxA s39,s33,4423936\nxT v[162:163],v211 offset:30720\nxT v[164:165],v211 offset:32768\n"
"xM v[32:35],v[188:191],a[64:67],v[32:35]\nxL v217,s[16:19],s39 offen lds\nxM v[32:35],v[192:195],a[80:83],v[32:35]\nds_write_b128 v226, v[128:131] offset:16384\n"
"xM v[40:43],v[184:187],a[52:55],v[40:43]\nxS v[128:131],v226,s[96:99],s62 offen offset:0 sc1\nxM v[40:43],v[188:191],a[68:71],v[40:43]\nxT v[166:167],v211 offset:34816\nxU v136,v104,v136\n"
"xM v[40:43],v[192:195],a[84:87],v[40:43]\nxA m0,s32,62464\nxT v[168:169],v210 offset:36864\nxU v137,v105,v137\n"
"xM v[48:51],v[184:187],a[56:59],v[48:51]\nxA s39,s33,4497664\nxE v104,-v104\n"
"xM v[48:51],v[188:191],a[72:75],v[48:51]\nxL v217,s[16:19],s39 offen lds\nxM v[48:51],v[192:195],a[88:91],v[48:51]\nxT v[170:171],v210 offset:38912\nxT v[172:173],v210 offset:40960\n"
"xM v[56:59],v[184:187],a[60:63],v[56:59]\nxE v105,-v105\nxM v[56:59],v[188:191],a[76:79],v[56:59]\nxW lgkmcnt(4)\nv_add_f32 v104, 1.0, v104\nv_add_f32 v105, 1.0, v105\n"
"xM v[56:59],v[192:195],a[92:95],v[56:59]\nxA m0,s32,63488\nv_rcp_f32 v104, v104\n"
"xM v[36:39],v[196:199],a[48:51],v[36:39]\nxA s39,s33,4571392\nxT v[174:175],v210 offset:43008\nxT v[176:177],v210 offset:45056\n"
"xM v[36:39],v[160:163],a[64:67],v[36:39]\nxW lgkmcnt(4)\nxL v217,s[16:19],s39 offen lds\n"
"xM v[36:39],v[164:167],a[80:83],v[36:39]\nv_rcp_f32 v105, v105\nxM v[44:47],v[196:199],a[52:55],v[44:47]\nxU v136,v136,v104\nxU v137,v137,v105\n"
"xM v[44:47],v[160:163],a[68:71],v[44:47]\nxT v[178:179],v210 offset:47104\nxC v136,v136,v137\n"
"xM v[44:47],v[164:167],a[84:87],v[44:47]\nxU v138,v106,v138\nxU v139,v107,v139\n"
"xM v[52:55],v[196:199],a[56:59],v[52:55]\nxT v[180:181],v211 offset:36864\nxT v[182:183],v211 offset:38912\n"
"xM v[52:55],v[160:163],a[72:75],v[52:55]\nxE v106,-v106\nxM v[52:55],v[164:167],a[88:91],v[52:55]\nxE v107,-v107\n"
"xM v[60:63],v[196:199],a[60:63],v[60:63]\nv_add_f32 v106, 1.0, v106\nv_add_f32 v107, 1.0, v107\n"
"xM v[60:63],v[160:163],a[76:79],v[60:63]\nxT v[184:185],v211 offset:40960\nxT v[186:187],v211 offset:43008\n"
"xM v[60:63],v[164:167],a[92:95],v[60:63]\nxW lgkmcnt(5)\nv_rcp_f32 v106, v106\n"
"xM v[64:67],v[168:171],a[48:51],v[64:67]\nv_rcp_f32 v107, v107\nxM v[64:67],v[172:175],a[64:67],v[64:67]\nxW lgkmcnt(4)\nxU v138,v138,v106\nxU v139,v139,v107\n"
"xM v[64:67],v[176:179],a[80:83],v[64:67]\nxT v[188:189],v211 offset:45056\nxC v137,v138,v139\n"
"xM v[72:75],v[168:171],a[52:55],v[72:75]\nxU v140,v108,v140\nxU v141,v109,v141\n"
"xM v[72:75],v[172:175],a[68:71],v[72:75]\nxW vmcnt(16)\ns_barrier\n"
"xM v[72:75],v[176:179],a[84:87],v[72:75]\nxA m0,s32,86016\nxT v[190:191],v211 offset:47104\nxT v[192:193],v210 offset:49152\n"
"xM v[80:83],v[168:171],a[56:59],v[80:83]\nxA s39,s33,4423936\nxE v108,-v108\n"
"xM v[80:83],v[172:175],a[72:75],v[80:83]\nxL v217,s[20:23],s39 offen lds\nxM v[80:83],v[176:179],a[88:91],v[80:83]\nxT v[194:195],v210 offset:51200\nxT v[196:197],v210 offset:53248\n"
"xM v[88:91],v[168:171],a[60:63],v[88:91]\nxE v109,-v109\nxM v[88:91],v[172:175],a[76:79],v[88:91]\nxW lgkmcnt(4)\nv_add_f32 v108, 1.0, v108\nv_add_f32 v109, 1.0, v109\n"
"xM v[88:91],v[176:179],a[92:95],v[88:91]\nxA m0,s32,87040\nv_rcp_f32 v108, v108\n"
"xM v[68:71],v[180:183],a[48:51],v[68:71]\nxA s39,s33,4497664\nxT v[198:199],v210 offset:55296\nxT v[160:161],v210 offset:57344\n"
"xM v[68:71],v[184:187],a[64:67],v[68:71]\nxW lgkmcnt(4)\nxL v217,s[20:23],s39 offen lds\n"
"xM v[68:71],v[188:191],a[80:83],v[68:71]\nv_rcp_f32 v109, v109\nxM v[76:79],v[180:183],a[52:55],v[76:79]\nxU v140,v140,v108\nxU v141,v141,v109\n"
"xM v[76:79],v[184:187],a[68:71],v[76:79]\nxT v[162:163],v210 offset:59392\nxC v138,v140,v141\n"
"xM v[76:79],v[188:191],a[84:87],v[76:79]\nxA m0,s32,88064\nxU v142,v110,v142\nxU v143,v111,v143\n"
"xM v[84:87],v[180:183],a[56:59],v[84:87]\nxA s39,s33,4571392\nxT v[164:165],v211 offset:49152\nxT v[166:167],v211 offset:51200\n"
"xM v[84:87],v[184:187],a[72:75],v[84:87]\nxL v217,s[20:23],s39 offen lds\nxM v[84:87],v[188:191],a[88:91],v[84:87]\nxE v110,-v110\n"
"xM v[92:95],v[180:183],a[60:63],v[92:95]\nxE v111,-v111\nxM v[92:95],v[184:187],a[76:79],v[92:95]\nxW lgkmcnt(3)\nxT v[168:169],v211 offset:53248\nv_add_f32 v110, 1.0, v110\n"
"xM v[92:95],v[188:191],a[92:95],v[92:95]\nxA m0,s32,147456\nxT v[170:171],v211 offset:55296\nv_add_f32 v111, 1.0, v111\n"
"xM v[32:35],v[192:195],a[96:99],v[32:35]\nxA s39,s33,384\nv_rcp_f32 v110, v110\n"
"xM v[32:35],v[196:199],a[112:115],v[32:35]\nxW lgkmcnt(4)\nxL v217,s[16:19],s39 offen lds\n"
"xM v[32:35],v[160:163],a[128:131],v[32:35]\nxT v[172:173],v211 offset:57344\nxT v[174:175],v211 offset:59392\n"
"xM v[40:43],v[192:195],a[100:103],v[40:43]\nv_rcp_f32 v111, v111\nxM v[40:43],v[196:199],a[116:119],v[40:43]\nxU v142,v142,v110\nxU v143,v143,v111\n"
"xM v[40:43],v[160:163],a[132:135],v[40:43]\nxA m0,s32,148480\nxT v[176:177],v212 offset:0\nxC v139,v142,v143\n"
"xM v[48:51],v[192:195],a[104:107],v[48:51]\nxA s39,s33,74112\nds_write_b128 v226, v[136:139] offset:17408\n"
"xM v[48:51],v[196:199],a[120:123],v[48:51]\nxL v217,s[16:19],s39 offen lds\nxM v[48:51],v[160:163],a[136:139],v[48:51]\nxT v[178:179],v212 offset:2048\nxT v[180:181],v212 offset:4096\n"
"xM v[56:59],v[192:195],a[108:111],v[56:59]\nxS v[136:139],v226,s[96:99],s62 offen offset:1024 sc1\nxM v[56:59],v[196:199],a[124:127],v[56:59]\nxW lgkmcnt(4)\nxU v144,v112,v144\nxU v145,v113,v145\n"
"xM v[56:59],v[160:163],a[140:143],v[56:59]\nxA m0,s32,149504\nxE v112,-v112\n"
"xM v[36:39],v[164:167],a[96:99],v[36:39]\nxA s39,s33,147840\nxT v[182:183],v212 offset:6144\nxT v[184:185],v212 offset:8192\n"
"xM v[36:39],v[168:171],a[112:115],v[36:39]\nxL v217,s[16:19],s39 offen lds\nxM v[36:39],v[172:175],a[128:131],v[36:39]\nxE v113,-v113\n"
"xM v[44:47],v[164:167],a[100:103],v[44:47]\nv_add_f32 v112, 1.0, v112\nv_add_f32 v113, 1.0, v113\n"
"xM v[44:47],v[168:171],a[116:119],v[44:47]\nxT v[186:187],v212 offset:10240\nxT v[188:189],v213 offset:0\n"
"xM v[44:47],v[172:175],a[132:135],v[44:47]\nv_rcp_f32 v112, v112\nxM v[52:55],v[164:167],a[104:107],v[52:55]\nv_rcp_f32 v113, v113\n"
"xM v[52:55],v[168:171],a[120:123],v[52:55]\nxU v144,v144,v112\nxU v145,v145,v113\n"
"xM v[52:55],v[172:175],a[136:139],v[52:55]\nxT v[190:191],v213 offset:2048\nxC v144,v144,v145\n"
"xM v[60:63],v[164:167],a[108:111],v[60:63]\nxU v146,v114,v146\nxU v147,v115,v147\n"
"xM v[60:63],v[168:171],a[124:127],v[60:63]\nxT v[192:193],v213 offset:4096\nxT v[194:195],v213 offset:6144\n"
"xM v[60:63],v[172:175],a[140:143],v[60:63]\nxW lgkmcnt(5)\nxE v114,-v114\n"
"xM v[64:67],v[176:179],a[96:99],v[64:67]\nxE v115,-v115\nxM v[64:67],v[180:183],a[112:115],v[64:67]\nxW lgkmcnt(3)\nv_add_f32 v114, 1.0, v114\nv_add_f32 v115, 1.0, v115\n"
"xM v[64:67],v[184:187],a[128:131],v[64:67]\nxT v[196:197],v213 offset:8192\nxT v[198:199],v213 offset:10240\n"
"xM v[72:75],v[176:179],a[100:103],v[72:75]\nv_rcp_f32 v114, v114\nxM v[72:75],v[180:183],a[116:119],v[72:75]\nxW vmcnt(17)\ns_barrier\n"
"xM v[72:75],v[184:187],a[132:135],v[72:75]\nxA m0,s32,98304\nv_rcp_f32 v115, v115\n"
"xM v[80:83],v[176:179],a[104:107],v[80:83]\nxA s39,s33,384\nxT v[160:161],v212 offset:12288\nxU v146,v146,v114\n"
"xM v[80:83],v[180:183],a[120:123],v[80:83]\nxL v217,s[20:23],s39 offen lds\nxM v[80:83],v[184:187],a[136:139],v[80:83]\nxT v[162:163],v212 offset:14336\nxU v147,v147,v115\n"
"xM v[88:91],v[176:179],a[108:111],v[88:91]\nxC v145,v146,v147\nxU v148,v116,v148\n"
"xM v[88:91],v[180:183],a[124:127],v[88:91]\nxW lgkmcnt(2)\nxT v[164:165],v212 offset:16384\nxU v149,v117,v149\n"
"xM v[88:91],v[184:187],a[140:143],v[88:91]\nxA m0,s32,99328\nxE v116,-v116\n"
"xM v[68:71],v[188:191],a[96:99],v[68:71]\nxA s39,s33,74112\nxT v[166:167],v212 offset:18432\nxT v[168:169],v212 offset:20480\n"
"xM v[68:71],v[192:195],a[112:115],v[68:71]\nxL v217,s[20:23],s39 offen lds\nxM v[68:71],v[196:199],a[128:131],v[68:71]\nxE v117,-v117\n"
"xM v[76:79],v[188:191],a[100:103],v[76:79]\nv_add_f32 v116, 1.0, v116\nv_add_f32 v117, 1.0, v117\n"
"xM v[76:79],v[192:195],a[116:119],v[76:79]\nxT v[170:171],v212 offset:22528\nxT v[172:173],v213 offset:12288\n"
"xM v[76:79],v[196:199],a[132:135],v[76:79]\nxA m0,s32,100352\nv_rcp_f32 v116, v116\n"
"xM v[84:87],v[188:191],a[104:107],v[84:87]\nxA s39,s33,147840\nv_rcp_f32 v117, v117\n"
"xM v[84:87],v[192:195],a[120:123],v[84:87]\nxL v217,s[20:23],s39 offen lds\nxM v[84:87],v[196:199],a[136:139],v[84:87]\nxT v[174:175],v213 offset:14336\nxU v148,v148,v116\n"
"xM v[92:95],v[188:191],a[108:111],v[92:95]\nxU v149,v149,v117\nxC v146,v148,v149\n"
"xM v[92:95],v[192:195],a[124:127],v[92:95]\nxW lgkmcnt(3)\nxT v[176:177],v213 offset:16384\nxU v150,v118,v150\n"
"xM v[92:95],v[196:199],a[140:143],v[92:95]\nxA m0,s32,110592\nxT v[178:179],v213 offset:18432\nxU v151,v119,v151\n"
"xM v[32:35],v[160:163],a[144:147],v[32:35]\nxA s39,s33,885120\nxE v118,-v118\n"
"xM v[32:35],v[164:167],a[160:163],v[32:35]\nxW lgkmcnt(3)\nxL v217,s[16:19],s39 offen lds\n"
"xM v[32:35],v[168:171],a[176:179],v[32:35]\nxT v[180:181],v213 offset:20480\nxT v[182:183],v213 offset:22528\n"
"xM v[40:43],v[160:163],a[148:151],v[40:43]\nxE v119,-v119\nxM v[40:43],v[164:167],a[164:167],v[40:43]\nv_add_f32 v118, 1.0, v118\nv_add_f32 v119, 1.0, v119\n"
"xM v[40:43],v[168:171],a[180:183],v[40:43]\nxA m0,s32,111616\nv_rcp_f32 v118, v118\n"
"xM v[48:51],v[160:163],a[152:155],v[48:51]\nxA s39,s33,958848\nxT v[184:185],v208 offset:36864\nxT v[186:187],v208 offset:38912\n"
"xM v[48:51],v[164:167],a[168:171],v[48:51]\nxL v217,s[16:19],s39 offen lds\nxM v[48:51],v[168:171],a[184:187],v[48:51]\nv_rcp_f32 v119, v119\n"
"xM v[56:59],v[160:163],a[156:159],v[56:59]\nxU v150,v150,v118\nxU v151,v151,v119\n"
"xM v[56:59],v[164:167],a[172:175],v[56:59]\nxW lgkmcnt(2)\nxT v[188:189],v208 offset:40960\nxC v147,v150,v151\n"
"xM v[56:59],v[168:171],a[188:191],v[56:59]\nxA m0,s32,112640\nds_write_b128 v226, v[144:147] offset:18432\n"
"xM v[36:39],v[172:175],a[144:147],v[36:39]\nxA s39,s33,1032576\nxT v[190:191],v208 offset:43008\nxT v[192:193],v208 offset:45056\n"
"xM v[36:39],v[176:179],a[160:163],v[36:39]\nxL v217,s[16:19],s39 offen lds\nxM v[36:39],v[180:183],a[176:179],v[36:39]\nxS v[144:147],v226,s[96:99],s62 offen offset:2048 sc1\n"
"xM v[44:47],v[172:175],a[148:151],v[44:47]\nxU v152,v120,v152\nxU v153,v121,v153\n"
"xM v[44:47],v[176:179],a[164:167],v[44:47]\nxT v[194:195],v208 offset:47104\nxT v[196:197],v209 offset:36864\n"
"xM v[44:47],v[180:183],a[180:183],v[44:47]\nxE v120,-v120\nxM v[52:55],v[172:175],a[152:155],v[52:55]\nxE v121,-v121\n"
"xM v[52:55],v[176:179],a[168:171],v[52:55]\nv_add_f32 v120, 1.0, v120\nv_add_f32 v121, 1.0, v121\n"
"xM v[52:55],v[180:183],a[184:187],v[52:55]\nxT v[198:199],v209 offset:38912\nxT v[160:161],v209 offset:40960\n"
"xM v[60:63],v[172:175],a[156:159],v[60:63]\nv_rcp_f32 v120, v120\nxM v[60:63],v[176:179],a[172:175],v[60:63]\nv_rcp_f32 v121, v121\n"
"xM v[60:63],v[180:183],a[188:191],v[60:63]\nxW lgkmcnt(4)\nxU v152,v152,v120\nxU v153,v153,v121\n"
"xM v[64:67],v[184:187],a[144:147],v[64:67]\nxT v[162:163],v209 offset:43008\nxC v152,v152,v153\n"
"xM v[64:67],v[188:191],a[160:163],v[64:67]\nxW lgkmcnt(3)\nxU v154,v122,v154\nxU v155,v123,v155\n"
"xM v[64:67],v[192:195],a[176:179],v[64:67]\nxT v[164:165],v209 offset:45056\nxT v[166:167],v209 offset:47104\n"
"xM v[72:75],v[184:187],a[148:151],v[72:75]\nxE v122,-v122\nxM v[72:75],v[188:191],a[164:167],v[72:75]\nxW vmcnt(18)\ns_barrier\n"
"xM v[72:75],v[192:195],a[180:183],v[72:75]\nxA m0,s32,122880\nxE v123,-v123\n"
"xM v[80:83],v[184:187],a[152:155],v[80:83]\nxA s39,s33,885120\nxT v[168:169],v208 offset:49152\nv_add_f32 v122, 1.0, v122\n"
"xM v[80:83],v[188:191],a[168:171],v[80:83]\nxL v217,s[20:23],s39 offen lds\nxM v[80:83],v[192:195],a[184:187],v[80:83]\nxT v[170:171],v208 offset:51200\nv_add_f32 v123, 1.0, v123\n"
"xM v[88:91],v[184:187],a[156:159],v[88:91]\nv_rcp_f32 v122, v122\nxM v[88:91],v[188:191],a[172:175],v[88:91]\nxW lgkmcnt(2)\nxT v[172:173],v208 offset:53248\nxT v[174:175],v208 offset:55296\n"
"xM v[88:91],v[192:195],a[188:191],v[88:91]\nxA m0,s32,123904\nv_rcp_f32 v123, v123\n"
"xM v[68:71],v[196:199],a[144:147],v[68:71]\nxA s39,s33,958848\nxU v154,v154,v122\nxU v155,v155,v123\n"
"xM v[68:71],v[160:163],a[160:163],v[68:71]\nxL v217,s[20:23],s39 offen lds\nxM v[68:71],v[164:167],a[176:179],v[68:71]\nxT v[176:177],v208 offset:57344\nxC v153,v154,v155\n"
"xM v[76:79],v[196:199],a[148:151],v[76:79]\nxU v156,v124,v156\nxU v157,v125,v157\n"
"xM v[76:79],v[160:163],a[164:167],v[76:79]\nxT v[178:179],v208 offset:59392\nxT v[180:181],v209 offset:49152\n"
"xM v[76:79],v[164:167],a[180:183],v[76:79]\nxA m0,s32,124928\nxE v124,-v124\n"
"xM v[84:87],v[196:199],a[152:155],v[84:87]\nxA s39,s33,1032576\nxE v125,-v125\n"
"xM v[84:87],v[160:163],a[168:171],v[84:87]\nxL v217,s[20:23],s39 offen lds\nxM v[84:87],v[164:167],a[184:187],v[84:87]\nxT v[182:183],v209 offset:51200\nv_add_f32 v124, 1.0, v124\n"
"xM v[92:95],v[196:199],a[156:159],v[92:95]\nxT v[184:185],v209 offset:53248\nv_add_f32 v125, 1.0, v125\n"
"xM v[92:95],v[160:163],a[172:175],v[92:95]\nxW lgkmcnt(4)\nv_rcp_f32 v124, v124\n"
"xM v[92:95],v[164:167],a[188:191],v[92:95]\nxA m0,s32,135168\nv_rcp_f32 v125, v125\n"
"xM v[32:35],v[168:171],a[192:195],v[32:35]\nxA s39,s33,1769856\nxT v[186:187],v209 offset:55296\nxU v156,v156,v124\n"
"xM v[32:35],v[172:175],a[208:211],v[32:35]\nxW lgkmcnt(3)\nxL v217,s[16:19],s39 offen lds\n"
"xM v[32:35],v[176:179],a[224:227],v[32:35]\nxT v[188:189],v209 offset:57344\nxU v157,v157,v125\n"
"xM v[40:43],v[168:171],a[196:199],v[40:43]\nxC v154,v156,v157\nxU v158,v126,v158\n"
"xM v[40:43],v[172:175],a[212:215],v[40:43]\nxT v[190:191],v209 offset:59392\nxU v159,v127,v159\n"
"xM v[40:43],v[176:179],a[228:231],v[40:43]\nxA m0,s32,136192\nxE v126,-v126\n"
"xM v[48:51],v[168:171],a[200:203],v[48:51]\nxA s39,s33,1843584\nxT v[192:193],v210 offset:12288\nxT v[194:195],v210 offset:14336\n"
"xM v[48:51],v[172:175],a[216:219],v[48:51]\nxL v217,s[16:19],s39 offen lds\nxM v[48:51],v[176:179],a[232:235],v[48:51]\nxE v127,-v127\n"
"xM v[56:59],v[168:171],a[204:207],v[56:59]\nv_add_f32 v126, 1.0, v126\nv_add_f32 v127, 1.0, v127\n"
"xM v[56:59],v[172:175],a[220:223],v[56:59]\nxW lgkmcnt(3)\nxT v[196:197],v210 offset:16384\nxT v[198:199],v210 offset:18432\n"
"xM v[56:59],v[176:179],a[236:239],v[56:59]\nxA m0,s32,137216\nv_rcp_f32 v126, v126\n"
"xM v[36:39],v[180:183],a[192:195],v[36:39]\nxA s39,s33,1917312\nv_rcp_f32 v127, v127\n"
"xM v[36:39],v[184:187],a[208:211],v[36:39]\nxW lgkmcnt(4)\nxL v217,s[16:19],s39 offen lds\n"
"xM v[36:39],v[188:191],a[224:227],v[36:39]\nxT v[160:161],v210 offset:20480\nxU v158,v158,v126\n"
"xM v[44:47],v[180:183],a[196:199],v[44:47]\nxU v159,v159,v127\nxC v155,v158,v159\n"
"xM v[44:47],v[184:187],a[212:215],v[44:47]\nxT v[162:163],v210 offset:22528\nxT v[164:165],v211 offset:12288\n"
"xM v[44:47],v[188:191],a[228:231],v[44:47]\nds_write_b128 v226, v[152:155] offset:19456\nxM v[52:55],v[180:183],a[200:203],v[52:55]\nxS v[152:155],v226,s[96:99],s62 offen offset:3072 sc1\n"
"xM v[52:55],v[184:187],a[216:219],v[52:55]\nxT v[166:167],v211 offset:14336\nxA s62,s62,16384\n"
"xM v[52:55],v[188:191],a[232:235],v[52:55]\nxT v[168:169],v211 offset:16384\nxT v[170:171],v211 offset:18432\n"
"xM v[60:63],v[180:183],a[204:207],v[60:63]\nxT v[172:173],v211 offset:20480\nxT v[174:175],v211 offset:22528\n"
"xM v[60:63],v[184:187],a[220:223],v[60:63]\nxM v[60:63],v[188:191],a[236:239],v[60:63]\nxW lgkmcnt(8)\n"
"xM v[64:67],v[192:195],a[192:195],v[64:67]\nxM v[64:67],v[196:199],a[208:211],v[64:67]\nxW lgkmcnt(5)\n"
"xM v[64:67],v[160:163],a[224:227],v[64:67]\nxM v[72:75],v[192:195],a[196:199],v[72:75]\nxM v[72:75],v[196:199],a[212:215],v[72:75]\nxW vmcnt(18) lgkmcnt(5)\ns_barrier\n"
"xM v[72:75],v[160:163],a[228:231],v[72:75]\nxA m0,s32,49152\nxT v[176:177],v210 offset:0\nxT v[178:179],v210 offset:2048\n"
"xM v[80:83],v[192:195],a[200:203],v[80:83]\nxA s39,s33,1769856\nxT v[180:181],v210 offset:4096\nxT v[182:183],v210 offset:6144\n"
"xM v[80:83],v[196:199],a[216:219],v[80:83]\nxL v217,s[20:23],s39 offen lds\nxM v[80:83],v[160:163],a[232:235],v[80:83]\nxT v[184:185],v210 offset:8192\nxT v[186:187],v210 offset:10240\n"
"xM v[88:91],v[192:195],a[204:207],v[88:91]\nxM v[88:91],v[196:199],a[220:223],v[88:91]\nxW lgkmcnt(6)\nxT v[188:189],v211 offset:0\n"
"xM v[88:91],v[160:163],a[236:239],v[88:91]\nxA m0,s32,50176\nxM v[68:71],v[164:167],a[192:195],v[68:71]\nxA s39,s33,1843584\nxT v[190:191],v211 offset:2048\n"
"xM v[68:71],v[168:171],a[208:211],v[68:71]\nTC1_2_1_%=:\ns_cbranch_execz TC1_2_2_%=\nxL v217,s[20:23],s39 offen lds\n"
"xM v[68:71],v[172:175],a[224:227],v[68:71]\nxT v[192:193],v211 offset:4096\nxM v[76:79],v[164:167],a[196:199],v[76:79]\n"
"xM v[76:79],v[168:171],a[212:215],v[76:79]\nxT v[194:195],v211 offset:6144\nxM v[76:79],v[172:175],a[228:231],v[76:79]\nxA m0,s32,51200\n"
"xM v[84:87],v[164:167],a[200:203],v[84:87]\nxA s39,s33,1917312\nxT v[196:197],v211 offset:8192\n"
"xM v[84:87],v[168:171],a[216:219],v[84:87]\nxL v217,s[20:23],s39 offen lds\nxM v[84:87],v[172:175],a[232:235],v[84:87]\nxT v[198:199],v211 offset:10240\n"
"xM v[92:95],v[164:167],a[204:207],v[92:95]\nxM v[92:95],v[168:171],a[220:223],v[92:95]\nxT v[160:161],v210 offset:24576\n"
"xM v[92:95],v[172:175],a[236:239],v[92:95]\nxW lgkmcnt(4)\nxM v[32:35],v[176:179],a[240:243],v[32:35]\nxT v[162:163],v210 offset:26624\n"
"xM v[32:35],v[180:183],v[0:3],v[32:35]\nxM v[32:35],v[184:187],v[16:19],v[32:35]\nxT v[164:165],v210 offset:28672\n"
"xM v[40:43],v[176:179],a[244:247],v[40:43]\nxM v[40:43],v[180:183],v[4:7],v[40:43]\nxT v[166:167],v210 offset:30720\n"
"xM v[40:43],v[184:187],v[20:23],v[40:43]\nxM v[48:51],v[176:179],a[248:251],v[48:51]\nxT v[168:169],v210 offset:32768\n"
"xM v[48:51],v[180:183],v[8:11],v[48:51]\nxM v[48:51],v[184:187],v[24:27],v[48:51]\nxT v[170:171],v210 offset:34816\n"
"xM v[56:59],v[176:179],a[252:255],v[56:59]\nxM v[56:59],v[180:183],v[12:15],v[56:59]\nxT v[172:173],v211 offset:24576\n"
"xM v[56:59],v[184:187],v[28:31],v[56:59]\nxM v[36:39],v[188:191],a[240:243],v[36:39]\nxW lgkmcnt(3)\nxT v[174:175],v211 offset:26624\n"
"xM v[36:39],v[192:195],v[0:3],v[36:39]\nxM v[36:39],v[196:199],v[16:19],v[36:39]\nxT v[176:177],v211 offset:28672\n"
"xM v[44:47],v[188:191],a[244:247],v[44:47]\nxM v[44:47],v[192:195],v[4:7],v[44:47]\nxT v[178:179],v211 offset:30720\n"
"xM v[44:47],v[196:199],v[20:23],v[44:47]\nxM v[52:55],v[188:191],a[248:251],v[52:55]\nxT v[180:181],v211 offset:32768\n"
"xM v[52:55],v[192:195],v[8:11],v[52:55]\nxM v[52:55],v[196:199],v[24:27],v[52:55]\nxT v[182:183],v211 offset:34816\n"
"xM v[60:63],v[188:191],a[252:255],v[60:63]\nxM v[60:63],v[192:195],v[12:15],v[60:63]\nxM v[60:63],v[196:199],v[28:31],v[60:63]\n"
"xM v[64:67],v[160:163],a[240:243],v[64:67]\nxM v[64:67],v[164:167],v[0:3],v[64:67]\nxW lgkmcnt(2)\n"
"xM v[64:67],v[168:171],v[16:19],v[64:67]\nxM v[72:75],v[160:163],a[244:247],v[72:75]\nxM v[72:75],v[164:167],v[4:7],v[72:75]\nxW vmcnt(14)\ns_barrier\n"
"xM v[72:75],v[168:171],v[20:23],v[72:75]\nxA m0,s32,73728\nxT v[184:185],v212 offset:24576\nxT v[186:187],v212 offset:26624\n"
"xM v[80:83],v[160:163],a[248:251],v[80:83]\nxA s39,s33,2654592\nxT v[188:189],v212 offset:28672\nxT v[190:191],v212 offset:30720\n"
"xM v[80:83],v[164:167],v[8:11],v[80:83]\nxL v217,s[16:19],s39 offen lds\nxM v[80:83],v[168:171],v[24:27],v[80:83]\nxT v[192:193],v212 offset:32768\nxT v[194:195],v212 offset:34816\n"
"xM v[88:91],v[160:163],a[252:255],v[88:91]\nxM v[88:91],v[164:167],v[12:15],v[88:91]\nxT v[196:197],v213 offset:24576\n"
"xM v[88:91],v[168:171],v[28:31],v[88:91]\nxA m0,s32,74752\nxM v[68:71],v[172:175],a[240:243],v[68:71]\nxA s39,s33,2728320\nxT v[198:199],v213 offset:26624\n"
"xM v[68:71],v[176:179],v[0:3],v[68:71]\nxW lgkmcnt(6)\nxL v217,s[16:19],s39 offen lds\n"
"xM v[68:71],v[180:183],v[16:19],v[68:71]\nxT v[160:161],v213 offset:28672\nxM v[76:79],v[172:175],a[244:247],v[76:79]\n"
"xM v[76:79],v[176:179],v[4:7],v[76:79]\nxT v[162:163],v213 offset:30720\nxM v[76:79],v[180:183],v[20:23],v[76:79]\nxA m0,s32,75776\n"
"xM v[84:87],v[172:175],a[248:251],v[84:87]\nxA s39,s33,2802048\nxT v[164:165],v213 offset:32768\n"
"xM v[84:87],v[176:179],v[8:11],v[84:87]\nxL v217,s[16:19],s39 offen lds\nxM v[84:87],v[180:183],v[24:27],v[84:87]\nxT v[166:167],v213 offset:34816\n"
"xM v[92:95],v[172:175],a[252:255],v[92:95]\nxM v[92:95],v[176:179],v[12:15],v[92:95]\nxT v[168:169],v210 offset:36864\n"
"xM v[92:95],v[180:183],v[28:31],v[92:95]\nxA m0,s32,61440\nxM v[96:99],v[184:187],a[0:3],0\nxA s39,s33,2654592\nxT v[170:171],v210 offset:38912\nxW lgkmcnt(10)\n"
"xM v[96:99],v[188:191],a[16:19],v[96:99]\nxW lgkmcnt(4)\nxL v217,s[20:23],s39 offen lds\n"
"xM v[96:99],v[192:195],a[32:35],v[96:99]\nxT v[172:173],v210 offset:40960\nxM v[104:107],v[184:187],a[4:7],0\nxU v64,v32,v64\nxU v65,v33,v65\n"
"xM v[104:107],v[188:191],a[20:23],v[104:107]\nxE v32,-v32\nxM v[104:107],v[192:195],a[36:39],v[104:107]\nxA m0,s32,62464\nxE v33,-v33\n"
"xM v[112:115],v[184:187],a[8:11],0\nxA s39,s33,2728320\nv_add_f32 v32, 1.0, v32\nv_add_f32 v33, 1.0, v33\n"
"xM v[112:115],v[188:191],a[24:27],v[112:115]\nxL v217,s[20:23],s39 offen lds\nxM v[112:115],v[192:195],a[40:43],v[112:115]\nv_rcp_f32 v32, v32\n"
"xM v[120:123],v[184:187],a[12:15],0\nv_rcp_f32 v33, v33\nxM v[120:123],v[188:191],a[28:31],v[120:123]\nxU v64,v64,v32\nxU v65,v65,v33\n"
"xM v[120:123],v[192:195],a[44:47],v[120:123]\nxA m0,s32,63488\nxC v64,v64,v65\nxU v66,v34,v66\n"
"xM v[100:103],v[196:199],a[0:3],0\nxA s39,s33,2802048\nxT v[174:175],v210 offset:43008\nxU v67,v35,v67\n"
"xM v[100:103],v[160:163],a[16:19],v[100:103]\nxW lgkmcnt(1)\nxL v217,s[20:23],s39 offen lds\n"
"xM v[100:103],v[164:167],a[32:35],v[100:103]\nxT v[176:177],v210 offset:45056\nxT v[178:179],v210 offset:47104\n"
"xM v[108:111],v[196:199],a[4:7],0\nxE v34,-v34\nxM v[108:111],v[160:163],a[20:23],v[108:111]\nxE v35,-v35\n"
"xM v[108:111],v[164:167],a[36:39],v[108:111]\nv_add_f32 v34, 1.0, v34\nv_add_f32 v35, 1.0, v35\n"
"xM v[116:119],v[196:199],a[8:11],0\nxT v[180:181],v211 offset:36864\nxT v[182:183],v211 offset:38912\n"
"xM v[116:119],v[160:163],a[24:27],v[116:119]\nv_rcp_f32 v34, v34\nxM v[116:119],v[164:167],a[40:43],v[116:119]\nv_rcp_f32 v35, v35\n"
"xM v[124:127],v[196:199],a[12:15],0\nxU v66,v66,v34\nxU v67,v67,v35\n"
"xM v[124:127],v[160:163],a[28:31],v[124:127]\nxT v[184:185],v211 offset:40960\nxC v65,v66,v67\n"
"xM v[124:127],v[164:167],a[44:47],v[124:127]\nxU v68,v36,v68\nxU v69,v37,v69\n"
"xM v[128:131],v[168:171],a[0:3],0\nxW lgkmcnt(3)\nxT v[186:187],v211 offset:43008\nxT v[188:189],v211 offset:45056\n"
"xM v[128:131],v[172:175],a[16:19],v[128:131]\nxE v36,-v36\nxM v[128:131],v[176:179],a[32:35],v[128:131]\nxE v37,-v37\n"
"xM v[136:139],v[168:171],a[4:7],0\nv_add_f32 v36, 1.0, v36\nv_add_f32 v37, 1.0, v37\n"
"xM v[136:139],v[172:175],a[20:23],v[136:139]\nxW vmcnt(13)\ns_barrier\n"
"xM v[136:139],v[176:179],a[36:39],v[136:139]\nxA m0,s32,86016\nxT v[190:191],v211 offset:47104\nxT v[192:193],v210 offset:49152\n"
"xM v[144:147],v[168:171],a[8:11],0\nxA s39,s33,3539328\nv_rcp_f32 v36, v36\n"
"xM v[144:147],v[172:175],a[24:27],v[144:147]\nxL v217,s[16:19],s39 offen lds\nxM v[144:147],v[176:179],a[40:43],v[144:147]\nxT v[194:195],v210 offset:51200\nxT v[196:197],v210 offset:53248\n"
"xM v[152:155],v[168:171],a[12:15],0\nv_rcp_f32 v37, v37\nxM v[152:155],v[172:175],a[28:31],v[152:155]\nxW lgkmcnt(4)\nxU v68,v68,v36\nxU v69,v69,v37\n"
"xM v[152:155],v[176:179],a[44:47],v[152:155]\nxA m0,s32,87040\nxC v66,v68,v69\nxU v70,v38,v70\n"
"xM v[132:135],v[180:183],a[0:3],0\nxA s39,s33,3613056\nxT v[198:199],v210 offset:55296\nxU v71,v39,v71\n"
"xM v[132:135],v[184:187],a[16:19],v[132:135]\nxW lgkmcnt(3)\nxL v217,s[16:19],s39 offen lds\n"
"xM v[132:135],v[188:191],a[32:35],v[132:135]\nxT v[160:161],v210 offset:57344\nxT v[162:163],v210 offset:59392\n"
"xM v[140:143],v[180:183],a[4:7],0\nxE v38,-v38\nxM v[140:143],v[184:187],a[20:23],v[140:143]\nxE v39,-v39\n"
"xM v[140:143],v[188:191],a[36:39],v[140:143]\nv_add_f32 v38, 1.0, v38\nv_add_f32 v39, 1.0, v39\n"
"xM v[148:151],v[180:183],a[8:11],0\nxW vmcnt(11)\ns_mov_b64 exec, 1\nbuffer_store_dwordx2 v[242:243], off, s[96:99], s53 sc1\ns_mov_b64 exec, -1\n"
"xM v[148:151],v[184:187],a[24:27],v[148:151]\nxA s53,s53,32\nxT v[164:165],v211 offset:49152\nxT v[166:167],v211 offset:51200\n"
"xM v[148:151],v[188:191],a[40:43],v[148:151]\nxA m0,s32,88064\nv_rcp_f32 v38, v38\n"
"xM v[156:159],v[180:183],a[12:15],0\nxA s39,s33,3686784\nv_rcp_f32 v39, v39\n"
"xM v[156:159],v[184:187],a[28:31],v[156:159]\nxL v217,s[16:19],s39 offen lds\nxM v[156:159],v[188:191],a[44:47],v[156:159]\nxW lgkmcnt(2)\nxT v[168:169],v211 offset:53248\nxU v70,v70,v38\n"
"xM v[96:99],v[192:195],a[48:51],v[96:99]\nxT v[170:171],v211 offset:55296\nxU v71,v71,v39\n"
"xM v[96:99],v[196:199],a[64:67],v[96:99]\nxT v[172:173],v211 offset:57344\nxC v67,v70,v71\n"
"xM v[96:99],v[160:163],a[80:83],v[96:99]\nxA m0,s32,147456\nds_write_b128 v226, v[64:67] offset:32768\n"
"xM v[104:107],v[192:195],a[52:55],v[104:107]\nxA s39,s33,3539328\nxS v[64:67],v226,s[96:99],s62 offen offset:0 sc1\n"
"xM v[104:107],v[196:199],a[68:71],v[104:107]\nxL v217,s[20:23],s39 offen lds\nxM v[104:107],v[160:163],a[84:87],v[104:107]\nxT v[174:175],v211 offset:59392\nxU v72,v40,v72\n"
"xM v[112:115],v[192:195],a[56:59],v[112:115]\nxT v[176:177],v212 offset:0\nxU v73,v41,v73\n"
"xM v[112:115],v[196:199],a[72:75],v[112:115]\nxE v40,-v40\nxM v[112:115],v[160:163],a[88:91],v[112:115]\nxA m0,s32,148480\nxT v[178:179],v212 offset:2048\nxT v[180:181],v212 offset:4096\n"
"xM v[120:123],v[192:195],a[60:63],v[120:123]\nxA s39,s33,3613056\nxE v41,-v41\n"
"xM v[120:123],v[196:199],a[76:79],v[120:123]\nxL v217,s[20:23],s39 offen lds\nxM v[120:123],v[160:163],a[92:95],v[120:123]\nxW lgkmcnt(4)\nv_add_f32 v40, 1.0, v40\nv_add_f32 v41, 1.0, v41\n"
"xM v[100:103],v[164:167],a[48:51],v[100:103]\nxT v[182:183],v212 offset:6144\nxT v[184:185],v212 offset:8192\n"
"xM v[100:103],v[168:171],a[64:67],v[100:103]\nxW lgkmcnt(5)\nv_rcp_f32 v40, v40\n"
"xM v[100:103],v[172:175],a[80:83],v[100:103]\nxA m0,s32,149504\nv_rcp_f32 v41, v41\n"
"xM v[108:111],v[164:167],a[52:55],v[108:111]\nxA s39,s33,3686784\nxU v72,v72,v40\nxU v73,v73,v41\n"
"xM v[108:111],v[168:171],a[68:71],v[108:111]\nxL v217,s[20:23],s39 offen lds\nxM v[108:111],v[172:175],a[84:87],v[108:111]\nxT v[186:187],v212 offset:10240\nxC v72,v72,v73\n"
"xM v[116:119],v[164:167],a[56:59],v[116:119]\nxT v[188:189],v213 offset:0\nxU v74,v42,v74\n"
"xM v[116:119],v[168:171],a[72:75],v[116:119]\nxT v[190:191],v213 offset:2048\nxU v75,v43,v75\n"
"xM v[116:119],v[172:175],a[88:91],v[116:119]\nxE v42,-v42\nxM v[124:127],v[164:167],a[60:63],v[124:127]\nxE v43,-v43\n"
"xM v[124:127],v[168:171],a[76:79],v[124:127]\nxT v[192:193],v213 offset:4096\nv_add_f32 v42, 1.0, v42\n"
"xM v[124:127],v[172:175],a[92:95],v[124:127]\nxW lgkmcnt(4)\nxT v[194:195],v213 offset:6144\nv_add_f32 v43, 1.0, v43\n"
"xM v[128:131],v[176:179],a[48:51],v[128:131]\nv_rcp_f32 v42, v42\nxM v[128:131],v[180:183],a[64:67],v[128:131]\nxW lgkmcnt(4)\nv_rcp_f32 v43, v43\n"
"xM v[128:131],v[184:187],a[80:83],v[128:131]\nxT v[196:197],v213 offset:8192\nxU v74,v74,v42\n"
"xM v[136:139],v[176:179],a[52:55],v[136:139]\nxU v75,v75,v43\nxC v73,v74,v75\n"
"xM v[136:139],v[180:183],a[68:71],v[136:139]\nxW vmcnt(14)\ns_barrier\n"
"xM v[136:139],v[184:187],a[84:87],v[136:139]\nxA m0,s32,98304\nxT v[198:199],v213 offset:10240\nxU v76,v44,v76\n"
"xM v[144:147],v[176:179],a[56:59],v[144:147]\nxA s39,s33,4424064\nxT v[160:161],v212 offset:12288\nxU v77,v45,v77\n"
"xM v[144:147],v[180:183],a[72:75],v[144:147]\nxL v217,s[16:19],s39 offen lds\nxM v[144:147],v[184:187],a[88:91],v[144:147]\nxT v[162:163],v212 offset:14336\nxT v[164:165],v212 offset:16384\n"
"xM v[152:155],v[176:179],a[60:63],v[152:155]\nxE v44,-v44\nxM v[152:155],v[180:183],a[76:79],v[152:155]\nxW lgkmcnt(4)\nxE v45,-v45\n"
"xM v[152:155],v[184:187],a[92:95],v[152:155]\nxA m0,s32,99328\nv_add_f32 v44, 1.0, v44\nv_add_f32 v45, 1.0, v45\n"
"xM v[132:135],v[188:191],a[48:51],v[132:135]\nxA s39,s33,4497792\nxT v[166:167],v212 offset:18432\nxT v[168:169],v212 offset:20480\n"
"xM v[132:135],v[192:195],a[64:67],v[132:135]\nxW lgkmcnt(5)\nxL v217,s[16:19],s39 offen lds\n"
"xM v[132:135],v[196:199],a[80:83],v[132:135]\nv_rcp_f32 v44, v44\nxM v[140:143],v[188:191],a[52:55],v[140:143]\nv_rcp_f32 v45, v45\n"
"xM v[140:143],v[192:195],a[68:71],v[140:143]\nxT v[170:171],v212 offset:22528\nxU v76,v76,v44\n"
"xM v[140:143],v[196:199],a[84:87],v[140:143]\nxA m0,s32,100352\nxU v77,v77,v45\nxC v74,v76,v77\n"
"xM v[148:151],v[188:191],a[56:59],v[148:151]\nxA s39,s33,4571520\nxT v[172:173],v213 offset:12288\nxU v78,v46,v78\n"
"xM v[148:151],v[192:195],a[72:75],v[148:151]\nxL v217,s[16:19],s39 offen lds\nxM v[148:151],v[196:199],a[88:91],v[148:151]\nxT v[174:175],v213 offset:14336\nxU v79,v47,v79\n"
"xM v[156:159],v[188:191],a[60:63],v[156:159]\nxE v46,-v46\nxM v[156:159],v[192:195],a[76:79],v[156:159]\nxW lgkmcnt(3)\nxT v[176:177],v213 offset:16384\nxT v[178:179],v213 offset:18432\n"
"xM v[156:159],v[196:199],a[92:95],v[156:159]\nxA m0,s32,110592\nxE v47,-v47\n"
"xM v[96:99],v[160:163],a[96:99],v[96:99]\nxA s39,s33,4424064\nv_add_f32 v46, 1.0, v46\nv_add_f32 v47, 1.0, v47\n"
"xM v[96:99],v[164:167],a[112:115],v[96:99]\nxW lgkmcnt(4)\nxL v217,s[20:23],s39 offen lds\n"
"xM v[96:99],v[168:171],a[128:131],v[96:99]\nxT v[180:181],v213 offset:20480\nxT v[182:183],v213 offset:22528\n"
"xM v[104:107],v[160:163],a[100:103],v[104:107]\nv_rcp_f32 v46, v46\nxM v[104:107],v[164:167],a[116:119],v[104:107]\nv_rcp_f32 v47, v47\n"
"xM v[104:107],v[168:171],a[132:135],v[104:107]\nxA m0,s32,111616\nxU v78,v78,v46\nxU v79,v79,v47\n"
"xM v[112:115],v[160:163],a[104:107],v[112:115]\nxA s39,s33,4497792\nxT v[184:185],v208 offset:49152\nxC v75,v78,v79\n"
"xM v[112:115],v[164:167],a[120:123],v[112:115]\nxL v217,s[20:23],s39 offen lds\nxM v[112:115],v[168:171],a[136:139],v[112:115]\nxT v[186:187],v208 offset:51200\nxT v[188:189],v208 offset:53248\n"
"xM v[120:123],v[160:163],a[108:111],v[120:123]\nds_write_b128 v226, v[72:75] offset:33792\nxM v[120:123],v[164:167],a[124:127],v[120:123]\nxW lgkmcnt(4)\nxS v[72:75],v226,s[96:99],s62 offen offset:1024 sc1\n"
"xM v[120:123],v[168:171],a[140:143],v[120:123]\nxA m0,s32,112640\nxU v80,v48,v80\nxU v81,v49,v81\n"
"xM v[100:103],v[172:175],a[96:99],v[100:103]\nxA s39,s33,4571520\nxT v[190:191],v208 offset:55296\nxT v[192:193],v208 offset:57344\n"
"xM v[100:103],v[176:179],a[112:115],v[100:103]\nxL v217,s[20:23],s39 offen lds\nxM v[100:103],v[180:183],a[128:131],v[100:103]\nxE v48,-v48\n"
"xM v[108:111],v[172:175],a[100:103],v[108:111]\nxE v49,-v49\nxM v[108:111],v[176:179],a[116:119],v[108:111]\nxT v[194:195],v208 offset:59392\nv_add_f32 v48, 1.0, v48\n"
"xM v[108:111],v[180:183],a[132:135],v[108:111]\nxT v[196:197],v209 offset:49152\nv_add_f32 v49, 1.0, v49\n"
"xM v[116:119],v[172:175],a[104:107],v[116:119]\nv_rcp_f32 v48, v48\nxM v[116:119],v[176:179],a[120:123],v[116:119]\nv_rcp_f32 v49, v49\n"
"xM v[116:119],v[180:183],a[136:139],v[116:119]\nxT v[198:199],v209 offset:51200\nxU v80,v80,v48\n"
"xM v[124:127],v[172:175],a[108:111],v[124:127]\nxU v81,v81,v49\nxC v80,v80,v81\n"
"xM v[124:127],v[176:179],a[124:127],v[124:127]\nxT v[160:161],v209 offset:53248\nxU v82,v50,v82\n"
"xM v[124:127],v[180:183],a[140:143],v[124:127]\nxW lgkmcnt(4)\nxT v[162:163],v209 offset:55296\nxU v83,v51,v83\n"
"xM v[128:131],v[184:187],a[96:99],v[128:131]\nbuffer_load_dwordx2 v[236:237], v238, s[96:99], s37 offen sc1\nxM v[128:131],v[188:191],a[112:115],v[128:131]\nxW lgkmcnt(3)\nxE v50,-v50\n"
"xM v[128:131],v[192:195],a[128:131],v[128:131]\nxT v[164:165],v209 offset:57344\nxT v[166:167],v209 offset:59392\n"
"xM v[136:139],v[184:187],a[100:103],v[136:139]\nxE v51,-v51\nxM v[136:139],v[188:191],a[116:119],v[136:139]\nxW vmcnt(16)\ns_barrier\n"
"xM v[136:139],v[192:195],a[132:135],v[136:139]\nxA m0,s32,122880\nv_add_f32 v50, 1.0, v50\nv_add_f32 v51, 1.0, v51\n"
"xM v[144:147],v[184:187],a[104:107],v[144:147]\nxA s39,s35,0\nxT v[168:169],v210 offset:12288\nxT v[170:171],v210 offset:14336\n"
"xM v[144:147],v[188:191],a[120:123],v[144:147]\nxL v218,s[24:27],s39 offen lds\nxM v[144:147],v[192:195],a[136:139],v[144:147]\nv_rcp_f32 v50, v50\n"
"xM v[152:155],v[184:187],a[108:111],v[152:155]\nv_rcp_f32 v51, v51\nxM v[152:155],v[188:191],a[124:127],v[152:155]\nxT v[172:173],v210 offset:16384\nxU v82,v82,v50\n"
"xM v[152:155],v[192:195],a[140:143],v[152:155]\nxW lgkmcnt(3)\nxU v83,v83,v51\nxC v81,v82,v83\n"
"xM v[132:135],v[196:199],a[96:99],v[132:135]\nxT v[174:175],v210 offset:18432\nxU v84,v52,v84\n"
"xM v[132:135],v[160:163],a[112:115],v[132:135]\nxA m0,s32,123904\nxT v[176:177],v210 offset:20480\nxU v85,v53,v85\n"
"xM v[132:135],v[164:167],a[128:131],v[132:135]\nxA s39,s35,147456\nxE v52,-v52\n"
"xM v[140:143],v[196:199],a[100:103],v[140:143]\nxL v218,s[24:27],s39 offen lds\nxM v[140:143],v[160:163],a[116:119],v[140:143]\nxT v[178:179],v210 offset:22528\nxT v[180:181],v211 offset:12288\n"
"xM v[140:143],v[164:167],a[132:135],v[140:143]\nxE v53,-v53\nxM v[148:151],v[196:199],a[104:107],v[148:151]\nv_add_f32 v52, 1.0, v52\nv_add_f32 v53, 1.0, v53\n"
"xM v[148:151],v[160:163],a[120:123],v[148:151]\nv_rcp_f32 v52, v52\nxM v[148:151],v[164:167],a[136:139],v[148:151]\nxT v[182:183],v211 offset:14336\nxT v[184:185],v211 offset:16384\n"
"xM v[156:159],v[196:199],a[108:111],v[156:159]\nxA m0,s32,124928\nv_rcp_f32 v53, v53\n"
"xM v[156:159],v[160:163],a[124:127],v[156:159]\nxA s39,s35,294912\nxU v84,v84,v52\nxU v85,v85,v53\n"
"xM v[156:159],v[164:167],a[140:143],v[156:159]\nxW lgkmcnt(4)\nxL v218,s[24:27],s39 offen lds\n"
"xM v[96:99],v[168:171],a[144:147],v[96:99]\nxT v[186:187],v211 offset:18432\nxC v82,v84,v85\n"
"xM v[96:99],v[172:175],a[160:163],v[96:99]\nxW lgkmcnt(3)\nxU v86,v54,v86\nxU v87,v55,v87\n"
"xM v[96:99],v[176:179],a[176:179],v[96:99]\nxT v[188:189],v211 offset:20480\nxT v[190:191],v211 offset:22528\n"
"xM v[104:107],v[168:171],a[148:151],v[104:107]\nxE v54,-v54\nxM v[104:107],v[172:175],a[164:167],v[104:107]\nxE v55,-v55\n"
"xM v[104:107],v[176:179],a[180:183],v[104:107]\nxA m0,s32,135168\nv_add_f32 v54, 1.0, v54\nv_add_f32 v55, 1.0, v55\n"
"xM v[112:115],v[168:171],a[152:155],v[112:115]\nxA s39,s35,884736\nxT v[192:193],v210 offset:0\nxT v[194:195],v210 offset:2048\n"
"xM v[112:115],v[172:175],a[168:171],v[112:115]\nxL v218,s[24:27],s39 offen lds\nxM v[112:115],v[176:179],a[184:187],v[112:115]\nv_rcp_f32 v54, v54\n"
"xM v[120:123],v[168:171],a[156:159],v[120:123]\nv_rcp_f32 v55, v55\nxM v[120:123],v[172:175],a[172:175],v[120:123]\nxT v[196:197],v210 offset:4096\nxU v86,v86,v54\n"
"xM v[120:123],v[176:179],a[188:191],v[120:123]\nxW lgkmcnt(3)\nxU v87,v87,v55\nxC v83,v86,v87\n"
"xM v[100:103],v[180:183],a[144:147],v[100:103]\nxT v[198:199],v210 offset:6144\nxT v[160:161],v210 offset:8192\n"
"xM v[100:103],v[184:187],a[160:163],v[100:103]\nxA m0,s32,136192\nds_write_b128 v226, v[80:83] offset:34816\n"
"xM v[100:103],v[188:191],a[176:179],v[100:103]\nxA s39,s35,1032192\nxS v[80:83],v226,s[96:99],s62 offen offset:2048 sc1\n"
"xM v[108:111],v[180:183],a[148:151],v[108:111]\nxL v218,s[24:27],s39 offen lds\nxM v[108:111],v[184:187],a[164:167],v[108:111]\nxT v[162:163],v210 offset:10240\nxU v88,v56,v88\n"
"xM v[108:111],v[188:191],a[180:183],v[108:111]\nxT v[164:165],v211 offset:0\nxU v89,v57,v89\n"
"xM v[116:119],v[180:183],a[152:155],v[116:119]\nTC1_3_1_%=:\ns_cbranch_execz TC1_3_2_%=\nxE v56,-v56\n"
"xM v[116:119],v[184:187],a[168:171],v[116:119]\nxE v57,-v57\nxM v[116:119],v[188:191],a[184:187],v[116:119]\nxT v[166:167],v211 offset:2048\nv_add_f32 v56, 1.0, v56\n"
"xM v[124:127],v[180:183],a[156:159],v[124:127]\nxA m0,s32,137216\nxT v[168:169],v211 offset:4096\nv_add_f32 v57, 1.0, v57\n"
"xM v[124:127],v[184:187],a[172:175],v[124:127]\nxA s39,s35,1179648\nv_rcp_f32 v56, v56\n"
"xM v[124:127],v[188:191],a[188:191],v[124:127]\nxW lgkmcnt(4)\nxL v218,s[24:27],s39 offen lds\n"
"xM v[128:131],v[192:195],a[144:147],v[128:131]\nxT v[170:171],v211 offset:6144\nxT v[172:173],v211 offset:8192\n"
"xM v[128:131],v[196:199],a[160:163],v[128:131]\nxW lgkmcnt(4)\nv_rcp_f32 v57, v57\n"
"xM v[128:131],v[160:163],a[176:179],v[128:131]\nxU v88,v88,v56\nxU v89,v89,v57\n"
"xM v[136:139],v[192:195],a[148:151],v[136:139]\nxC v88,v88,v89\nxU v90,v58,v90\n"
"xM v[136:139],v[196:199],a[164:167],v[136:139]\nxW vmcnt(15)\ns_barrier\n"
"xM v[136:139],v[160:163],a[180:183],v[136:139]\nxA m0,s32,73728\nxT v[174:175],v211 offset:10240\nxU v91,v59,v91\n"
"xM v[144:147],v[192:195],a[152:155],v[144:147]\nxA s39,s35,1769472\nxT v[176:177],v210 offset:24576\nxT v[178:179],v210 offset:26624\n"
"xM v[144:147],v[196:199],a[168:171],v[144:147]\nxL v218,s[24:27],s39 offen lds\nxM v[144:147],v[160:163],a[184:187],v[144:147]\nxE v58,-v58\n"
"xM v[152:155],v[192:195],a[156:159],v[152:155]\nxE v59,-v59\nxM v[152:155],v[196:199],a[172:175],v[152:155]\nxT v[180:181],v210 offset:28672\nv_add_f32 v58, 1.0, v58\n"
"xM v[152:155],v[160:163],a[188:191],v[152:155]\nxW lgkmcnt(4)\nxT v[182:183],v210 offset:30720\nv_add_f32 v59, 1.0, v59\n"
"xM v[132:135],v[164:167],a[144:147],v[132:135]\nxW lgkmcnt(4)\nv_rcp_f32 v58, v58\n"
"xM v[132:135],v[168:171],a[160:163],v[132:135]\nxA m0,s32,74752\nv_rcp_f32 v59, v59\n"
"xM v[132:135],v[172:175],a[176:179],v[132:135]\nxA s39,s35,1916928\nxT v[184:185],v210 offset:32768\nxU v90,v90,v58\n"
"xM v[140:143],v[164:167],a[148:151],v[140:143]\nxL v218,s[24:27],s39 offen lds\nxM v[140:143],v[168:171],a[164:167],v[140:143]\nxT v[186:187],v210 offset:34816\nxU v91,v91,v59\n"
"xM v[140:143],v[172:175],a[180:183],v[140:143]\nxC v89,v90,v91\nxU v92,v60,v92\n"
"xM v[148:151],v[164:167],a[152:155],v[148:151]\nxT v[188:189],v211 offset:24576\nxU v93,v61,v93\n"
"xM v[148:151],v[168:171],a[168:171],v[148:151]\nxE v60,-v60\nxM v[148:151],v[172:175],a[184:187],v[148:151]\nxT v[190:191],v211 offset:26624\nxT v[192:193],v211 offset:28672\n"
"xM v[156:159],v[164:167],a[156:159],v[156:159]\nxA m0,s32,75776\nxE v61,-v61\n"
"xM v[156:159],v[168:171],a[172:175],v[156:159]\nxA s39,s35,2064384\nv_add_f32 v60, 1.0, v60\nv_add_f32 v61, 1.0, v61\n"
"xM v[156:159],v[172:175],a[188:191],v[156:159]\nxW lgkmcnt(4)\nxL v218,s[24:27],s39 offen lds\n"
"xM v[96:99],v[176:179],a[192:195],v[96:99]\nxT v[194:195],v211 offset:30720\nxT v[196:197],v211 offset:32768\n"
"xM v[96:99],v[180:183],a[208:211],v[96:99]\nxW lgkmcnt(5)\nv_rcp_f32 v60, v60\n"
"xM v[96:99],v[184:187],a[224:227],v[96:99]\nv_rcp_f32 v61, v61\nxM v[104:107],v[176:179],a[196:199],v[104:107]\nxU v92,v92,v60\nxU v93,v93,v61\n"
"xM v[104:107],v[180:183],a[212:215],v[104:107]\nxT v[198:199],v211 offset:34816\nxC v90,v92,v93\n"
"xM v[104:107],v[184:187],a[228:231],v[104:107]\nxU v94,v62,v94\nxU v95,v63,v95\n"
"xM v[112:115],v[176:179],a[200:203],v[112:115]\nxT v[160:161],v212 offset:24576\nxT v[162:163],v212 offset:26624\n"
"xM v[112:115],v[180:183],a[216:219],v[112:115]\nxE v62,-v62\nxM v[112:115],v[184:187],a[232:235],v[112:115]\nxE v63,-v63\n"
"xM v[120:123],v[176:179],a[204:207],v[120:123]\nv_add_f32 v62, 1.0, v62\nv_add_f32 v63, 1.0, v63\n"
"xM v[120:123],v[180:183],a[220:223],v[120:123]\nxT v[164:165],v212 offset:28672\nxT v[166:167],v212 offset:30720\n"
"xM v[120:123],v[184:187],a[236:239],v[120:123]\nxW lgkmcnt(5)\nv_rcp_f32 v62, v62\n"
"xM v[100:103],v[188:191],a[192:195],v[100:103]\nv_rcp_f32 v63, v63\nxM v[100:103],v[192:195],a[208:211],v[100:103]\nxW lgkmcnt(4)\nxU v94,v94,v62\nxU v95,v95,v63\n"
"xM v[100:103],v[196:199],a[224:227],v[100:103]\nxT v[168:169],v212 offset:32768\nxC v91,v94,v95\n"
"xM v[108:111],v[188:191],a[196:199],v[108:111]\nds_write_b128 v226, v[88:91] offset:35840\nxM v[108:111],v[192:195],a[212:215],v[108:111]\nxT v[170:171],v212 offset:34816\nxT v[172:173],v213 offset:24576\n"
"xM v[108:111],v[196:199],a[228:231],v[108:111]\nxS v[88:91],v226,s[96:99],s62 offen offset:3072 sc1\nxM v[116:119],v[188:191],a[200:203],v[116:119]\nxT v[174:175],v213 offset:26624\nxA s62,s62,16384\n"
"xM v[116:119],v[192:195],a[216:219],v[116:119]\nxT v[176:177],v213 offset:28672\nxT v[178:179],v213 offset:30720\n"
"xM v[116:119],v[196:199],a[232:235],v[116:119]\nxT v[180:181],v213 offset:32768\nxT v[182:183],v213 offset:34816\n"
"xM v[124:127],v[188:191],a[204:207],v[124:127]\nxM v[124:127],v[192:195],a[220:223],v[124:127]\nxM v[124:127],v[196:199],a[236:239],v[124:127]\nxW lgkmcnt(7)\n"
"xM v[128:131],v[160:163],a[192:195],v[128:131]\nxM v[128:131],v[164:167],a[208:211],v[128:131]\nxW lgkmcnt(5)\n"
"xM v[128:131],v[168:171],a[224:227],v[128:131]\nxM v[136:139],v[160:163],a[196:199],v[136:139]\nxM v[136:139],v[164:167],a[212:215],v[136:139]\nxW vmcnt(11) lgkmcnt(7)\nv_cmp_ne_u64 vcc, s[68:69], v[236:237]\ns_and_b64 vcc, vcc, s[100:101]\n"
"s_and_b32 s38, vcc_lo, 15\ns_cbranch_scc1 SPINY0_%=\nRETY0_%=:\ns_barrier\n"
"xM v[136:139],v[168:171],a[228:231],v[136:139]\nxT v[184:185],v210 offset:36864\nxT v[186:187],v210 offset:38912\n"
"xM v[144:147],v[160:163],a[200:203],v[144:147]\nxA s38,s45,s63\nxT v[188:189],v210 offset:40960\nxT v[190:191],v210 offset:43008\n"
"xM v[144:147],v[164:167],a[216:219],v[144:147]\nxA s38,s38,0\nxT v[192:193],v210 offset:45056\n"
"xM v[144:147],v[168:171],a[232:235],v[144:147]\nxA m0,s32,86016\ns_mov_b64 exec, 0xffff\nbuffer_load_dword v231, v234, s[96:99], s38 offen sc1\ns_mov_b64 exec, -1\n"
"xM v[152:155],v[160:163],a[204:207],v[152:155]\nxA s39,s35,2654208\nxT v[194:195],v210 offset:47104\n"
"xM v[152:155],v[164:167],a[220:223],v[152:155]\nxL v218,s[24:27],s39 offen lds\nxM v[152:155],v[168:171],a[236:239],v[152:155]\nxW lgkmcnt(6)\nxT v[196:197],v211 offset:36864\n"
"xM v[132:135],v[172:175],a[192:195],v[132:135]\nxT v[198:199],v211 offset:38912\nxM v[132:135],v[176:179],a[208:211],v[132:135]\n"
"xM v[132:135],v[180:183],a[224:227],v[132:135]\nxT v[160:161],v211 offset:40960\nxM v[140:143],v[172:175],a[196:199],v[140:143]\n"
"xM v[140:143],v[176:179],a[212:215],v[140:143]\nxA m0,s32,87040\nxT v[162:163],v211 offset:43008\n"
"xM v[140:143],v[180:183],a[228:231],v[140:143]\nxA s39,s35,2801664\nxM v[148:151],v[172:175],a[200:203],v[148:151]\nxL v218,s[24:27],s39 offen lds\n"
"xM v[148:151],v[176:179],a[216:219],v[148:151]\nxT v[164:165],v211 offset:45056\nxM v[148:151],v[180:183],a[232:235],v[148:151]\nxT v[166:167],v211 offset:47104\n"
"xM v[156:159],v[172:175],a[204:207],v[156:159]\nxM v[156:159],v[176:179],a[220:223],v[156:159]\nxT v[168:169],v210 offset:49152\n"
"xM v[156:159],v[180:183],a[236:239],v[156:159]\nxW lgkmcnt(4)\nxM v[96:99],v[184:187],a[240:243],v[96:99]\nxA m0,s32,88064\nxT v[170:171],v210 offset:51200\n"
"xM v[96:99],v[188:191],v[0:3],v[96:99]\nxA s39,s35,2949120\nxM v[96:99],v[192:195],v[16:19],v[96:99]\nxL v218,s[24:27],s39 offen lds\n"
"xM v[104:107],v[184:187],a[244:247],v[104:107]\nxT v[172:173],v210 offset:53248\nxM v[104:107],v[188:191],v[4:7],v[104:107]\nxT v[174:175],v210 offset:55296\n"
"xM v[104:107],v[192:195],v[20:23],v[104:107]\nxM v[112:115],v[184:187],a[248:251],v[112:115]\nxT v[176:177],v210 offset:57344\n"
"xM v[112:115],v[188:191],v[8:11],v[112:115]\nxM v[112:115],v[192:195],v[24:27],v[112:115]\nxT v[178:179],v210 offset:59392\n"
"xM v[120:123],v[184:187],a[252:255],v[120:123]\nxM v[120:123],v[188:191],v[12:15],v[120:123]\nxT v[180:181],v211 offset:49152\n"
"xM v[120:123],v[192:195],v[28:31],v[120:123]\nxM v[100:103],v[196:199],a[240:243],v[100:103]\nxW lgkmcnt(3)\nxT v[182:183],v211 offset:51200\n"
"xM v[100:103],v[160:163],v[0:3],v[100:103]\nxM v[100:103],v[164:167],v[16:19],v[100:103]\nxT v[184:185],v211 offset:53248\n"
"xM v[108:111],v[196:199],a[244:247],v[108:111]\nxM v[108:111],v[160:163],v[4:7],v[108:111]\nxT v[186:187],v211 offset:55296\n"
"xM v[108:111],v[164:167],v[20:23],v[108:111]\nxM v[116:119],v[196:199],a[248:251],v[116:119]\nxA s38,s45,s63\nxT v[188:189],v211 offset:57344\n"
"xM v[116:119],v[160:163],v[8:11],v[116:119]\nxA s38,s38,8192\nxM v[116:119],v[164:167],v[24:27],v[116:119]\ns_mov_b64 exec, 0xffff\nbuffer_load_dword v231, v234, s[96:99], s38 offen sc1\ns_mov_b64 exec, -1\n"
"xM v[124:127],v[196:199],a[252:255],v[124:127]\nxT v[190:191],v211 offset:59392\nxM v[124:127],v[160:163],v[12:15],v[124:127]\n"
"xM v[124:127],v[164:167],v[28:31],v[124:127]\nxM v[128:131],v[168:171],a[240:243],v[128:131]\nxM v[128:131],v[172:175],v[0:3],v[128:131]\nxW lgkmcnt(2)\n"
"xM v[128:131],v[176:179],v[16:19],v[128:131]\nxM v[136:139],v[168:171],a[244:247],v[136:139]\nxM v[136:139],v[172:175],v[4:7],v[136:139]\nxW vmcnt(9)\ns_barrier\n"
"xM v[136:139],v[176:179],v[20:23],v[136:139]\nxA m0,s32,98304\nxT v[192:193],v216 offset:0\nxT v[194:195],v216 offset:1024\n"
"xM v[144:147],v[168:171],a[248:251],v[144:147]\nxA s39,s35,3538944\nxT v[196:197],v216 offset:2048\nxT v[198:199],v216 offset:3072\n"
"xM v[144:147],v[172:175],v[8:11],v[144:147]\nxL v218,s[24:27],s39 offen lds\nxM v[144:147],v[176:179],v[24:27],v[144:147]\nxT v[160:161],v216 offset:4096\nxT v[162:163],v216 offset:5120\n"
"xM v[152:155],v[168:171],a[252:255],v[152:155]\nxM v[152:155],v[172:175],v[12:15],v[152:155]\nxT v[164:165],v216 offset:6144\n"
"xM v[152:155],v[176:179],v[28:31],v[152:155]\nxM v[132:135],v[180:183],a[240:243],v[132:135]\nxW lgkmcnt(7)\nxT v[166:167],v216 offset:7168\n"
"xM v[132:135],v[184:187],v[0:3],v[132:135]\nxA m0,s32,99328\nxM v[132:135],v[188:191],v[16:19],v[132:135]\nxA s39,s35,3686400\nxT v[168:169],v216 offset:8192\n"
"xM v[140:143],v[180:183],a[244:247],v[140:143]\nxL v218,s[24:27],s39 offen lds\nxM v[140:143],v[184:187],v[4:7],v[140:143]\nxT v[170:171],v216 offset:9216\n"
"xM v[140:143],v[188:191],v[20:23],v[140:143]\nxM v[148:151],v[180:183],a[248:251],v[148:151]\nxT v[172:173],v216 offset:10240\n"
"xM v[148:151],v[184:187],v[8:11],v[148:151]\nxM v[148:151],v[188:191],v[24:27],v[148:151]\nxT v[174:175],v216 offset:11264\n"
"xM v[156:159],v[180:183],a[252:255],v[156:159]\nxA m0,s32,100352\nxM v[156:159],v[184:187],v[12:15],v[156:159]\nxA s39,s35,3833856\nxT v[176:177],v216 offset:12288\n"
"xM v[156:159],v[188:191],v[28:31],v[156:159]\nxW lgkmcnt(4)\nxL v218,s[24:27],s39 offen lds\n"
"xM v[32:35],v[192:195],a[0:3],0\nxT v[178:179],v216 offset:13312\nxM v[32:35],v[196:199],a[16:19],v[32:35]\n"
"xM v[32:35],v[160:163],a[32:35],v[32:35]\nxT v[180:181],v216 offset:14336\nxM v[36:39],v[192:195],a[4:7],0\nxU v128,v96,v128\nxU v129,v97,v129\n"
"xM v[36:39],v[196:199],a[20:23],v[36:39]\nxE v96,-v96\nxM v[36:39],v[160:163],a[36:39],v[36:39]\nxE v97,-v97\n"
"xM v[40:43],v[192:195],a[8:11],0\nxA s38,s45,s63\nv_add_f32 v96, 1.0, v96\nv_add_f32 v97, 1.0, v97\n"
"xM v[40:43],v[196:199],a[24:27],v[40:43]\nxA s38,s38,16384\nv_rcp_f32 v96, v96\n"
"xM v[40:43],v[160:163],a[40:43],v[40:43]\ns_mov_b64 exec, 0xffff\nbuffer_load_dword v231, v234, s[96:99], s38 offen sc1\ns_mov_b64 exec, -1\n"
"xM v[44:47],v[192:195],a[12:15],0\nxW vmcnt(9)\ns_mov_b64 exec, 1\nbuffer_store_dwordx2 v[242:243], off, s[96:99], s53 sc1\ns_mov_b64 exec, -1\n"
"xM v[44:47],v[196:199],a[28:31],v[44:47]\nxA s53,s53,32\nv_rcp_f32 v97, v97\n"
"xM v[44:47],v[160:163],a[44:47],v[44:47]\nxA m0,s32,147456\nxU v128,v128,v96\nxU v129,v129,v97\n"
"xM v[48:51],v[164:167],a[0:3],0\nxA s39,s35,4423680\nxT v[182:183],v216 offset:15360\nxC v128,v128,v129\nxW lgkmcnt(6)\n"
"xM v[48:51],v[168:171],a[16:19],v[48:51]\nxW lgkmcnt(1)\nxL v218,s[24:27],s39 offen lds\n"
"xM v[48:51],v[172:175],a[32:35],v[48:51]\nxT v[184:185],v216 offset:16384\nxU v130,v98,v130\n"
"xM v[52:55],v[164:167],a[4:7],0\nxT v[186:187],v216 offset:17408\nxU v131,v99,v131\n"
"xM v[52:55],v[168:171],a[20:23],v[52:55]\nxE v98,-v98\nxM v[52:55],v[172:175],a[36:39],v[52:55]\nxE v99,-v99\n"
"xM v[56:59],v[164:167],a[8:11],0\nxT v[188:189],v216 offset:18432\nv_add_f32 v98, 1.0, v98\n"
"xM v[56:59],v[168:171],a[24:27],v[56:59]\nxA m0,s32,148480\nxT v[190:191],v216 offset:19456\nv_add_f32 v99, 1.0, v99\n"
"xM v[56:59],v[172:175],a[40:43],v[56:59]\nxA s39,s35,4571136\nv_rcp_f32 v98, v98\n"
"xM v[60:63],v[164:167],a[12:15],0\nxL v218,s[24:27],s39 offen lds\nxM v[60:63],v[168:171],a[28:31],v[60:63]\nxT v[192:193],v216 offset:20480\nxT v[194:195],v216 offset:21504\n"
"xM v[60:63],v[172:175],a[44:47],v[60:63]\nv_rcp_f32 v99, v99\nxM v[32:35],v[176:179],a[48:51],v[32:35]\nxW lgkmcnt(4)\nxU v130,v130,v98\nxU v131,v131,v99\n"
"xM v[32:35],v[180:183],a[64:67],v[32:35]\nxC v129,v130,v131\nxU v132,v100,v132\n"
"xM v[32:35],v[184:187],a[80:83],v[32:35]\nxT v[196:197],v216 offset:22528\nxU v133,v101,v133\n"
"xM v[36:39],v[176:179],a[52:55],v[36:39]\nxE v100,-v100\nxM v[36:39],v[180:183],a[68:71],v[36:39]\nxW vmcnt(8)\ns_barrier\n"
"xM v[36:39],v[184:187],a[84:87],v[36:39]\nxA m0,s32,149504\nxT v[198:199],v216 offset:23552\nxT v[160:161],v215 offset:12288\n"
"xM v[40:43],v[176:179],a[56:59],v[40:43]\nxA s39,s35,4718592\nxE v101,-v101\n"
"xM v[40:43],v[180:183],a[72:75],v[40:43]\nxL v218,s[24:27],s39 offen lds\nxM v[40:43],v[184:187],a[88:91],v[40:43]\nxT v[162:163],v215 offset:13312\nv_add_f32 v100, 1.0, v100\n"
"xM v[44:47],v[176:179],a[60:63],v[44:47]\nxT v[164:165],v215 offset:14336\nv_add_f32 v101, 1.0, v101\n"
"xM v[44:47],v[180:183],a[76:79],v[44:47]\nv_rcp_f32 v100, v100\nxM v[44:47],v[184:187],a[92:95],v[44:47]\nxW lgkmcnt(4)\nv_rcp_f32 v101, v101\n"
"xM v[48:51],v[188:191],a[48:51],v[48:51]\nxW lgkmcnt(3)\nxT v[166:167],v215 offset:15360\nxU v132,v132,v100\n"
"xM v[48:51],v[192:195],a[64:67],v[48:51]\nxA m0,s32,110592\nxU v133,v133,v101\nxC v130,v132,v133\n"
"xM v[48:51],v[196:199],a[80:83],v[48:51]\nxA s39,s34,0\nxT v[168:169],v215 offset:16384\nxU v134,v102,v134\n"
"xM v[52:55],v[188:191],a[52:55],v[52:55]\nxL v219,s[56:59],s39 offen lds\nxM v[52:55],v[192:195],a[68:71],v[52:55]\nxT v[170:171],v215 offset:17408\nxU v135,v103,v135\n"
"xM v[52:55],v[196:199],a[84:87],v[52:55]\nxE v102,-v102\nxM v[56:59],v[188:191],a[56:59],v[56:59]\nxA s38,s45,s63\nxT v[172:173],v215 offset:18432\nxT v[174:175],v215 offset:19456\n"
"xM v[56:59],v[192:195],a[72:75],v[56:59]\nxA s38,s38,24576\nxE v103,-v103\n"
"xM v[56:59],v[196:199],a[88:91],v[56:59]\nxA m0,s32,111616\ns_mov_b64 exec, 0xffff\nbuffer_load_dword v231, v234, s[96:99], s38 offen sc1\ns_mov_b64 exec, -1\n"
"xM v[60:63],v[188:191],a[60:63],v[60:63]\nxA s39,s34,9216\nv_add_f32 v102, 1.0, v102\nv_add_f32 v103, 1.0, v103\n"
"xM v[60:63],v[192:195],a[76:79],v[60:63]\nxL v219,s[56:59],s39 offen lds\nxM v[60:63],v[196:199],a[92:95],v[60:63]\nxW lgkmcnt(3)\nxT v[176:177],v215 offset:20480\nxT v[178:179],v215 offset:21504\n"
"xM v[32:35],v[160:163],a[96:99],v[32:35]\nv_rcp_f32 v102, v102\nxM v[32:35],v[164:167],a[112:115],v[32:35]\nxW lgkmcnt(4)\nv_rcp_f32 v103, v103\n"
"xM v[32:35],v[168:171],a[128:131],v[32:35]\nxA m0,s32,112640\nxT v[180:181],v215 offset:22528\nxU v134,v134,v102\n"
"xM v[36:39],v[160:163],a[100:103],v[36:39]\nxA s39,s34,18432\nxU v135,v135,v103\nxC v131,v134,v135\n"
"xM v[36:39],v[164:167],a[116:119],v[36:39]\nxL v219,s[56:59],s39 offen lds\nxM v[36:39],v[168:171],a[132:135],v[36:39]\nxT v[182:183],v215 offset:23552\nxT v[184:185],v215 offset:24576\n"
"xM v[40:43],v[160:163],a[104:107],v[40:43]\nds_write_b128 v226, v[128:131] offset:49152\nxM v[40:43],v[164:167],a[120:123],v[40:43]\nxR a[0:3],v228 offset:0\nxT v[186:187],v215 offset:25600\n"
"xM v[40:43],v[168:171],a[136:139],v[40:43]\nxR a[16:19],v228 offset:8192\nxT v[188:189],v215 offset:26624\n"
"xM v[44:47],v[160:163],a[108:111],v[44:47]\nxA m0,s32,122880\nxR a[32:35],v228 offset:16384\nxT v[190:191],v215 offset:27648\n"
"xM v[44:47],v[164:167],a[124:127],v[44:47]\nxA s39,s34,110592\nbuffer_load_dwordx2 v[236:237], v238, s[96:99], s37 offen sc1\n"
"xM v[44:47],v[168:171],a[140:143],v[44:47]\nxW lgkmcnt(9)\nxL v219,s[56:59],s39 offen lds\n"
"xM v[48:51],v[172:175],a[96:99],v[48:51]\nxR a[4:7],v228 offset:1024\nxT v[192:193],v215 offset:28672\n"
"xM v[48:51],v[176:179],a[112:115],v[48:51]\nxW lgkmcnt(9)\nxR a[20:23],v228 offset:9216\nxT v[194:195],v215 offset:29696\n"
"xM v[48:51],v[180:183],a[128:131],v[48:51]\nxR a[36:39],v228 offset:17408\nxT v[196:197],v215 offset:30720\n"
"xM v[52:55],v[172:175],a[100:103],v[52:55]\nxA m0,s32,123904\nxR a[8:11],v228 offset:2048\nxT v[198:199],v215 offset:31744\n"
"xM v[52:55],v[176:179],a[116:119],v[52:55]\nxA s39,s34,119808\nxR a[24:27],v228 offset:10240\nxT v[160:161],v215 offset:32768\n"
"xM v[52:55],v[180:183],a[132:135],v[52:55]\nxL v219,s[56:59],s39 offen lds\nxM v[56:59],v[172:175],a[104:107],v[56:59]\nxR a[40:43],v228 offset:18432\nxT v[162:163],v215 offset:33792\n"
"xM v[56:59],v[176:179],a[120:123],v[56:59]\nxR a[12:15],v228 offset:3072\nxT v[164:165],v215 offset:34816\n"
"xM v[56:59],v[180:183],a[136:139],v[56:59]\nxR a[28:31],v228 offset:11264\nxT v[166:167],v215 offset:35840\n"
"xM v[60:63],v[172:175],a[108:111],v[60:63]\nxA m0,s32,124928\nxR a[44:47],v228 offset:19456\n"
"xM v[60:63],v[176:179],a[124:127],v[60:63]\nxA s39,s34,129024\nxR a[48:51],v228 offset:24576\n"
"xM v[60:63],v[180:183],a[140:143],v[60:63]\nxW lgkmcnt(10)\nxL v219,s[56:59],s39 offen lds\n"
"xM v[32:35],v[184:187],a[144:147],v[32:35]\nxR a[64:67],v228 offset:32768\nxM v[32:35],v[188:191],a[160:163],v[32:35]\nxR a[80:83],v228 offset:40960\n"
"xM v[32:35],v[192:195],a[176:179],v[32:35]\nxR a[52:55],v228 offset:25600\nxM v[36:39],v[184:187],a[148:151],v[36:39]\nxR a[68:71],v228 offset:33792\n"
"xM v[36:39],v[188:191],a[164:167],v[36:39]\nxW vmcnt(8)\ns_barrier\n"
"xM v[36:39],v[192:195],a[180:183],v[36:39]\nxA m0,s32,73728\nxR a[84:87],v228 offset:41984\nxT v[168:169],v215 offset:36864\n"
"xM v[40:43],v[184:187],a[152:155],v[40:43]\nxA s39,s34,128\nxR a[56:59],v228 offset:26624\nxT v[170:171],v215 offset:37888\n"
"xM v[40:43],v[188:191],a[168:171],v[40:43]\nxL v219,s[56:59],s39 offen lds\nxM v[40:43],v[192:195],a[184:187],v[40:43]\nxR a[72:75],v228 offset:34816\nxT v[172:173],v215 offset:38912\n"
"xM v[44:47],v[184:187],a[156:159],v[44:47]\nxR a[88:91],v228 offset:43008\nxT v[174:175],v215 offset:39936\n"
"xM v[44:47],v[188:191],a[172:175],v[44:47]\nxR a[60:63],v228 offset:27648\nxT v[176:177],v215 offset:40960\n"
"xM v[44:47],v[192:195],a[188:191],v[44:47]\nxA m0,s32,74752\nxR a[76:79],v228 offset:35840\nxT v[178:179],v215 offset:41984\n"
"xM v[48:51],v[196:199],a[144:147],v[48:51]\nxA s39,s34,9344\nxR a[92:95],v228 offset:44032\nxT v[180:181],v215 offset:43008\nxW lgkmcnt(15)\n"
"xM v[48:51],v[160:163],a[160:163],v[48:51]\nxL v219,s[56:59],s39 offen lds\nxM v[48:51],v[164:167],a[176:179],v[48:51]\nxS v[128:131],v226,s[96:99],s62 offen offset:0 sc1\n"
"xM v[52:55],v[196:199],a[148:151],v[52:55]\nxU v136,v104,v136\nxU v137,v105,v137\n"
"xM v[52:55],v[160:163],a[164:167],v[52:55]\nxE v104,-v104\nTC1_1_2_%=:\ns_cbranch_execz TC1_1_3_%=\n"
"xM v[52:55],v[164:167],a[180:183],v[52:55]\nxA m0,s32,75776\nxE v105,-v105\n"
"xM v[56:59],v[196:199],a[152:155],v[56:59]\nxA s39,s34,18560\nv_add_f32 v104, 1.0, v104\nv_add_f32 v105, 1.0, v105\n"
"xM v[56:59],v[160:163],a[168:171],v[56:59]\nxL v219,s[56:59],s39 offen lds\nxM v[56:59],v[164:167],a[184:187],v[56:59]\nxT v[182:183],v215 offset:44032\nxT v[184:185],v215 offset:45056\n"
"xM v[60:63],v[196:199],a[156:159],v[60:63]\nv_rcp_f32 v104, v104\nxM v[60:63],v[160:163],a[172:175],v[60:63]\nxW lgkmcnt(2)\nv_rcp_f32 v105, v105\n"
"xM v[60:63],v[164:167],a[188:191],v[60:63]\nxA m0,s32,135168\nxU v136,v136,v104\nxU v137,v137,v105\n"
"xM v[32:35],v[168:171],a[192:195],v[32:35]\nxA s39,s34,110720\nxT v[186:187],v215 offset:46080\nxC v136,v136,v137\n"
"xM v[32:35],v[172:175],a[208:211],v[32:35]\nxL v219,s[56:59],s39 offen lds\nxM v[32:35],v[176:179],a[224:227],v[32:35]\nxT v[188:189],v215 offset:47104\nxU v138,v106,v138\n"
"xM v[36:39],v[168:171],a[196:199],v[36:39]\nxT v[190:191],v215 offset:48128\nxU v139,v107,v139\n"
"xM v[36:39],v[172:175],a[212:215],v[36:39]\nxE v106,-v106\nxM v[36:39],v[176:179],a[228:231],v[36:39]\nxA m0,s32,136192\nxE v107,-v107\n"
"xM v[40:43],v[168:171],a[200:203],v[40:43]\nxA s39,s34,119936\nxT v[192:193],v216 offset:24576\nv_add_f32 v106, 1.0, v106\n"
"xM v[40:43],v[172:175],a[216:219],v[40:43]\nxL v219,s[56:59],s39 offen lds\nxM v[40:43],v[176:179],a[232:235],v[40:43]\nxT v[194:195],v216 offset:25600\nv_add_f32 v107, 1.0, v107\nv_rcp_f32 v106, v106\n"
"xM v[44:47],v[168:171],a[204:207],v[44:47]\nv_rcp_f32 v107, v107\nxU v138,v138,v106\n"
"xM v[44:47],v[172:175],a[220:223],v[44:47]\nxW lgkmcnt(3)\nxT v[196:197],v216 offset:26624\nxU v139,v139,v107\nxC v137,v138,v139\n"
"xM v[44:47],v[176:179],a[236:239],v[44:47]\nxA m0,s32,137216\nxU v140,v108,v140\nxU v141,v109,v141\nxE v108,-v108\n"
"xM v[48:51],v[180:183],a[192:195],v[48:51]\nxA s39,s34,129152\nxT v[198:199],v216 offset:27648\nxT v[160:161],v216 offset:28672\nxE v109,-v109\n"
"xM v[48:51],v[184:187],a[208:211],v[48:51]\nxW lgkmcnt(5)\nxL v219,s[56:59],s39 offen lds\nv_add_f32 v108, 1.0, v108\nv_add_f32 v109, 1.0, v109\nv_rcp_f32 v108, v108\n"
"xM v[48:51],v[188:191],a[224:227],v[48:51]\nv_rcp_f32 v109, v109\nxM v[52:55],v[180:183],a[196:199],v[52:55]\nxU v140,v140,v108\nxU v141,v141,v109\n"
"xM v[52:55],v[184:187],a[212:215],v[52:55]\nxT v[162:163],v216 offset:29696\nxC v138,v140,v141\nxU v142,v110,v142\nxU v143,v111,v143\n"
"xM v[52:55],v[188:191],a[228:231],v[52:55]\nxE v110,-v110\nxE v111,-v111\n"
"xM v[56:59],v[180:183],a[200:203],v[56:59]\nxT v[164:165],v216 offset:30720\nv_add_f32 v110, 1.0, v110\n"
"xM v[56:59],v[184:187],a[216:219],v[56:59]\nxT v[166:167],v216 offset:31744\nv_add_f32 v111, 1.0, v111\nv_rcp_f32 v110, v110\n"
"xM v[56:59],v[188:191],a[232:235],v[56:59]\nv_rcp_f32 v111, v111\nxU v142,v142,v110\n"
"xM v[60:63],v[180:183],a[204:207],v[60:63]\nxU v143,v143,v111\nxC v139,v142,v143\n"
"xM v[60:63],v[184:187],a[220:223],v[60:63]\nxT v[168:169],v216 offset:32768\nxT v[170:171],v216 offset:33792\nds_write_b128 v226, v[136:139] offset:50176\nxS v[136:139],v226,s[96:99],s62 offen offset:1024 sc1\n"
"xM v[60:63],v[188:191],a[236:239],v[60:63]\nxW lgkmcnt(6)\nxU v144,v112,v144\nxU v145,v113,v145\n"
"xM v[32:35],v[192:195],a[240:243],v[32:35]\nxE v112,-v112\nxE v113,-v113\n"
"xM v[32:35],v[196:199],v[0:3],v[32:35]\nxW lgkmcnt(5)\nv_add_f32 v112, 1.0, v112\nv_add_f32 v113, 1.0, v113\n"
"xM v[32:35],v[160:163],v[16:19],v[32:35]\nxT v[172:173],v216 offset:34816\nxT v[174:175],v216 offset:35840\nv_rcp_f32 v112, v112\n"
"xM v[36:39],v[192:195],a[244:247],v[36:39]\nv_rcp_f32 v113, v113\nxU v144,v144,v112\n"
"xM v[36:39],v[196:199],v[4:7],v[36:39]\nxW vmcnt(8) lgkmcnt(2)\nv_cmp_ne_u64 vcc, s[68:69], v[236:237]\ns_and_b64 vcc, vcc, s[100:101]\ns_and_b32 s38, vcc_lo, 240\ns_cbranch_scc1 SPINY1_%=\n"
"RETY1_%=:\ns_barrier\nxM v[36:39],v[160:163],v[20:23],v[36:39]\nxA m0,s32,86016\nxU v145,v145,v113\nxC v144,v144,v145\n"
"xU v146,v114,v146\nxU v147,v115,v147\nxE v114,-v114\n"
"xM v[40:43],v[192:195],a[248:251],v[40:43]\nxA s39,s40,0\nxT v[176:177],v210 offset:49152\nxT v[178:179],v210 offset:51200\nxE v115,-v115\n"
"xM v[40:43],v[196:199],v[8:11],v[40:43]\nxL v219,s[56:59],s39 offen lds\nv_add_f32 v114, 1.0, v114\nv_add_f32 v115, 1.0, v115\nv_rcp_f32 v114, v114\n"
"xM v[40:43],v[160:163],v[24:27],v[40:43]\nv_rcp_f32 v115, v115\nxM v[44:47],v[192:195],a[252:255],v[44:47]\nxA m0,s45,0\nxU v146,v146,v114\nxU v147,v147,v115\n"
"xC v145,v146,v147\nxM v[44:47],v[196:199],v[12:15],v[44:47]\nxA s39,s63,0\nxT v[180:181],v210 offset:53248\nxU v148,v116,v148\nxU v149,v117,v149\n"
"xM v[44:47],v[160:163],v[28:31],v[44:47]\nxL v224,s[96:99],s39 offen sc1 lds\nxE v116,-v116\nxE v117,-v117\n"
"xM v[48:51],v[164:167],a[240:243],v[48:51]\nxW lgkmcnt(3)\nxT v[182:183],v210 offset:55296\nv_add_f32 v116, 1.0, v116\nv_add_f32 v117, 1.0, v117\n"
"xM v[48:51],v[168:171],v[0:3],v[48:51]\nxA m0,s45,4096\nv_rcp_f32 v116, v116\nv_rcp_f32 v117, v117\n"
"xM v[48:51],v[172:175],v[16:19],v[48:51]\nxA s39,s63,4096\nxT v[184:185],v210 offset:57344\nxU v148,v148,v116\n"
"xM v[52:55],v[164:167],a[244:247],v[52:55]\nxL v224,s[96:99],s39 offen sc1 lds\nxU v149,v149,v117\nxC v146,v148,v149\nxU v150,v118,v150\n"
"xM v[52:55],v[168:171],v[4:7],v[52:55]\nxT v[186:187],v210 offset:59392\nxU v151,v119,v151\nxE v118,-v118\n"
"xM v[52:55],v[172:175],v[20:23],v[52:55]\nxA m0,s32,87040\nxE v119,-v119\nv_add_f32 v118, 1.0, v118\n"
"xM v[56:59],v[164:167],a[248:251],v[56:59]\nxA s39,s41,0\nxT v[188:189],v211 offset:49152\nv_add_f32 v119, 1.0, v119\nv_rcp_f32 v118, v118\n"
"xM v[56:59],v[168:171],v[8:11],v[56:59]\nxL v219,s[56:59],s39 offen lds\nv_rcp_f32 v119, v119\n"
"xM v[56:59],v[172:175],v[24:27],v[56:59]\nxT v[190:191],v211 offset:51200\nxU v150,v150,v118\nxU v151,v151,v119\nxC v147,v150,v151\n"
"xM v[60:63],v[164:167],a[252:255],v[60:63]\nxA m0,s45,8192\nds_write_b128 v226, v[144:147] offset:51200\nxS v[144:147],v226,s[96:99],s62 offen offset:2048 sc1\n"
"xM v[60:63],v[168:171],v[12:15],v[60:63]\nxA s39,s63,8192\nxT v[192:193],v211 offset:53248\nxU v152,v120,v152\n"
"xM v[60:63],v[172:175],v[28:31],v[60:63]\nxW lgkmcnt(5)\nxL v224,s[96:99],s39 offen sc1 lds\nxU v153,v121,v153\nxE v120,-v120\n"
"xM v[64:67],v[176:179],a[0:3],0\nxW lgkmcnt(4)\nxT v[194:195],v211 offset:55296\nxT v[196:197],v211 offset:57344\nxE v121,-v121\nv_add_f32 v120, 1.0, v120\n"
"xM v[64:67],v[180:183],a[16:19],v[64:67]\nxA m0,s45,12288\nxT v[198:199],v211 offset:59392\nv_add_f32 v121, 1.0, v121\nv_rcp_f32 v120, v120\n"
"xM v[64:67],v[184:187],a[32:35],v[64:67]\nxA s39,s63,12288\nv_rcp_f32 v121, v121\n"
"xM v[72:75],v[176:179],a[4:7],0\nxL v224,s[96:99],s39 offen sc1 lds\nxU v152,v152,v120\nxU v153,v153,v121\nxC v152,v152,v153\n"
"xM v[72:75],v[180:183],a[20:23],v[72:75]\nxU v154,v122,v154\nxU v155,v123,v155\nxE v122,-v122\n"
"xM v[72:75],v[184:187],a[36:39],v[72:75]\nxA m0,s32,88064\nxE v123,-v123\n"
"xM v[80:83],v[176:179],a[8:11],0\nxA s39,s42,0\nxT v[160:161],v212 offset:0\nv_add_f32 v122, 1.0, v122\nv_add_f32 v123, 1.0, v123\n"
"xM v[80:83],v[180:183],a[24:27],v[80:83]\nxL v219,s[56:59],s39 offen lds\nv_rcp_f32 v122, v122\nv_rcp_f32 v123, v123\n"
"xM v[80:83],v[184:187],a[40:43],v[80:83]\nxT v[162:163],v212 offset:2048\nxU v154,v154,v122\nxU v155,v155,v123\n"
"xM v[88:91],v[176:179],a[12:15],0\nxA m0,s45,16384\nxC v153,v154,v155\nxU v156,v124,v156\nxU v157,v125,v157\n"
"xM v[88:91],v[180:183],a[28:31],v[88:91]\nxA s39,s63,16384\nxT v[164:165],v212 offset:4096\nxT v[166:167],v212 offset:6144\nxE v124,-v124\n"
"xM v[88:91],v[184:187],a[44:47],v[88:91]\nxW lgkmcnt(4)\nxL v224,s[96:99],s39 offen sc1 lds\nxE v125,-v125\nv_add_f32 v124, 1.0, v124\n"
"xM v[68:71],v[188:191],a[0:3],0\nxT v[168:169],v212 offset:8192\nv_add_f32 v125, 1.0, v125\nv_rcp_f32 v124, v124\n"
"xM v[68:71],v[192:195],a[16:19],v[68:71]\nxA m0,s45,20480\nv_rcp_f32 v125, v125\nxU v156,v156,v124\n"
"xM v[68:71],v[196:199],a[32:35],v[68:71]\nxA s39,s63,20480\nxU v157,v157,v125\nxC v154,v156,v157\n"
"xM v[76:79],v[188:191],a[4:7],0\nxL v224,s[96:99],s39 offen sc1 lds\nxU v158,v126,v158\nxU v159,v127,v159\nxE v126,-v126\n"
"xM v[76:79],v[192:195],a[20:23],v[76:79]\nxT v[170:171],v212 offset:10240\nxT v[172:173],v213 offset:0\nxE v127,-v127\n"
"xM v[76:79],v[196:199],a[36:39],v[76:79]\nxA m0,s32,98304\nv_add_f32 v126, 1.0, v126\nv_add_f32 v127, 1.0, v127\nv_rcp_f32 v126, v126\n"
"xM v[84:87],v[188:191],a[8:11],0\nxA s39,s40,128\nv_rcp_f32 v127, v127\n"
"xM v[84:87],v[192:195],a[24:27],v[84:87]\nxL v219,s[56:59],s39 offen lds\nxU v158,v158,v126\nxU v159,v159,v127\n"
"xM v[84:87],v[196:199],a[40:43],v[84:87]\nxT v[174:175],v213 offset:2048\nxC v155,v158,v159\n"
"xM v[92:95],v[188:191],a[12:15],0\nxA m0,s45,24576\nds_write_b128 v226, v[152:155] offset:52224\nxS v[152:155],v226,s[96:99],s62 offen offset:3072 sc1\ns_sub_u32 s62, s62, 49152\n"
"xM v[92:95],v[192:195],a[28:31],v[92:95]\nxA s39,s63,24576\nxT v[176:177],v213 offset:4096\nxU v48,v32,v48\nxU v49,v33,v49\n"
"xM v[92:95],v[196:199],a[44:47],v[92:95]\nxW lgkmcnt(5)\nxL v224,s[96:99],s39 offen sc1 lds\nxE v32,-v32\nxE v33,-v33\n"
"xM v[64:67],v[160:163],a[48:51],v[64:67]\nxW lgkmcnt(3)\nxT v[178:179],v213 offset:6144\nv_add_f32 v32, 1.0, v32\nv_add_f32 v33, 1.0, v33\n"
"xM v[64:67],v[164:167],a[64:67],v[64:67]\nxA m0,s45,28672\nv_rcp_f32 v32, v32\nv_rcp_f32 v33, v33\n"
"xM v[64:67],v[168:171],a[80:83],v[64:67]\nxA s39,s63,28672\nxT v[180:181],v213 offset:8192\nxU v48,v48,v32\n"
"xM v[72:75],v[160:163],a[52:55],v[72:75]\nxL v224,s[96:99],s39 offen sc1 lds\nxU v49,v49,v33\nxC v48,v48,v49\nxU v50,v34,v50\n"
"xM v[72:75],v[164:167],a[68:71],v[72:75]\nxW vmcnt(15) lgkmcnt(3)\ns_barrier\n"
"xM v[72:75],v[168:171],a[84:87],v[72:75]\nxA m0,s32,99328\nxT v[182:183],v213 offset:10240\nxU v51,v35,v51\nxE v34,-v34\nxE v35,-v35\n"
"v_add_f32 v34, 1.0, v34\nxM v[80:83],v[160:163],a[56:59],v[80:83]\nxA s39,s41,128\nxT v[184:185],v210 offset:12288\nv_add_f32 v35, 1.0, v35\nv_rcp_f32 v34, v34\n"
"xM v[80:83],v[164:167],a[72:75],v[80:83]\nxL v219,s[56:59],s39 offen lds\nv_rcp_f32 v35, v35\n"
"xM v[80:83],v[168:171],a[88:91],v[80:83]\nxT v[186:187],v210 offset:14336\nxU v50,v50,v34\nxU v51,v51,v35\nxC v49,v50,v51\n"
"xM v[88:91],v[160:163],a[60:63],v[88:91]\nxA m0,s45,32768\nds_write_b64 v227, v[48:49] offset:0\nbuffer_store_dwordx2 v[48:49], v227, s[96:99], s62 offen offset:0 sc1\n"
"xM v[88:91],v[164:167],a[76:79],v[88:91]\nxA s39,s63,32768\nxT v[188:189],v210 offset:16384\nxU v52,v36,v52\n"
"xM v[88:91],v[168:171],a[92:95],v[88:91]\nxL v224,s[96:99],s39 offen sc1 lds\nxU v53,v37,v53\nxE v36,-v36\n"
"xM v[68:71],v[172:175],a[48:51],v[68:71]\nxW lgkmcnt(4)\nxT v[190:191],v210 offset:18432\nxT v[192:193],v210 offset:20480\nxE v37,-v37\nv_add_f32 v36, 1.0, v36\n"
"xM v[68:71],v[176:179],a[64:67],v[68:71]\nxA m0,s45,36864\nxT v[194:195],v210 offset:22528\nv_add_f32 v37, 1.0, v37\nv_rcp_f32 v36, v36\n"
"xM v[68:71],v[180:183],a[80:83],v[68:71]\nxA s39,s63,36864\nv_rcp_f32 v37, v37\n"
"xM v[76:79],v[172:175],a[52:55],v[76:79]\nxL v224,s[96:99],s39 offen sc1 lds\nxU v52,v52,v36\nxU v53,v53,v37\nxC v52,v52,v53\n"
"xM v[76:79],v[176:179],a[68:71],v[76:79]\nxU v54,v38,v54\nxU v55,v39,v55\nxE v38,-v38\n"
"xM v[76:79],v[180:183],a[84:87],v[76:79]\nxA m0,s32,100352\nxE v39,-v39\n"
"xM v[84:87],v[172:175],a[56:59],v[84:87]\nxA s39,s42,128\nxT v[196:197],v211 offset:12288\nv_add_f32 v38, 1.0, v38\nv_add_f32 v39, 1.0, v39\n"
"xM v[84:87],v[176:179],a[72:75],v[84:87]\nxL v219,s[56:59],s39 offen lds\nv_rcp_f32 v38, v38\nv_rcp_f32 v39, v39\n"
"xM v[84:87],v[180:183],a[88:91],v[84:87]\nxT v[198:199],v211 offset:14336\nxU v54,v54,v38\nxU v55,v55,v39\n"
"xM v[92:95],v[172:175],a[60:63],v[92:95]\nxA m0,s45,40960\nxR a[96:99],v228 offset:49152\nxC v53,v54,v55\n"
"xM v[92:95],v[176:179],a[76:79],v[92:95]\nxA s39,s63,40960\nxT v[160:161],v211 offset:16384\nxT v[162:163],v211 offset:18432\nds_write_b64 v227, v[52:53] offset:1024\nbuffer_store_dwordx2 v[52:53], v227, s[96:99], s62 offen offset:1024 sc1\n"
"xM v[92:95],v[180:183],a[92:95],v[92:95]\nxW lgkmcnt(6)\nxL v224,s[96:99],s39 offen sc1 lds\nxU v56,v40,v56\nxU v57,v41,v57\nxE v40,-v40\n"
"xM v[96:99],v[184:187],a[0:3],0\nxE v41,-v41\nxM v[96:99],v[188:191],a[16:19],v[96:99]\nxA m0,s45,45056\nv_add_f32 v40, 1.0, v40\nv_add_f32 v41, 1.0, v41\n"
"v_rcp_f32 v40, v40\nxM v[96:99],v[192:195],a[32:35],v[96:99]\nxA s39,s63,45056\nxT v[164:165],v211 offset:20480\nxT v[166:167],v211 offset:22528\nv_rcp_f32 v41, v41\n"
"xM v[104:107],v[184:187],a[4:7],0\nxL v224,s[96:99],s39 offen sc1 lds\nxU v56,v56,v40\nxU v57,v57,v41\n"
"xM v[104:107],v[188:191],a[20:23],v[104:107]\nxR a[112:115],v228 offset:57344\nxC v56,v56,v57\nxU v58,v42,v58\nxU v59,v43,v59\n"
"xM v[104:107],v[192:195],a[36:39],v[104:107]\nxA m0,s32,110592\nxE v42,-v42\nxE v43,-v43\n"
"xM v[112:115],v[184:187],a[8:11],0\nxA s39,s28,0\nxT v[168:169],v212 offset:12288\nv_add_f32 v42, 1.0, v42\n"
"xM v[112:115],v[188:191],a[24:27],v[112:115]\nxL v219,s[56:59],s39 offen lds\nv_add_f32 v43, 1.0, v43\nv_rcp_f32 v42, v42\n"
"xM v[112:115],v[192:195],a[40:43],v[112:115]\nxT v[170:171],v212 offset:14336\nxT v[172:173],v212 offset:16384\nv_rcp_f32 v43, v43\nxU v58,v58,v42\n"
"xM v[120:123],v[184:187],a[12:15],0\nxU v59,v59,v43\nxC v57,v58,v59\n"
"xM v[120:123],v[188:191],a[28:31],v[120:123]\nxW lgkmcnt(4)\nds_write_b64 v227, v[56:57] offset:2048\nbuffer_store_dwordx2 v[56:57], v227, s[96:99], s62 offen offset:2048 sc1\n"
"xM v[120:123],v[192:195],a[44:47],v[120:123]\nxA m0,s32,111616\nxU v60,v44,v60\nxU v61,v45,v61\n"
"xM v[100:103],v[196:199],a[0:3],0\nxA s39,s28,9216\nxT v[174:175],v212 offset:18432\nxT v[176:177],v212 offset:20480\nxE v44,-v44\nxE v45,-v45\n"
"xM v[100:103],v[160:163],a[16:19],v[100:103]\nxL v219,s[56:59],s39 offen lds\nv_add_f32 v44, 1.0, v44\nv_add_f32 v45, 1.0, v45\n"
"xM v[100:103],v[164:167],a[32:35],v[100:103]\nv_rcp_f32 v44, v44\nxM v[108:111],v[196:199],a[4:7],0\nv_rcp_f32 v45, v45\nxU v60,v60,v44\n"
"xM v[108:111],v[160:163],a[20:23],v[108:111]\nxT v[178:179],v212 offset:22528\nxU v61,v61,v45\nxC v60,v60,v61\nxU v62,v46,v62\n"
"xM v[108:111],v[164:167],a[36:39],v[108:111]\nxA m0,s32,112640\nxT v[180:181],v213 offset:12288\nxU v63,v47,v63\nxE v46,-v46\n"
"xM v[116:119],v[196:199],a[8:11],0\nxA s39,s28,18432\nxE v47,-v47\n"
"xM v[116:119],v[160:163],a[24:27],v[116:119]\nxL v219,s[56:59],s39 offen lds\nv_add_f32 v46, 1.0, v46\nv_add_f32 v47, 1.0, v47\nv_rcp_f32 v46, v46\n"
"xM v[116:119],v[164:167],a[40:43],v[116:119]\nxT v[182:183],v213 offset:14336\nxT v[184:185],v213 offset:16384\nv_rcp_f32 v47, v47\n"
"xM v[124:127],v[196:199],a[12:15],0\nxU v62,v62,v46\nxU v63,v63,v47\nxC v61,v62,v63\n"
"xM v[124:127],v[160:163],a[28:31],v[124:127]\nxW lgkmcnt(4)\nds_write_b64 v227, v[60:61] offset:3072\nbuffer_store_dwordx2 v[60:61], v227, s[96:99], s62 offen offset:3072 sc1\n"
"xM v[124:127],v[164:167],a[44:47],v[124:127]\nxA m0,s32,147456\nv_add_u32 v214, v214, v233\nv_add_u32 v215, v215, v233\n"
"xM v[96:99],v[168:171],a[48:51],v[96:99]\nxA s39,s28,110592\nxT v[186:187],v213 offset:18432\nv_add_u32 v216, v216, v233\n"
"xM v[96:99],v[172:175],a[64:67],v[96:99]\nxW lgkmcnt(4)\nxL v219,s[56:59],s39 offen lds\n"
"xM v[96:99],v[176:179],a[80:83],v[96:99]\nxT v[188:189],v213 offset:20480\nxT v[190:191],v213 offset:22528\n"
"xM v[104:107],v[168:171],a[52:55],v[104:107]\nxM v[104:107],v[172:175],a[68:71],v[104:107]\nxW vmcnt(9) lgkmcnt(3)\ns_barrier\n"
"xM v[104:107],v[176:179],a[84:87],v[104:107]\nxA m0,s32,148480\nds_read_b64 a[128:129], v229 offset:0\nxT v[192:193],v210 offset:24576\n"
"xM v[112:115],v[168:171],a[56:59],v[112:115]\nxA s39,s28,119808\nds_read_b64 a[130:131], v229 offset:512\nxT v[194:195],v210 offset:26624\n"
"xM v[112:115],v[172:175],a[72:75],v[112:115]\nxL v219,s[56:59],s39 offen lds\nxM v[112:115],v[176:179],a[88:91],v[112:115]\nxR a[100:103],v228 offset:50176\nxT v[196:197],v210 offset:28672\n"
"xM v[120:123],v[168:171],a[60:63],v[120:123]\nxR a[116:119],v228 offset:58368\nxT v[198:199],v210 offset:30720\n"
"xM v[120:123],v[172:175],a[76:79],v[120:123]\nds_read_b64 a[132:133], v229 offset:1024\nxT v[160:161],v210 offset:32768\n"
"xM v[120:123],v[176:179],a[92:95],v[120:123]\nxA m0,s32,149504\nds_read_b64 a[134:135], v229 offset:1536\nxT v[162:163],v210 offset:34816\n"
"xM v[100:103],v[180:183],a[48:51],v[100:103]\nxA s39,s28,129024\nxR a[104:107],v228 offset:51200\nxT v[164:165],v211 offset:24576\nxW lgkmcnt(15)\n"
"xM v[100:103],v[184:187],a[64:67],v[100:103]\nxW lgkmcnt(12)\nxL v219,s[56:59],s39 offen lds\n"
"xM v[100:103],v[188:191],a[80:83],v[100:103]\nxR a[120:123],v228 offset:59392\nxT v[166:167],v211 offset:26624\n"
"xM v[108:111],v[180:183],a[52:55],v[108:111]\nds_read_b64 a[136:137], v229 offset:2048\nxT v[168:169],v211 offset:28672\n"
"xM v[108:111],v[184:187],a[68:71],v[108:111]\nds_read_b64 a[138:139], v229 offset:2560\nxT v[170:171],v211 offset:30720\n"
"xM v[108:111],v[188:191],a[84:87],v[108:111]\nxA m0,s32,73728\nxR a[108:111],v228 offset:52224\n"
"xM v[116:119],v[180:183],a[56:59],v[116:119]\nxA s39,s28,128\nxR a[124:127],v228 offset:60416\nxT v[172:173],v211 offset:32768\n"
"xM v[116:119],v[184:187],a[72:75],v[116:119]\nxL v219,s[56:59],s39 offen lds\nxM v[116:119],v[188:191],a[88:91],v[116:119]\nds_read_b64 a[140:141], v229 offset:3072\nxT v[174:175],v211 offset:34816\n"
"xM v[124:127],v[180:183],a[60:63],v[124:127]\nds_read_b64 a[142:143], v229 offset:3584\nxM v[124:127],v[184:187],a[76:79],v[124:127]\nxW lgkmcnt(10)\nxR a[144:147],v228 offset:0\nxT v[176:177],v210 offset:36864\n"
"xM v[124:127],v[188:191],a[92:95],v[124:127]\nxA m0,s32,74752\nxR a[160:163],v228 offset:8192\n"
"xM v[64:67],v[192:195],a[96:99],v[64:67]\nxA s39,s28,9344\nxR a[176:179],v228 offset:16384\nxT v[178:179],v210 offset:38912\n"
"xM v[64:67],v[196:199],a[112:115],v[64:67]\nxL v219,s[56:59],s39 offen lds\nxM v[64:67],v[160:163],a[128:131],v[64:67]\nxR a[148:151],v228 offset:1024\nxT v[180:181],v210 offset:40960\n"
"xM v[72:75],v[192:195],a[100:103],v[72:75]\nTC1_2_2_%=:\ns_cbranch_execz TDONE1_%=\nxR a[164:167],v228 offset:9216\n"
"xM v[72:75],v[196:199],a[116:119],v[72:75]\nxR a[180:183],v228 offset:17408\nxT v[182:183],v210 offset:43008\n"
"xM v[72:75],v[160:163],a[132:135],v[72:75]\nxA m0,s32,75776\nxR a[152:155],v228 offset:2048\n"
"xM v[80:83],v[192:195],a[104:107],v[80:83]\nxA s39,s28,18560\nxR a[168:171],v228 offset:10240\nxT v[184:185],v210 offset:45056\n"
"xM v[80:83],v[196:199],a[120:123],v[80:83]\nxW lgkmcnt(10)\nxL v219,s[56:59],s39 offen lds\n"
"xM v[80:83],v[160:163],a[136:139],v[80:83]\nxR a[184:187],v228 offset:18432\nxT v[186:187],v210 offset:47104\n"
"xM v[88:91],v[192:195],a[108:111],v[88:91]\nxR a[156:159],v228 offset:3072\nxM v[88:91],v[196:199],a[124:127],v[88:91]\nxR a[172:175],v228 offset:11264\nxT v[188:189],v211 offset:36864\n"
"xM v[88:91],v[160:163],a[140:143],v[88:91]\nxA m0,s32,122880\nxR a[188:191],v228 offset:19456\n"
"xM v[68:71],v[164:167],a[96:99],v[68:71]\nxA s39,s28,110720\nxR a[192:195],v228 offset:24576\nxT v[190:191],v211 offset:38912\n"
"xM v[68:71],v[168:171],a[112:115],v[68:71]\nxL v219,s[56:59],s39 offen lds\nxM v[68:71],v[172:175],a[128:131],v[68:71]\nxT v[192:193],v211 offset:40960\n"
"xM v[76:79],v[164:167],a[100:103],v[76:79]\nxM v[76:79],v[168:171],a[116:119],v[76:79]\nxT v[194:195],v211 offset:43008\n"
"xM v[76:79],v[172:175],a[132:135],v[76:79]\nxA m0,s32,123904\nxM v[84:87],v[164:167],a[104:107],v[84:87]\nxA s39,s28,119936\nxT v[196:197],v211 offset:45056\n"
"xM v[84:87],v[168:171],a[120:123],v[84:87]\nxL v219,s[56:59],s39 offen lds\nxM v[84:87],v[172:175],a[136:139],v[84:87]\nxT v[198:199],v211 offset:47104\n"
"xM v[92:95],v[164:167],a[108:111],v[92:95]\nxM v[92:95],v[168:171],a[124:127],v[92:95]\nxW lgkmcnt(3)\n"
"xM v[92:95],v[172:175],a[140:143],v[92:95]\nxA m0,s32,124928\nxM v[96:99],v[176:179],a[96:99],v[96:99]\nxA s39,s28,129152\n"
"xM v[96:99],v[180:183],a[112:115],v[96:99]\nxL v219,s[56:59],s39 offen lds\nxM v[96:99],v[184:187],a[128:131],v[96:99]\n"
"xM v[104:107],v[176:179],a[100:103],v[104:107]\nxM v[104:107],v[180:183],a[116:119],v[104:107]\nxW vmcnt(6)\ns_barrier\n"
"xM v[104:107],v[184:187],a[132:135],v[104:107]\nxR a[208:211],v228 offset:32768\nxT v[160:161],v210 offset:49152\n"
"xM v[112:115],v[176:179],a[104:107],v[112:115]\nxW vmcnt(9)\ns_mov_b64 exec, 1\nbuffer_store_dwordx2 v[242:243], off, s[96:99], s53 sc1\ns_mov_b64 exec, -1\n"
"xM v[112:115],v[180:183],a[120:123],v[112:115]\nxA s53,s53,32\nxR a[224:227],v228 offset:40960\nxT v[162:163],v210 offset:51200\n"
"xM v[112:115],v[184:187],a[136:139],v[112:115]\nxA m0,s32,86016\nxR a[196:199],v228 offset:25600\nxT v[164:165],v210 offset:53248\n"
"xM v[120:123],v[176:179],a[108:111],v[120:123]\nxA s39,s34,256\nxR a[212:215],v228 offset:33792\nxT v[166:167],v210 offset:55296\n"
"xM v[120:123],v[180:183],a[124:127],v[120:123]\nxL v219,s[56:59],s39 offen lds\nxM v[120:123],v[184:187],a[140:143],v[120:123]\nxR a[228:231],v228 offset:41984\nxT v[168:169],v210 offset:57344\n"
"xM v[100:103],v[188:191],a[96:99],v[100:103]\nxW lgkmcnt(10)\nxR a[200:203],v228 offset:26624\nxT v[170:171],v210 offset:59392\n"
"xM v[100:103],v[192:195],a[112:115],v[100:103]\nxR a[216:219],v228 offset:34816\nxT v[172:173],v211 offset:49152\n"
"xM v[100:103],v[196:199],a[128:131],v[100:103]\nxA m0,s32,87040\nxR a[232:235],v228 offset:43008\nxT v[174:175],v211 offset:51200\n"
"xM v[108:111],v[188:191],a[100:103],v[108:111]\nxA s39,s34,9472\nxR a[204:207],v228 offset:27648\nxT v[176:177],v211 offset:53248\n"
"xM v[108:111],v[192:195],a[116:119],v[108:111]\nxL v219,s[56:59],s39 offen lds\nxM v[108:111],v[196:199],a[132:135],v[108:111]\nxR a[220:223],v228 offset:35840\nxT v[178:179],v211 offset:55296\n"
"xM v[116:119],v[188:191],a[104:107],v[116:119]\nxR a[236:239],v228 offset:44032\nxT v[180:181],v211 offset:57344\n"
"xM v[116:119],v[192:195],a[120:123],v[116:119]\nxM v[116:119],v[196:199],a[136:139],v[116:119]\nxA m0,s32,88064\nxT v[182:183],v211 offset:59392\n"
"xM v[124:127],v[188:191],a[108:111],v[124:127]\nxA s39,s34,18688\nxM v[124:127],v[192:195],a[124:127],v[124:127]\nxL v219,s[56:59],s39 offen lds\n"
"xM v[124:127],v[196:199],a[140:143],v[124:127]\nxW lgkmcnt(5)\nxT v[184:185],v212 offset:24576\n"
"xM v[64:67],v[160:163],a[144:147],v[64:67]\nxT v[186:187],v212 offset:26624\nxM v[64:67],v[164:167],a[160:163],v[64:67]\n"
"xM v[64:67],v[168:171],a[176:179],v[64:67]\nxA m0,s32,135168\nxT v[188:189],v212 offset:28672\n"
"xM v[72:75],v[160:163],a[148:151],v[72:75]\nxA s39,s34,110848\nxM v[72:75],v[164:167],a[164:167],v[72:75]\nxL v219,s[56:59],s39 offen lds\n"
"xM v[72:75],v[168:171],a[180:183],v[72:75]\nxT v[190:191],v212 offset:30720\nxM v[80:83],v[160:163],a[152:155],v[80:83]\nxT v[192:193],v212 offset:32768\n"
"xM v[80:83],v[164:167],a[168:171],v[80:83]\nxM v[80:83],v[168:171],a[184:187],v[80:83]\nxA m0,s32,136192\nxT v[194:195],v212 offset:34816\n"
"xM v[88:91],v[160:163],a[156:159],v[88:91]\nxA s39,s34,120064\nxM v[88:91],v[164:167],a[172:175],v[88:91]\nxL v219,s[56:59],s39 offen lds\n"
"xM v[88:91],v[168:171],a[188:191],v[88:91]\nxT v[196:197],v213 offset:24576\nxM v[68:71],v[172:175],a[144:147],v[68:71]\nxW lgkmcnt(4)\nxT v[198:199],v213 offset:26624\n"
"xM v[68:71],v[176:179],a[160:163],v[68:71]\nxM v[68:71],v[180:183],a[176:179],v[68:71]\nxA m0,s32,137216\nxT v[160:161],v213 offset:28672\n"
"xM v[76:79],v[172:175],a[148:151],v[76:79]\nxA s39,s34,129280\nxM v[76:79],v[176:179],a[164:167],v[76:79]\nxL v219,s[56:59],s39 offen lds\n"
"xM v[76:79],v[180:183],a[180:183],v[76:79]\nxT v[162:163],v213 offset:30720\nxM v[84:87],v[172:175],a[152:155],v[84:87]\nxT v[164:165],v213 offset:32768\n"
"xM v[84:87],v[176:179],a[168:171],v[84:87]\nxM v[84:87],v[180:183],a[184:187],v[84:87]\nxT v[166:167],v213 offset:34816\n"
"xM v[92:95],v[172:175],a[156:159],v[92:95]\nxM v[92:95],v[176:179],a[172:175],v[92:95]\nxM v[92:95],v[180:183],a[188:191],v[92:95]\n"
"xM v[64:67],v[184:187],a[192:195],v[64:67]\nxW lgkmcnt(3)\nxM v[64:67],v[188:191],a[208:211],v[64:67]\n"
"xM v[64:67],v[192:195],a[224:227],v[64:67]\nxM v[72:75],v[184:187],a[196:199],v[72:75]\nxM v[72:75],v[188:191],a[212:215],v[72:75]\nxW vmcnt(7)\ns_barrier\n"
"xM v[72:75],v[192:195],a[228:231],v[72:75]\nxA m0,s32,98304\nxT v[168:169],v210 offset:12288\nxT v[170:171],v210 offset:14336\n"
"xM v[80:83],v[184:187],a[200:203],v[80:83]\nxA s39,s40,256\nxT v[172:173],v210 offset:16384\nxT v[174:175],v210 offset:18432\n"
"xM v[80:83],v[188:191],a[216:219],v[80:83]\nxL v219,s[56:59],s39 offen lds\nxM v[80:83],v[192:195],a[232:235],v[80:83]\nbuffer_load_dwordx2 v[236:237], v238, s[96:99], s37 offen sc1\n"
"xM v[88:91],v[184:187],a[204:207],v[88:91]\nxT v[176:177],v210 offset:20480\nxT v[178:179],v210 offset:22528\n"
"xM v[88:91],v[188:191],a[220:223],v[88:91]\nxT v[180:181],v211 offset:12288\nxM v[88:91],v[192:195],a[236:239],v[88:91]\nxA m0,s32,99328\n"
"xM v[68:71],v[196:199],a[192:195],v[68:71]\nxA s39,s41,256\nxT v[182:183],v211 offset:14336\nxW lgkmcnt(10)\n"
"xM v[68:71],v[160:163],a[208:211],v[68:71]\nxW lgkmcnt(6)\nxL v219,s[56:59],s39 offen lds\n"
"xM v[68:71],v[164:167],a[224:227],v[68:71]\nxT v[184:185],v211 offset:16384\nxM v[76:79],v[196:199],a[196:199],v[76:79]\n"
"xM v[76:79],v[160:163],a[212:215],v[76:79]\nxT v[186:187],v211 offset:18432\nxM v[76:79],v[164:167],a[228:231],v[76:79]\nxA m0,s32,100352\n"
"xM v[84:87],v[196:199],a[200:203],v[84:87]\nxA s39,s42,256\nxT v[188:189],v211 offset:20480\n"
"xM v[84:87],v[160:163],a[216:219],v[84:87]\nxL v219,s[56:59],s39 offen lds\nxM v[84:87],v[164:167],a[232:235],v[84:87]\nxT v[190:191],v211 offset:22528\n"
"xM v[92:95],v[196:199],a[204:207],v[92:95]\nxM v[92:95],v[160:163],a[220:223],v[92:95]\nxT v[192:193],v212 offset:0\n"
"xM v[92:95],v[164:167],a[236:239],v[92:95]\nxA m0,s32,110592\nxM v[96:99],v[168:171],a[144:147],v[96:99]\nxA s39,s28,256\nxT v[194:195],v212 offset:2048\nxW lgkmcnt(10)\n"
"xM v[96:99],v[172:175],a[160:163],v[96:99]\nxW lgkmcnt(4)\nxL v219,s[56:59],s39 offen lds\n"
"xM v[96:99],v[176:179],a[176:179],v[96:99]\nxT v[196:197],v212 offset:4096\nxM v[104:107],v[168:171],a[148:151],v[104:107]\n"
"xM v[104:107],v[172:175],a[164:167],v[104:107]\nxT v[198:199],v212 offset:6144\nxM v[104:107],v[176:179],a[180:183],v[104:107]\nxA m0,s32,111616\n"
"xM v[112:115],v[168:171],a[152:155],v[112:115]\nxA s39,s28,9472\nxT v[160:161],v212 offset:8192\n"
"xM v[112:115],v[172:175],a[168:171],v[112:115]\nxL v219,s[56:59],s39 offen lds\nxM v[112:115],v[176:179],a[184:187],v[112:115]\nxT v[162:163],v212 offset:10240\n"
"xM v[120:123],v[168:171],a[156:159],v[120:123]\nxM v[120:123],v[172:175],a[172:175],v[120:123]\nxT v[164:165],v213 offset:0\n"
"xM v[120:123],v[176:179],a[188:191],v[120:123]\nxA m0,s32,112640\nxM v[100:103],v[180:183],a[144:147],v[100:103]\nxA s39,s28,18688\nxT v[166:167],v213 offset:2048\n"
"xM v[100:103],v[184:187],a[160:163],v[100:103]\nxW lgkmcnt(4)\nxL v219,s[56:59],s39 offen lds\n"
"xM v[100:103],v[188:191],a[176:179],v[100:103]\nxT v[168:169],v213 offset:4096\nxM v[108:111],v[180:183],a[148:151],v[108:111]\n"
"xM v[108:111],v[184:187],a[164:167],v[108:111]\nxT v[170:171],v213 offset:6144\nxM v[108:111],v[188:191],a[180:183],v[108:111]\n"
"xM v[116:119],v[180:183],a[152:155],v[116:119]\nxT v[172:173],v213 offset:8192\nxM v[116:119],v[184:187],a[168:171],v[116:119]\n"
"xM v[116:119],v[188:191],a[184:187],v[116:119]\nxT v[174:175],v213 offset:10240\nxM v[124:127],v[180:183],a[156:159],v[124:127]\n"
"xM v[124:127],v[184:187],a[172:175],v[124:127]\nxM v[124:127],v[188:191],a[188:191],v[124:127]\nxM v[96:99],v[192:195],a[192:195],v[96:99]\n"
"xM v[96:99],v[196:199],a[208:211],v[96:99]\nxW lgkmcnt(2)\nxM v[96:99],v[160:163],a[224:227],v[96:99]\n"
"xM v[104:107],v[192:195],a[196:199],v[104:107]\nxM v[104:107],v[196:199],a[212:215],v[104:107]\nxW vmcnt(5) lgkmcnt(15)\nv_cmp_ne_u64 vcc, s[68:69], v[236:237]\ns_and_b64 vcc, vcc, s[100:101]\ns_and_b32 s38, vcc_lo, 3840\n"
"s_cbranch_scc1 SPINY2_%=\nRETY2_%=:\ns_barrier\n"
"xM v[104:107],v[160:163],a[228:231],v[104:107]\nxA m0,s32,73728\nxT v[176:177],v210 offset:24576\nxT v[178:179],v210 offset:26624\n"
"xM v[112:115],v[192:195],a[200:203],v[112:115]\nxA s39,s29,0\nxT v[180:181],v210 offset:28672\nxT v[182:183],v210 offset:30720\n"
"xM v[112:115],v[196:199],a[216:219],v[112:115]\nxL v219,s[56:59],s39 offen lds\nxM v[112:115],v[160:163],a[232:235],v[112:115]\nxT v[184:185],v210 offset:32768\nxT v[186:187],v210 offset:34816\n"
"xM v[120:123],v[192:195],a[204:207],v[120:123]\nxA m0,s45,49152\nxM v[120:123],v[196:199],a[220:223],v[120:123]\nxA s39,s63,49152\nxT v[188:189],v211 offset:24576\n"
"xM v[120:123],v[160:163],a[236:239],v[120:123]\nxL v224,s[96:99],s39 offen sc1 lds\nxM v[100:103],v[164:167],a[192:195],v[100:103]\nxW lgkmcnt(7)\nxT v[190:191],v211 offset:26624\n"
"xM v[100:103],v[168:171],a[208:211],v[100:103]\nxA m0,s45,53248\nxM v[100:103],v[172:175],a[224:227],v[100:103]\nxA s39,s63,53248\nxT v[192:193],v211 offset:28672\n"
"xM v[108:111],v[164:167],a[196:199],v[108:111]\nxL v224,s[96:99],s39 offen sc1 lds\nxM v[108:111],v[168:171],a[212:215],v[108:111]\nxT v[194:195],v211 offset:30720\n"
"xM v[108:111],v[172:175],a[228:231],v[108:111]\nxA m0,s32,74752\nxM v[116:119],v[164:167],a[200:203],v[116:119]\nxA s39,s30,0\nxT v[196:197],v211 offset:32768\n"
"xM v[116:119],v[168:171],a[216:219],v[116:119]\nxL v219,s[56:59],s39 offen lds\nxM v[116:119],v[172:175],a[232:235],v[116:119]\nxT v[198:199],v211 offset:34816\n"
"xM v[124:127],v[164:167],a[204:207],v[124:127]\nxA m0,s45,57344\nxM v[124:127],v[168:171],a[220:223],v[124:127]\nxA s39,s63,57344\nxT v[160:161],v212 offset:12288\n"
"xM v[124:127],v[172:175],a[236:239],v[124:127]\nxW lgkmcnt(4)\nxL v224,s[96:99],s39 offen sc1 lds\n"
"xM v[128:131],v[176:179],a[0:3],0\nxT v[162:163],v212 offset:14336\nxM v[128:131],v[180:183],a[16:19],v[128:131]\nxA m0,s45,61440\n"
"xM v[128:131],v[184:187],a[32:35],v[128:131]\nxA s39,s63,61440\nxT v[164:165],v212 offset:16384\n"
"xM v[136:139],v[176:179],a[4:7],0\nxL v224,s[96:99],s39 offen sc1 lds\nxM v[136:139],v[180:183],a[20:23],v[136:139]\nxT v[166:167],v212 offset:18432\n"
"xM v[136:139],v[184:187],a[36:39],v[136:139]\nxA m0,s32,75776\nxM v[144:147],v[176:179],a[8:11],0\nxA s39,s31,0\nxT v[168:169],v212 offset:20480\n"
"xM v[144:147],v[180:183],a[24:27],v[144:147]\nxL v219,s[56:59],s39 offen lds\nxM v[144:147],v[184:187],a[40:43],v[144:147]\nxT v[170:171],v212 offset:22528\n"
"xM v[152:155],v[176:179],a[12:15],0\nxA m0,s45,65536\nxM v[152:155],v[180:183],a[28:31],v[152:155]\nxA s39,s63,65536\nxT v[172:173],v213 offset:12288\n"
"xM v[152:155],v[184:187],a[44:47],v[152:155]\nxL v224,s[96:99],s39 offen sc1 lds\nxM v[132:135],v[188:191],a[0:3],0\nxW lgkmcnt(3)\nxT v[174:175],v213 offset:14336\n"
"xM v[132:135],v[192:195],a[16:19],v[132:135]\nxA m0,s45,69632\nxM v[132:135],v[196:199],a[32:35],v[132:135]\nxA s39,s63,69632\nxT v[176:177],v213 offset:16384\n"
"xM v[140:143],v[188:191],a[4:7],0\nxL v224,s[96:99],s39 offen sc1 lds\nxM v[140:143],v[192:195],a[20:23],v[140:143]\nxT v[178:179],v213 offset:18432\n"
"xM v[140:143],v[196:199],a[36:39],v[140:143]\nxA m0,s32,147456\nxM v[148:151],v[188:191],a[8:11],0\nxA s39,s29,128\nxT v[180:181],v213 offset:20480\n"
"xM v[148:151],v[192:195],a[24:27],v[148:151]\nxL v219,s[56:59],s39 offen lds\nxM v[148:151],v[196:199],a[40:43],v[148:151]\nxT v[182:183],v213 offset:22528\n"
"xM v[156:159],v[188:191],a[12:15],0\nxM v[156:159],v[192:195],a[28:31],v[156:159]\nxM v[156:159],v[196:199],a[44:47],v[156:159]\nxA m0,s32,148480\n"
"xM v[128:131],v[160:163],a[48:51],v[128:131]\nxA s39,s30,128\nxM v[128:131],v[164:167],a[64:67],v[128:131]\nxW lgkmcnt(2)\nxL v219,s[56:59],s39 offen lds\n"
"xM v[128:131],v[168:171],a[80:83],v[128:131]\nxM v[136:139],v[160:163],a[52:55],v[136:139]\nxM v[136:139],v[164:167],a[68:71],v[136:139]\nxW vmcnt(11)\ns_barrier\n"
"xM v[136:139],v[168:171],a[84:87],v[136:139]\nxA m0,s32,149504\nxT v[184:185],v210 offset:36864\nxT v[186:187],v210 offset:38912\n"
"xM v[144:147],v[160:163],a[56:59],v[144:147]\nxA s39,s31,128\nxT v[188:189],v210 offset:40960\nxT v[190:191],v210 offset:43008\n"
"xM v[144:147],v[164:167],a[72:75],v[144:147]\nxL v219,s[56:59],s39 offen lds\nxM v[144:147],v[168:171],a[88:91],v[144:147]\nxT v[192:193],v210 offset:45056\nxT v[194:195],v210 offset:47104\n"
"xM v[152:155],v[160:163],a[60:63],v[152:155]\nxM v[152:155],v[164:167],a[76:79],v[152:155]\nxT v[196:197],v211 offset:36864\n"
"xM v[152:155],v[168:171],a[92:95],v[152:155]\nxA m0,s32,86016\nxM v[132:135],v[172:175],a[48:51],v[132:135]\nxA s39,s28,110848\nxT v[198:199],v211 offset:38912\n"
"xM v[132:135],v[176:179],a[64:67],v[132:135]\nxW lgkmcnt(6)\nxL v219,s[56:59],s39 offen lds\n"
"xM v[132:135],v[180:183],a[80:83],v[132:135]\nxT v[160:161],v211 offset:40960\nxM v[140:143],v[172:175],a[52:55],v[140:143]\n"
"xM v[140:143],v[176:179],a[68:71],v[140:143]\nxT v[162:163],v211 offset:43008\nxM v[140:143],v[180:183],a[84:87],v[140:143]\nxA m0,s32,87040\n"
"xM v[148:151],v[172:175],a[56:59],v[148:151]\nxA s39,s28,120064\nxT v[164:165],v211 offset:45056\n"
"xM v[148:151],v[176:179],a[72:75],v[148:151]\nxL v219,s[56:59],s39 offen lds\nxM v[148:151],v[180:183],a[88:91],v[148:151]\nxT v[166:167],v211 offset:47104\n"
"xM v[156:159],v[172:175],a[60:63],v[156:159]\nxM v[156:159],v[176:179],a[76:79],v[156:159]\nxT v[168:169],v210 offset:49152\n"
"xM v[156:159],v[180:183],a[92:95],v[156:159]\nxA m0,s32,88064\nxM v[128:131],v[184:187],a[96:99],v[128:131]\nxA s39,s28,129280\nxT v[170:171],v210 offset:51200\nxW lgkmcnt(10)\n"
"xM v[128:131],v[188:191],a[112:115],v[128:131]\nxW lgkmcnt(4)\nxL v219,s[56:59],s39 offen lds\n"
"xM v[128:131],v[192:195],a[128:131],v[128:131]\nxT v[172:173],v210 offset:53248\nxM v[136:139],v[184:187],a[100:103],v[136:139]\n"
"xM v[136:139],v[188:191],a[116:119],v[136:139]\nxT v[174:175],v210 offset:55296\nxM v[136:139],v[192:195],a[132:135],v[136:139]\nxA m0,s32,122880\n"
"xM v[144:147],v[184:187],a[104:107],v[144:147]\nxA s39,s29,256\nxT v[176:177],v210 offset:57344\n"
"xM v[144:147],v[188:191],a[120:123],v[144:147]\nxL v219,s[56:59],s39 offen lds\nxM v[144:147],v[192:195],a[136:139],v[144:147]\nxT v[178:179],v210 offset:59392\n"
"xM v[152:155],v[184:187],a[108:111],v[152:155]\nxM v[152:155],v[188:191],a[124:127],v[152:155]\nxT v[180:181],v211 offset:49152\n"
"xM v[152:155],v[192:195],a[140:143],v[152:155]\nxA m0,s32,123904\nxM v[132:135],v[196:199],a[96:99],v[132:135]\nxA s39,s30,256\nxT v[182:183],v211 offset:51200\n"
"xM v[132:135],v[160:163],a[112:115],v[132:135]\nxW lgkmcnt(4)\nxL v219,s[56:59],s39 offen lds\n"
"xM v[132:135],v[164:167],a[128:131],v[132:135]\nxT v[184:185],v211 offset:53248\nxM v[140:143],v[196:199],a[100:103],v[140:143]\n"
"xM v[140:143],v[160:163],a[116:119],v[140:143]\nxT v[186:187],v211 offset:55296\nxM v[140:143],v[164:167],a[132:135],v[140:143]\nxA m0,s32,124928\n"
"xM v[148:151],v[196:199],a[104:107],v[148:151]\nxA s39,s31,256\nxT v[188:189],v211 offset:57344\n"
"xM v[148:151],v[160:163],a[120:123],v[148:151]\nxL v219,s[56:59],s39 offen lds\nxM v[148:151],v[164:167],a[136:139],v[148:151]\nxT v[190:191],v211 offset:59392\n"
"xM v[156:159],v[196:199],a[108:111],v[156:159]\nxM v[156:159],v[160:163],a[124:127],v[156:159]\nxM v[156:159],v[164:167],a[140:143],v[156:159]\n"
"xM v[128:131],v[168:171],a[144:147],v[128:131]\nxM v[128:131],v[172:175],a[160:163],v[128:131]\nxW lgkmcnt(2)\n"
"xM v[128:131],v[176:179],a[176:179],v[128:131]\nxM v[136:139],v[168:171],a[148:151],v[136:139]\nxM v[136:139],v[172:175],a[164:167],v[136:139]\nxW vmcnt(6)\ns_barrier\n"
"xM v[136:139],v[176:179],a[180:183],v[136:139]\nxA m0,s32,98304\nxR a[240:243],v228 offset:49152\nxT v[192:193],v210 offset:12288\n"
"xM v[144:147],v[168:171],a[152:155],v[144:147]\nxA s39,s34,384\nxR v[0:3],v228 offset:57344\nxT v[194:195],v210 offset:14336\n"
"xM v[144:147],v[172:175],a[168:171],v[144:147]\nxL v219,s[56:59],s39 offen lds\nxM v[144:147],v[176:179],a[184:187],v[144:147]\nds_read_b64 v[16:17], v229 offset:0\nxT v[196:197],v210 offset:16384\n"
"xM v[152:155],v[168:171],a[156:159],v[152:155]\nds_read_b64 v[18:19], v229 offset:512\nxT v[198:199],v210 offset:18432\n"
"xM v[152:155],v[172:175],a[172:175],v[152:155]\nxR a[244:247],v228 offset:50176\nxT v[160:161],v210 offset:20480\n"
"xM v[152:155],v[176:179],a[188:191],v[152:155]\nxA m0,s32,99328\nxR v[4:7],v228 offset:58368\nxT v[162:163],v210 offset:22528\n"
"xM v[132:135],v[180:183],a[144:147],v[132:135]\nxA s39,s34,9600\nds_read_b64 v[20:21], v229 offset:1024\nxT v[164:165],v211 offset:12288\n"
"xM v[132:135],v[184:187],a[160:163],v[132:135]\nxW lgkmcnt(12)\nxL v219,s[56:59],s39 offen lds\n"
"xM v[132:135],v[188:191],a[176:179],v[132:135]\nds_read_b64 v[22:23], v229 offset:1536\nxT v[166:167],v211 offset:14336\n"
"xM v[140:143],v[180:183],a[148:151],v[140:143]\nxR a[248:251],v228 offset:51200\nxT v[168:169],v211 offset:16384\n"
"xM v[140:143],v[184:187],a[164:167],v[140:143]\nxR v[8:11],v228 offset:59392\nxT v[170:171],v211 offset:18432\n"
"xM v[140:143],v[188:191],a[180:183],v[140:143]\nxA m0,s32,100352\nds_read_b64 v[24:25], v229 offset:2048\n"
"xM v[148:151],v[180:183],a[152:155],v[148:151]\nxA s39,s34,18816\nds_read_b64 v[26:27], v229 offset:2560\nxT v[172:173],v211 offset:20480\n"
"xM v[148:151],v[184:187],a[168:171],v[148:151]\nxL v219,s[56:59],s39 offen lds\nxM v[148:151],v[188:191],a[184:187],v[148:151]\nTC1_3_2_%=:\ns_cbranch_execz TDONE1_%=\nxR a[252:255],v228 offset:52224\n"
"xT v[174:175],v211 offset:22528\nxM v[156:159],v[180:183],a[156:159],v[156:159]\nxR v[12:15],v228 offset:60416\n"
"xM v[156:159],v[184:187],a[172:175],v[156:159]\nxW lgkmcnt(10)\nds_read_b64 v[28:29], v229 offset:3072\nxT v[176:177],v212 offset:24576\n"
"xM v[156:159],v[188:191],a[188:191],v[156:159]\nxA m0,s32,135168\nds_read_b64 v[30:31], v229 offset:3584\n"
"xM v[64:67],v[192:195],a[240:243],v[64:67]\nxA s39,s34,110976\nxT v[178:179],v212 offset:26624\n"
"xM v[64:67],v[196:199],v[0:3],v[64:67]\nxL v219,s[56:59],s39 offen lds\nxM v[64:67],v[160:163],v[16:19],v[64:67]\nxT v[180:181],v212 offset:28672\n"
"xM v[72:75],v[192:195],a[244:247],v[72:75]\nxM v[72:75],v[196:199],v[4:7],v[72:75]\nxW lgkmcnt(6)\nxT v[182:183],v212 offset:30720\n"
"xM v[72:75],v[160:163],v[20:23],v[72:75]\nxA m0,s32,136192\nxM v[80:83],v[192:195],a[248:251],v[80:83]\nxA s39,s34,120192\nxT v[184:185],v212 offset:32768\n"
"xM v[80:83],v[196:199],v[8:11],v[80:83]\nxL v219,s[56:59],s39 offen lds\nxM v[80:83],v[160:163],v[24:27],v[80:83]\nxT v[186:187],v212 offset:34816\n"
"xM v[88:91],v[192:195],a[252:255],v[88:91]\nxW lgkmcnt(4)\nxM v[88:91],v[196:199],v[12:15],v[88:91]\nxT v[188:189],v213 offset:24576\n"
"xM v[88:91],v[160:163],v[28:31],v[88:91]\nxA m0,s32,137216\nxM v[68:71],v[164:167],a[240:243],v[68:71]\nxA s39,s34,129408\nxT v[190:191],v213 offset:26624\n"
"xM v[68:71],v[168:171],v[0:3],v[68:71]\nxL v219,s[56:59],s39 offen lds\nxM v[68:71],v[172:175],v[16:19],v[68:71]\nxT v[192:193],v213 offset:28672\n"
"xM v[76:79],v[164:167],a[244:247],v[76:79]\nxM v[76:79],v[168:171],v[4:7],v[76:79]\nxT v[194:195],v213 offset:30720\n"
"xM v[76:79],v[172:175],v[20:23],v[76:79]\nxM v[84:87],v[164:167],a[248:251],v[84:87]\nxT v[196:197],v213 offset:32768\n"
"xM v[84:87],v[168:171],v[8:11],v[84:87]\nxM v[84:87],v[172:175],v[24:27],v[84:87]\nxT v[198:199],v213 offset:34816\n"
"xM v[92:95],v[164:167],a[252:255],v[92:95]\nxM v[92:95],v[168:171],v[12:15],v[92:95]\nxM v[92:95],v[172:175],v[28:31],v[92:95]\n"
"xM v[96:99],v[176:179],a[240:243],v[96:99]\nxW lgkmcnt(2)\nxM v[96:99],v[180:183],v[0:3],v[96:99]\n"
"xM v[96:99],v[184:187],v[16:19],v[96:99]\nxM v[104:107],v[176:179],a[244:247],v[104:107]\nxC v64,v64,v65\nxC v65,v66,v67\n"
"xM v[104:107],v[180:183],v[4:7],v[104:107]\nxW vmcnt(6)\ns_barrier\n"
"xM v[104:107],v[184:187],v[20:23],v[104:107]\nxC v66,v68,v69\nxC v67,v70,v71\n"
"xM v[112:115],v[176:179],a[248:251],v[112:115]\nxA m0,s32,73728\nxT v[160:161],v210 offset:24576\nxC v72,v72,v73\n"
"xM v[112:115],v[180:183],v[8:11],v[112:115]\nxA s39,s40,384\nxS v[64:67],v220,s[96:99],s46 offen sc1\n"
"xM v[112:115],v[184:187],v[24:27],v[112:115]\nxL v219,s[56:59],s39 offen lds\nxM v[120:123],v[176:179],a[252:255],v[120:123]\nxT v[162:163],v210 offset:26624\nxC v73,v74,v75\n"
"xM v[120:123],v[180:183],v[12:15],v[120:123]\nxT v[164:165],v210 offset:28672\nxC v74,v76,v77\n"
"xM v[120:123],v[184:187],v[28:31],v[120:123]\nxC v75,v78,v79\nxC v80,v80,v81\n"
"xM v[100:103],v[188:191],a[240:243],v[100:103]\nxA m0,s32,74752\nxT v[166:167],v210 offset:30720\nxC v81,v82,v83\n"
"xM v[100:103],v[192:195],v[0:3],v[100:103]\nxA s39,s41,384\nxC v82,v84,v85\nxC v83,v86,v87\nxW lgkmcnt(4)\n"
"xM v[100:103],v[196:199],v[16:19],v[100:103]\nxL v219,s[56:59],s39 offen lds\nxM v[108:111],v[188:191],a[244:247],v[108:111]\nxT v[168:169],v210 offset:32768\nxC v88,v88,v89\n"
"xM v[108:111],v[192:195],v[4:7],v[108:111]\nxT v[170:171],v210 offset:34816\nxC v89,v90,v91\n"
"xM v[108:111],v[196:199],v[20:23],v[108:111]\nxC v90,v92,v93\nxC v91,v94,v95\n"
"xM v[116:119],v[188:191],a[248:251],v[116:119]\nxA m0,s32,75776\nxT v[172:173],v211 offset:24576\nxT v[174:175],v211 offset:26624\n"
"xM v[116:119],v[192:195],v[8:11],v[116:119]\nxA s39,s42,384\nxT v[176:177],v211 offset:28672\nxT v[178:179],v211 offset:30720\n"
"xM v[116:119],v[196:199],v[24:27],v[116:119]\nxL v219,s[56:59],s39 offen lds\nxM v[124:127],v[188:191],a[252:255],v[124:127]\nxT v[180:181],v211 offset:32768\nxT v[182:183],v211 offset:34816\n"
"xM v[124:127],v[192:195],v[12:15],v[124:127]\nxT v[184:185],v212 offset:0\nxM v[124:127],v[196:199],v[28:31],v[124:127]\nxW lgkmcnt(8)\nxS v[72:75],v221,s[96:99],s46 offen sc1\n"
"xM v[128:131],v[160:163],a[192:195],v[128:131]\nxA m0,s32,110592\nxT v[186:187],v212 offset:2048\n"
"xM v[128:131],v[164:167],a[208:211],v[128:131]\nxA s39,s28,384\nxW lgkmcnt(8)\n"
"xM v[128:131],v[168:171],a[224:227],v[128:131]\nxL v219,s[56:59],s39 offen lds\nxM v[136:139],v[160:163],a[196:199],v[136:139]\nxC v96,v96,v97\nxC v97,v98,v99\n"
"xM v[136:139],v[164:167],a[212:215],v[136:139]\nxC v98,v100,v101\nxC v99,v102,v103\n"
"xM v[136:139],v[168:171],a[228:231],v[136:139]\nxC v104,v104,v105\nxC v105,v106,v107\n"
"xM v[144:147],v[160:163],a[200:203],v[144:147]\nxA m0,s32,111616\nxC v106,v108,v109\nxC v107,v110,v111\n"
"xM v[144:147],v[164:167],a[216:219],v[144:147]\nxA s39,s28,9600\nxC v112,v112,v113\nxC v113,v114,v115\n"
"xM v[144:147],v[168:171],a[232:235],v[144:147]\nxL v219,s[56:59],s39 offen lds\nxM v[152:155],v[160:163],a[204:207],v[152:155]\nxC v114,v116,v117\nxC v115,v118,v119\n"
"xM v[152:155],v[164:167],a[220:223],v[152:155]\nxT v[188:189],v212 offset:4096\nxC v120,v120,v121\n"
"xM v[152:155],v[168:171],a[236:239],v[152:155]\nxW lgkmcnt(1)\nxC v121,v122,v123\nxC v122,v124,v125\n"
"xM v[132:135],v[172:175],a[192:195],v[132:135]\nxA m0,s32,112640\nxT v[190:191],v212 offset:6144\nxC v123,v126,v127\n"
"xM v[132:135],v[176:179],a[208:211],v[132:135]\nxA s39,s28,18816\nxT v[192:193],v212 offset:8192\nxT v[194:195],v212 offset:10240\n"
"xM v[132:135],v[180:183],a[224:227],v[132:135]\nxL v219,s[56:59],s39 offen lds\nxM v[140:143],v[172:175],a[196:199],v[140:143]\nxS v[80:83],v222,s[96:99],s46 offen sc1\n"
"xM v[140:143],v[176:179],a[212:215],v[140:143]\nxT v[196:197],v213 offset:0\nxT v[198:199],v213 offset:2048\n"
"xM v[140:143],v[180:183],a[228:231],v[140:143]\nxT v[160:161],v213 offset:4096\nxT v[162:163],v213 offset:6144\n"
"xM v[148:151],v[172:175],a[200:203],v[148:151]\nxT v[164:165],v213 offset:8192\nxM v[148:151],v[176:179],a[216:219],v[148:151]\n"
"xM v[148:151],v[180:183],a[232:235],v[148:151]\nxT v[166:167],v213 offset:10240\nxM v[156:159],v[172:175],a[204:207],v[156:159]\n"
"xM v[156:159],v[176:179],a[220:223],v[156:159]\nxM v[156:159],v[180:183],a[236:239],v[156:159]\nxM v[128:131],v[184:187],a[240:243],v[128:131]\nxW lgkmcnt(4)\n"
"xM v[128:131],v[188:191],v[0:3],v[128:131]\nxM v[128:131],v[192:195],v[16:19],v[128:131]\nxM v[136:139],v[184:187],a[244:247],v[136:139]\n"
"xM v[136:139],v[188:191],v[4:7],v[136:139]\nxW vmcnt(9)\ns_barrier\n"
"xM v[136:139],v[192:195],v[20:23],v[136:139]\nxT v[168:169],v210 offset:36864\nxT v[170:171],v210 offset:38912\n"
"xM v[144:147],v[184:187],a[248:251],v[144:147]\nxA m0,s32,86016\nxT v[172:173],v210 offset:40960\nxT v[174:175],v210 offset:43008\n"
"xM v[144:147],v[188:191],v[8:11],v[144:147]\nxA s39,s28,110976\nxS v[88:91],v223,s[96:99],s46 offen sc1\n"
"xM v[144:147],v[192:195],v[24:27],v[144:147]\nxA s46,s46,1024\nxL v219,s[56:59],s39 offen lds\n"
"xM v[152:155],v[184:187],a[252:255],v[152:155]\nxT v[176:177],v210 offset:45056\nxT v[178:179],v210 offset:47104\n"
"xM v[152:155],v[188:191],v[12:15],v[152:155]\nxT v[180:181],v211 offset:36864\nxM v[152:155],v[192:195],v[28:31],v[152:155]\nxW lgkmcnt(7)\n"
"xM v[132:135],v[196:199],a[240:243],v[132:135]\nxA m0,s32,87040\nxT v[182:183],v211 offset:38912\n"
"xM v[132:135],v[160:163],v[0:3],v[132:135]\nxA s39,s28,120192\nxM v[132:135],v[164:167],v[16:19],v[132:135]\nxL v219,s[56:59],s39 offen lds\n"
"xM v[140:143],v[196:199],a[244:247],v[140:143]\nxT v[184:185],v211 offset:40960\nxM v[140:143],v[160:163],v[4:7],v[140:143]\nxT v[186:187],v211 offset:43008\n"
"xM v[140:143],v[164:167],v[20:23],v[140:143]\nxM v[148:151],v[196:199],a[248:251],v[148:151]\nxA m0,s32,88064\nxT v[188:189],v211 offset:45056\n"
"xM v[148:151],v[160:163],v[8:11],v[148:151]\nxA s39,s28,129408\nxM v[148:151],v[164:167],v[24:27],v[148:151]\nxL v219,s[56:59],s39 offen lds\n"
"xM v[156:159],v[196:199],a[252:255],v[156:159]\nxT v[190:191],v211 offset:47104\nxM v[156:159],v[160:163],v[12:15],v[156:159]\nxT v[192:193],v212 offset:12288\n"
"xM v[156:159],v[164:167],v[28:31],v[156:159]\nxW lgkmcnt(4)\nxS v[96:99],v220,s[96:99],s46 offen sc1\n"
"xM v[32:35],v[168:171],a[0:3],0\nxA m0,s32,147456\nxT v[194:195],v212 offset:14336\n"
"xM v[32:35],v[172:175],a[16:19],v[32:35]\nxA s39,s29,384\nxM v[32:35],v[176:179],a[32:35],v[32:35]\nxL v219,s[56:59],s39 offen lds\n"
"xM v[40:43],v[168:171],a[4:7],0\nxC v128,v128,v129\nxC v129,v130,v131\n"
"xM v[40:43],v[172:175],a[20:23],v[40:43]\nxC v130,v132,v133\nxC v131,v134,v135\n"
"xM v[40:43],v[176:179],a[36:39],v[40:43]\nxC v136,v136,v137\nxC v137,v138,v139\n"
"xM v[48:51],v[168:171],a[8:11],0\nxA m0,s32,148480\nxC v138,v140,v141\nxC v139,v142,v143\n"
"xM v[48:51],v[172:175],a[24:27],v[48:51]\nxA s39,s30,384\nxC v144,v144,v145\nxC v145,v146,v147\n"
"xM v[48:51],v[176:179],a[40:43],v[48:51]\nxL v219,s[56:59],s39 offen lds\nxM v[56:59],v[168:171],a[12:15],0\nxC v146,v148,v149\nxC v147,v150,v151\n"
"xM v[56:59],v[172:175],a[28:31],v[56:59]\nxT v[196:197],v212 offset:16384\nxC v152,v152,v153\n"
"xM v[56:59],v[176:179],a[44:47],v[56:59]\nxW lgkmcnt(1)\nxC v153,v154,v155\nxC v154,v156,v157\n"
"xM v[36:39],v[180:183],a[0:3],0\nxA m0,s32,149504\nxT v[198:199],v212 offset:18432\nxC v155,v158,v159\n"
"xM v[36:39],v[184:187],a[16:19],v[36:39]\nxA s39,s31,384\nxT v[160:161],v212 offset:20480\nxT v[162:163],v212 offset:22528\n"
"xM v[36:39],v[188:191],a[32:35],v[36:39]\nxL v219,s[56:59],s39 offen lds\nxM v[44:47],v[180:183],a[4:7],0\nxS v[104:107],v221,s[96:99],s46 offen sc1\n"
"xM v[44:47],v[184:187],a[20:23],v[44:47]\nxT v[164:165],v213 offset:12288\nxT v[166:167],v213 offset:14336\n"
"xM v[44:47],v[188:191],a[36:39],v[44:47]\nxT v[168:169],v213 offset:16384\nxT v[170:171],v213 offset:18432\n"
"xM v[52:55],v[180:183],a[8:11],0\nxA m0,s32,0\nxT v[172:173],v213 offset:20480\n"
"xM v[52:55],v[184:187],a[24:27],v[52:55]\nxA s39,s48,0\nxM v[52:55],v[188:191],a[40:43],v[52:55]\nxL v232,s[56:59],s39 offen lds\n"
"xM v[60:63],v[180:183],a[12:15],0\nxT v[174:175],v213 offset:22528\nxM v[60:63],v[184:187],a[28:31],v[60:63]\n"
"xM v[60:63],v[188:191],a[44:47],v[60:63]\nxM v[32:35],v[192:195],a[48:51],v[32:35]\nxW lgkmcnt(4)\n"
"xM v[32:35],v[196:199],a[64:67],v[32:35]\nxM v[32:35],v[160:163],a[80:83],v[32:35]\nxM v[40:43],v[192:195],a[52:55],v[40:43]\n"
"xM v[40:43],v[196:199],a[68:71],v[40:43]\nxW vmcnt(11)\ns_barrier\n"
"xM v[40:43],v[160:163],a[84:87],v[40:43]\nxT v[176:177],v210 offset:12288\nxT v[178:179],v210 offset:14336\n"
"xM v[48:51],v[192:195],a[56:59],v[48:51]\nxA m0,s32,1024\nxT v[180:181],v210 offset:16384\nxT v[182:183],v210 offset:18432\n"
"xM v[48:51],v[196:199],a[72:75],v[48:51]\nxA s39,s48,18432\nxS v[112:115],v222,s[96:99],s46 offen sc1\n"
"xM v[48:51],v[160:163],a[88:91],v[48:51]\nxL v232,s[56:59],s39 offen lds\nxM v[56:59],v[192:195],a[60:63],v[56:59]\nxT v[184:185],v210 offset:20480\nxT v[186:187],v210 offset:22528\n"
"xM v[56:59],v[196:199],a[76:79],v[56:59]\nxT v[188:189],v211 offset:12288\nxM v[56:59],v[160:163],a[92:95],v[56:59]\n"
"xM v[36:39],v[164:167],a[48:51],v[36:39]\nxW lgkmcnt(7)\nxT v[190:191],v211 offset:14336\n"
"xM v[36:39],v[168:171],a[64:67],v[36:39]\nxM v[36:39],v[172:175],a[80:83],v[36:39]\nxA m0,s32,2048\nxT v[192:193],v211 offset:16384\n"
"xM v[44:47],v[164:167],a[52:55],v[44:47]\nxA s39,s48,36864\nxM v[44:47],v[168:171],a[68:71],v[44:47]\nxL v232,s[56:59],s39 offen lds\n"
"xM v[44:47],v[172:175],a[84:87],v[44:47]\nxT v[194:195],v211 offset:18432\nxM v[52:55],v[164:167],a[56:59],v[52:55]\nxT v[196:197],v211 offset:20480\n"
"xM v[52:55],v[168:171],a[72:75],v[52:55]\nxM v[52:55],v[172:175],a[88:91],v[52:55]\nxT v[198:199],v211 offset:22528\n"
"xM v[60:63],v[164:167],a[60:63],v[60:63]\nxM v[60:63],v[168:171],a[76:79],v[60:63]\nxA m0,s32,12288\nxT v[160:161],v210 offset:49152\n"
"xM v[60:63],v[172:175],a[92:95],v[60:63]\nxA s39,s48,110592\nxS v[120:123],v223,s[96:99],s46 offen sc1\nxW lgkmcnt(11)\n"
"xM v[32:35],v[176:179],a[96:99],v[32:35]\nxA s46,s46,1024\nxL v232,s[56:59],s39 offen lds\nxW lgkmcnt(9)\n"
"xM v[32:35],v[180:183],a[112:115],v[32:35]\nxW lgkmcnt(3)\nxT v[162:163],v210 offset:51200\n"
"xM v[32:35],v[184:187],a[128:131],v[32:35]\nxT v[164:165],v210 offset:53248\nxM v[40:43],v[176:179],a[100:103],v[40:43]\n"
"xM v[40:43],v[180:183],a[116:119],v[40:43]\nxT v[166:167],v210 offset:55296\nxM v[40:43],v[184:187],a[132:135],v[40:43]\n"
"xM v[48:51],v[176:179],a[104:107],v[48:51]\nxA m0,s32,13312\nxT v[168:169],v210 offset:57344\n"
"xM v[48:51],v[180:183],a[120:123],v[48:51]\nxA s39,s48,129024\nxM v[48:51],v[184:187],a[136:139],v[48:51]\nxL v232,s[56:59],s39 offen lds\n"
"xM v[56:59],v[176:179],a[108:111],v[56:59]\nxT v[170:171],v210 offset:59392\nxM v[56:59],v[180:183],a[124:127],v[56:59]\nxT v[172:173],v211 offset:49152\n"
"xM v[56:59],v[184:187],a[140:143],v[56:59]\nxM v[36:39],v[188:191],a[96:99],v[36:39]\nxT v[174:175],v211 offset:51200\n"
"xM v[36:39],v[192:195],a[112:115],v[36:39]\nxW lgkmcnt(4)\nxM v[36:39],v[196:199],a[128:131],v[36:39]\nxA m0,s32,14336\nxT v[176:177],v211 offset:53248\n"
"xM v[44:47],v[188:191],a[100:103],v[44:47]\nxA s39,s48,147456\nxS v[128:131],v220,s[96:99],s46 offen sc1\n"
"xM v[44:47],v[192:195],a[116:119],v[44:47]\nxL v232,s[56:59],s39 offen lds\nxM v[44:47],v[196:199],a[132:135],v[44:47]\nxT v[178:179],v211 offset:55296\n"
"xM v[52:55],v[188:191],a[104:107],v[52:55]\nxT v[180:181],v211 offset:57344\nxM v[52:55],v[192:195],a[120:123],v[52:55]\n"
"xM v[52:55],v[196:199],a[136:139],v[52:55]\nxT v[182:183],v211 offset:59392\nxM v[60:63],v[188:191],a[108:111],v[60:63]\n"
"xM v[60:63],v[192:195],a[124:127],v[60:63]\nxA m0,s32,24576\nxM v[60:63],v[196:199],a[140:143],v[60:63]\nxA s39,s48,221184\n"
"xM v[32:35],v[160:163],a[144:147],v[32:35]\nxL v232,s[56:59],s39 offen lds\nxM v[32:35],v[164:167],a[160:163],v[32:35]\nxW lgkmcnt(2)\n"
"xM v[32:35],v[168:171],a[176:179],v[32:35]\nxM v[40:43],v[160:163],a[148:151],v[40:43]\nxM v[40:43],v[164:167],a[164:167],v[40:43]\nxW vmcnt(11)\ns_barrier\n"
"xM v[40:43],v[168:171],a[180:183],v[40:43]\nxT v[184:185],v210 offset:24576\nxT v[186:187],v210 offset:26624\n"
"xM v[48:51],v[160:163],a[152:155],v[48:51]\nxA m0,s32,25600\nxT v[188:189],v210 offset:28672\nxT v[190:191],v210 offset:30720\n"
"xM v[48:51],v[164:167],a[168:171],v[48:51]\ns_mov_b32 s39, s60\nxS v[136:139],v221,s[96:99],s46 offen sc1\n"
"xM v[48:51],v[168:171],a[184:187],v[48:51]\nxL v232,s[56:59],s39 offen lds\nxM v[56:59],v[160:163],a[156:159],v[56:59]\nxT v[192:193],v210 offset:32768\nxT v[194:195],v210 offset:34816\n"
"xM v[56:59],v[164:167],a[172:175],v[56:59]\nxT v[196:197],v211 offset:24576\nxM v[56:59],v[168:171],a[188:191],v[56:59]\n"
"xM v[36:39],v[172:175],a[144:147],v[36:39]\nxT v[198:199],v211 offset:26624\nxM v[36:39],v[176:179],a[160:163],v[36:39]\nxW lgkmcnt(6)\n"
"xM v[36:39],v[180:183],a[176:179],v[36:39]\nxA m0,s32,26624\nxT v[160:161],v211 offset:28672\n"
"xM v[44:47],v[172:175],a[148:151],v[44:47]\ns_mov_b32 s39, s61\nxM v[44:47],v[176:179],a[164:167],v[44:47]\nxL v232,s[56:59],s39 offen lds\n"
"xM v[44:47],v[180:183],a[180:183],v[44:47]\nxT v[162:163],v211 offset:30720\nxM v[52:55],v[172:175],a[152:155],v[52:55]\nxT v[164:165],v211 offset:32768\n"
"xM v[52:55],v[176:179],a[168:171],v[52:55]\nxM v[52:55],v[180:183],a[184:187],v[52:55]\nxT v[166:167],v211 offset:34816\n"
"xM v[60:63],v[172:175],a[156:159],v[60:63]\nxM v[60:63],v[176:179],a[172:175],v[60:63]\nxT v[168:169],v212 offset:24576\n"
"xM v[60:63],v[180:183],a[188:191],v[60:63]\nxS v[144:147],v222,s[96:99],s46 offen sc1\nxM v[32:35],v[184:187],a[192:195],v[32:35]\nxW lgkmcnt(4)\nxT v[170:171],v212 offset:26624\n"
"xM v[32:35],v[188:191],a[208:211],v[32:35]\nxM v[32:35],v[192:195],a[224:227],v[32:35]\nxT v[172:173],v212 offset:28672\n"
"xM v[40:43],v[184:187],a[196:199],v[40:43]\nxM v[40:43],v[188:191],a[212:215],v[40:43]\nxT v[174:175],v212 offset:30720\n"
"xM v[40:43],v[192:195],a[228:231],v[40:43]\nxM v[48:51],v[184:187],a[200:203],v[48:51]\nxT v[176:177],v212 offset:32768\n"
"xM v[48:51],v[188:191],a[216:219],v[48:51]\nxM v[48:51],v[192:195],a[232:235],v[48:51]\nxT v[178:179],v212 offset:34816\n"
"xM v[56:59],v[184:187],a[204:207],v[56:59]\nxM v[56:59],v[188:191],a[220:223],v[56:59]\nxT v[180:181],v213 offset:24576\n"
"xM v[56:59],v[192:195],a[236:239],v[56:59]\nxM v[36:39],v[196:199],a[192:195],v[36:39]\nxW lgkmcnt(3)\nxT v[182:183],v213 offset:26624\n"
"xM v[36:39],v[160:163],a[208:211],v[36:39]\nxM v[36:39],v[164:167],a[224:227],v[36:39]\nxT v[184:185],v213 offset:28672\n"
"xM v[44:47],v[196:199],a[196:199],v[44:47]\nxS v[152:155],v223,s[96:99],s46 offen sc1\nxM v[44:47],v[160:163],a[212:215],v[44:47]\nxA s46,s46,1024\nxT v[186:187],v213 offset:30720\n"
"xM v[44:47],v[164:167],a[228:231],v[44:47]\nxM v[52:55],v[196:199],a[200:203],v[52:55]\nxT v[188:189],v213 offset:32768\n"
"xM v[52:55],v[160:163],a[216:219],v[52:55]\nxM v[52:55],v[164:167],a[232:235],v[52:55]\nxT v[190:191],v213 offset:34816\n"
"xM v[60:63],v[196:199],a[204:207],v[60:63]\nxM v[60:63],v[160:163],a[220:223],v[60:63]\nxM v[60:63],v[164:167],a[236:239],v[60:63]\n"
"xM v[32:35],v[168:171],a[240:243],v[32:35]\nxM v[32:35],v[172:175],v[0:3],v[32:35]\nxW lgkmcnt(2)\n"
"xM v[32:35],v[176:179],v[16:19],v[32:35]\nxM v[40:43],v[168:171],a[244:247],v[40:43]\nxM v[40:43],v[172:175],v[4:7],v[40:43]\nxW vmcnt(6)\ns_barrier\n"
"xM v[40:43],v[176:179],v[20:23],v[40:43]\nxT v[192:193],v214 offset:0\nxT v[194:195],v214 offset:1024\n"
"xM v[48:51],v[168:171],a[248:251],v[48:51]\nxT v[196:197],v214 offset:2048\nxT v[198:199],v214 offset:3072\n"
"xM v[48:51],v[172:175],v[8:11],v[48:51]\nxT v[160:161],v214 offset:4096\nxM v[48:51],v[176:179],v[24:27],v[48:51]\nxT v[162:163],v214 offset:5120\n"
"xM v[56:59],v[168:171],a[252:255],v[56:59]\nxM v[56:59],v[172:175],v[12:15],v[56:59]\nxT v[164:165],v214 offset:6144\n"
"xM v[56:59],v[176:179],v[28:31],v[56:59]\nxM v[36:39],v[180:183],a[240:243],v[36:39]\nxT v[166:167],v214 offset:7168\n"
"xM v[36:39],v[184:187],v[0:3],v[36:39]\nxW lgkmcnt(6)\nxM v[36:39],v[188:191],v[16:19],v[36:39]\nxT v[168:169],v214 offset:8192\n"
"xM v[44:47],v[180:183],a[244:247],v[44:47]\nxM v[44:47],v[184:187],v[4:7],v[44:47]\nxT v[170:171],v214 offset:9216\n"
"xM v[44:47],v[188:191],v[20:23],v[44:47]\nTC1_1_3_%=:\ns_cbranch_execz TDONE1_%=\n"
"xM v[52:55],v[180:183],a[248:251],v[52:55]\nxT v[172:173],v214 offset:10240\nxM v[52:55],v[184:187],v[8:11],v[52:55]\n"
"xM v[52:55],v[188:191],v[24:27],v[52:55]\nxT v[174:175],v214 offset:11264\nxM v[60:63],v[180:183],a[252:255],v[60:63]\n"
"xM v[60:63],v[184:187],v[12:15],v[60:63]\nxT v[176:177],v214 offset:12288\nxM v[60:63],v[188:191],v[28:31],v[60:63]\n"
"xM v[64:67],v[192:195],a[0:3],0\nxW lgkmcnt(3)\nxT v[178:179],v214 offset:13312\n"
"xM v[64:67],v[196:199],a[16:19],v[64:67]\nxM v[64:67],v[160:163],a[32:35],v[64:67]\nxT v[180:181],v214 offset:14336\n"
"xM v[68:71],v[192:195],a[4:7],0\nxC v32,v32,v33\nxC v33,v34,v35\n"
"xM v[68:71],v[196:199],a[20:23],v[68:71]\nxC v34,v36,v37\nxC v35,v38,v39\n"
"xM v[68:71],v[160:163],a[36:39],v[68:71]\nxC v40,v40,v41\nxC v41,v42,v43\n"
"xM v[72:75],v[192:195],a[8:11],0\nxC v42,v44,v45\nxC v43,v46,v47\n"
"xM v[72:75],v[196:199],a[24:27],v[72:75]\nxS v[32:35],v220,s[96:99],s46 offen sc1\nxM v[72:75],v[160:163],a[40:43],v[72:75]\nxC v48,v48,v49\nxC v49,v50,v51\n"
"xM v[76:79],v[192:195],a[12:15],0\nxC v50,v52,v53\nxC v51,v54,v55\n"
"xM v[76:79],v[196:199],a[28:31],v[76:79]\nxC v56,v56,v57\nxC v57,v58,v59\n"
"xM v[76:79],v[160:163],a[44:47],v[76:79]\nxC v58,v60,v61\nxC v59,v62,v63\n"
"xM v[64:67],v[164:167],a[144:147],v[64:67]\nxT v[182:183],v214 offset:15360\nxT v[184:185],v214 offset:16384\n"
"xM v[64:67],v[168:171],a[160:163],v[64:67]\nxW lgkmcnt(2)\nxT v[186:187],v214 offset:17408\nxT v[188:189],v214 offset:18432\n"
"xM v[64:67],v[172:175],a[176:179],v[64:67]\nxT v[190:191],v214 offset:19456\nxT v[192:193],v214 offset:20480\n"
"xM v[68:71],v[164:167],a[148:151],v[68:71]\nxM v[68:71],v[168:171],a[164:167],v[68:71]\nxT v[194:195],v214 offset:21504\n"
"xM v[68:71],v[172:175],a[180:183],v[68:71]\nxM v[72:75],v[164:167],a[152:155],v[72:75]\nxT v[196:197],v214 offset:22528\n"
"xM v[72:75],v[168:171],a[168:171],v[72:75]\nxM v[72:75],v[172:175],a[184:187],v[72:75]\nxT v[198:199],v214 offset:23552\n"
"xM v[76:79],v[164:167],a[156:159],v[76:79]\nxM v[76:79],v[168:171],a[172:175],v[76:79]\nxM v[76:79],v[172:175],a[188:191],v[76:79]\nxS v[40:43],v221,s[96:99],s46 offen sc1\n"
"xM v[64:67],v[176:179],a[48:51],v[64:67]\nxW lgkmcnt(2)\nxM v[64:67],v[180:183],a[64:67],v[64:67]\n"
"xM v[64:67],v[184:187],a[80:83],v[64:67]\nxM v[68:71],v[176:179],a[52:55],v[68:71]\nxM v[68:71],v[180:183],a[68:71],v[68:71]\nxW vmcnt(4)\ns_barrier\n"
"xM v[68:71],v[184:187],a[84:87],v[68:71]\nxT v[160:161],v214 offset:24576\nxT v[162:163],v214 offset:25600\n"
"xM v[72:75],v[176:179],a[56:59],v[72:75]\nxT v[164:165],v214 offset:26624\nxT v[166:167],v214 offset:27648\n"
"xM v[72:75],v[180:183],a[72:75],v[72:75]\nxT v[168:169],v214 offset:28672\nxM v[72:75],v[184:187],a[88:91],v[72:75]\nxT v[170:171],v214 offset:29696\n"
"xM v[76:79],v[176:179],a[60:63],v[76:79]\nxM v[76:79],v[180:183],a[76:79],v[76:79]\nxT v[172:173],v214 offset:30720\n"
"xM v[76:79],v[184:187],a[92:95],v[76:79]\nxM v[64:67],v[188:191],a[192:195],v[64:67]\nxW lgkmcnt(7)\nxT v[174:175],v214 offset:31744\n"
"xM v[64:67],v[192:195],a[208:211],v[64:67]\nxW vmcnt(2)\ns_mov_b64 exec, 1\nbuffer_store_dwordx2 v[242:243], off, s[96:99], s50 sc1\ns_mov_b64 exec, -1\n"
"xM v[64:67],v[196:199],a[224:227],v[64:67]\nxA s50,s50,256\nxT v[176:177],v214 offset:32768\n"
"xM v[68:71],v[188:191],a[196:199],v[68:71]\nxS v[48:51],v222,s[96:99],s46 offen sc1\nxM v[68:71],v[192:195],a[212:215],v[68:71]\nxT v[178:179],v214 offset:33792\n"
"xM v[68:71],v[196:199],a[228:231],v[68:71]\nxM v[72:75],v[188:191],a[200:203],v[72:75]\nxT v[180:181],v214 offset:34816\n"
"xM v[72:75],v[192:195],a[216:219],v[72:75]\nxM v[72:75],v[196:199],a[232:235],v[72:75]\nxT v[182:183],v214 offset:35840\n"
"xM v[76:79],v[188:191],a[204:207],v[76:79]\nxM v[76:79],v[192:195],a[220:223],v[76:79]\nxM v[76:79],v[196:199],a[236:239],v[76:79]\nxW lgkmcnt(3)\n"
"xM v[64:67],v[160:163],a[96:99],v[64:67]\nxM v[64:67],v[164:167],a[112:115],v[64:67]\nxM v[64:67],v[168:171],a[128:131],v[64:67]\n"
"xM v[68:71],v[160:163],a[100:103],v[68:71]\nxM v[68:71],v[164:167],a[116:119],v[68:71]\nxM v[68:71],v[168:171],a[132:135],v[68:71]\n"
"xM v[72:75],v[160:163],a[104:107],v[72:75]\nxM v[72:75],v[164:167],a[120:123],v[72:75]\nxS v[56:59],v223,s[96:99],s46 offen sc1\n"
"xM v[72:75],v[168:171],a[136:139],v[72:75]\nxA s46,s46,1024\nxM v[76:79],v[160:163],a[108:111],v[76:79]\ns_sub_u32 s46, s46, s43\n"
"xM v[76:79],v[164:167],a[124:127],v[76:79]\nxM v[76:79],v[168:171],a[140:143],v[76:79]\nxM v[64:67],v[172:175],a[240:243],v[64:67]\nxW lgkmcnt(0)\n"
"xM v[64:67],v[176:179],v[0:3],v[64:67]\nxM v[64:67],v[180:183],v[16:19],v[64:67]\nxM v[68:71],v[172:175],a[244:247],v[68:71]\n"
"xM v[68:71],v[176:179],v[4:7],v[68:71]\nxM v[68:71],v[180:183],v[20:23],v[68:71]\nxM v[72:75],v[172:175],a[248:251],v[72:75]\n"
"xM v[72:75],v[176:179],v[8:11],v[72:75]\nxM v[72:75],v[180:183],v[24:27],v[72:75]\nxM v[76:79],v[172:175],a[252:255],v[76:79]\n"
"xM v[76:79],v[176:179],v[12:15],v[76:79]\nxM v[76:79],v[180:183],v[28:31],v[76:79]\ns_nop 7\nxC v64,v64,v65\nxC v65,v66,v67\nxC v66,v68,v69\n"
"xC v67,v70,v71\ns_nop 0\nv_permlane16_swap_b32 v64, v66\ns_nop 0\nv_permlane16_swap_b32 v65, v67\nxS v[64:67],v235,s[96:99],s46 offen sc1\n"
"xA s46,s46,294912\nxC v72,v72,v73\nxC v73,v74,v75\nxC v74,v76,v77\nxC v75,v78,v79\ns_nop 0\n"
"v_permlane16_swap_b32 v72, v74\ns_nop 0\nv_permlane16_swap_b32 v73, v75\nxS v[72:75],v235,s[96:99],s46 offen sc1\nxA s46,s46,294912\ns_mov_b32 exec_hi, 0\n"
"buffer_load_dwordx2 v[236:237], v238, s[96:99], s51 offen sc1\ns_mov_b64 exec, -1\nv_add_u32 v230, 160256, v241\nxR v[160:163],v230 offset:0\nxR v[164:167],v230 offset:512\nxR v[168:171],v230 offset:1024\n"
"xR v[172:175],v230 offset:1536\nxR v[176:179],v230 offset:2048\nv_lshlrev_b32 v96, 16, v244\nv_and_b32 v97, 0xffff0000, v244\nv_lshlrev_b32 v98, 16, v245\nv_and_b32 v99, 0xffff0000, v245\n"
"v_lshlrev_b32 v100, 16, v246\nv_and_b32 v101, 0xffff0000, v246\nv_lshlrev_b32 v102, 16, v247\nv_and_b32 v103, 0xffff0000, v247\nv_lshlrev_b32 v104, 16, v248\nv_and_b32 v105, 0xffff0000, v248\n"
"v_lshlrev_b32 v106, 16, v249\nv_and_b32 v107, 0xffff0000, v249\nv_lshlrev_b32 v108, 16, v250\nv_and_b32 v109, 0xffff0000, v250\nv_lshlrev_b32 v110, 16, v251\nv_and_b32 v111, 0xffff0000, v251\n"
"v_lshlrev_b32 v112, 16, v252\nv_and_b32 v113, 0xffff0000, v252\nv_lshlrev_b32 v114, 16, v253\nv_and_b32 v115, 0xffff0000, v253\nxW vmcnt(1)\ns_mov_b64 exec, 1\n"
"buffer_store_dwordx2 v[242:243], off, s[96:99], s50 sc1\ns_mov_b64 exec, -1\nxW vmcnt(1)\nv_cmp_ne_u64 vcc, s[68:69], v[236:237]\ns_and_b64 vcc, vcc, s[100:101]\ns_and_b32 s39, vcc_lo, 0xffffffff\n"
"s_and_b32 s39, s39, s54\ns_cbranch_scc1 SPINB1_%=\nRETB1_%=:\ns_barrier\nxA s38,s49,0\nxL a[0:3],v239,s[96:99],s38 offen sc1\n"
"xA s38,s49,36864\nxL a[4:7],v239,s[96:99],s38 offen sc1\nxA s38,s49,1024\nxL a[8:11],v239,s[96:99],s38 offen sc1\nxA s38,s49,37888\nxL a[12:15],v239,s[96:99],s38 offen sc1\n"
"xA s38,s49,2048\nxL a[16:19],v239,s[96:99],s38 offen sc1\nxA s38,s49,38912\nxL a[20:23],v239,s[96:99],s38 offen sc1\ns_mov_b32 exec_lo, 0\nbuffer_load_dwordx2 v[236:237], v238, s[96:99], s51 offen sc1\n"
"s_mov_b64 exec, -1\nxA s38,s49,5120\nxL a[40:43],v239,s[96:99],s38 offen sc1\nxA s38,s49,41984\nxL a[44:47],v239,s[96:99],s38 offen sc1\nxA s38,s49,6144\n"
"xL a[48:51],v239,s[96:99],s38 offen sc1\nxA s38,s49,43008\nxL a[52:55],v239,s[96:99],s38 offen sc1\nxA s38,s49,7168\nxL a[56:59],v239,s[96:99],s38 offen sc1\nxA s38,s49,44032\n"
"xL a[60:63],v239,s[96:99],s38 offen sc1\nxW vmcnt(6)\nv_cmp_ne_u64 vcc, s[68:69], v[236:237]\ns_and_b64 vcc, vcc, s[100:101]\ns_and_b32 s39, vcc_hi, 0xffffffff\ns_and_b32 s39, s39, s54\n"
"s_cbranch_scc1 SPINB2_%=\nRETB2_%=:\ns_barrier\nxA s38,s49,3072\nxL a[24:27],v239,s[96:99],s38 offen sc1\nxA s38,s49,39936\n"
"xL a[28:31],v239,s[96:99],s38 offen sc1\nxA s38,s49,8192\nxL a[64:67],v239,s[96:99],s38 offen sc1\nxA s38,s49,45056\nxL a[68:71],v239,s[96:99],s38 offen sc1\nxA s38,s49,4096\n"
"xL a[32:35],v239,s[96:99],s38 offen sc1\nxA s38,s49,40960\nxL a[36:39],v239,s[96:99],s38 offen sc1\nxW vmcnt(6)\n"
"xM v[128:131],v[200:203],a[0:3],0\nxM v[128:131],v[204:207],a[8:11],v[128:131]\nxM v[132:135],v[200:203],a[16:19],0\n"
"xM v[136:139],v[204:207],a[40:43],0\nxM v[140:143],v[200:203],a[48:51],0\nxM v[140:143],v[204:207],a[56:59],v[140:143]\n"
"xM v[128:131],v[200:203],a[4:7],v[128:131]\nxM v[128:131],v[204:207],a[12:15],v[128:131]\nxM v[132:135],v[200:203],a[20:23],v[132:135]\n"
"xM v[136:139],v[204:207],a[44:47],v[136:139]\nxM v[140:143],v[200:203],a[52:55],v[140:143]\nxM v[140:143],v[204:207],a[60:63],v[140:143]\nxW vmcnt(0)\n"
"xM v[132:135],v[204:207],a[24:27],v[132:135]\nxM v[144:147],v[200:203],a[64:67],0\nxM v[136:139],v[200:203],a[32:35],v[136:139]\n"
"xM v[132:135],v[204:207],a[28:31],v[132:135]\nxM v[144:147],v[200:203],a[68:71],v[144:147]\nxM v[136:139],v[200:203],a[36:39],v[136:139]\ns_nop 7\nxW lgkmcnt(4)\nv_pk_fma_f32 v[128:129], v[160:161], v[128:129], v[96:97]\n"
"v_pk_fma_f32 v[130:131], v[162:163], v[130:131], v[98:99]\nxC v96,v128,v129\nxC v97,v130,v131\nds_write_b64 v240, v[96:97] offset:0\nxW lgkmcnt(4)\nv_pk_fma_f32 v[132:133], v[164:165], v[132:133], v[100:101]\n"
"v_pk_fma_f32 v[134:135], v[166:167], v[134:135], v[102:103]\nxC v100,v132,v133\nxC v101,v134,v135\nds_write_b64 v240, v[100:101] offset:256\nxW lgkmcnt(4)\nv_pk_fma_f32 v[136:137], v[168:169], v[136:137], v[104:105]\n"
"v_pk_fma_f32 v[138:139], v[170:171], v[138:139], v[106:107]\nxC v104,v136,v137\nxC v105,v138,v139\nds_write_b64 v240, v[104:105] offset:512\nxW lgkmcnt(4)\nv_pk_fma_f32 v[140:141], v[172:173], v[140:141], v[108:109]\n"
"v_pk_fma_f32 v[142:143], v[174:175], v[142:143], v[110:111]\nxC v108,v140,v141\nxC v109,v142,v143\nds_write_b64 v240, v[108:109] offset:768\nxW lgkmcnt(4)\nv_pk_fma_f32 v[144:145], v[176:177], v[144:145], v[112:113]\n"
"v_pk_fma_f32 v[146:147], v[178:179], v[146:147], v[114:115]\nxC v112,v144,v145\nxC v113,v146,v147\ns_mov_b32 exec_hi, 0\nds_write_b64 v240, v[112:113] offset:1024\ns_mov_b32 exec_hi, -1\n"
"v_add_u32 v224, s45, v224\nxW lgkmcnt(0)\ns_barrier\nxR v[128:131],v224 offset:0\nxR v[132:135],v224 offset:4096\nxR v[136:139],v224 offset:8192\n"
"s_mov_b32 s38, 0\nxW lgkmcnt(2)\nxS v[128:131],v224,s[12:15],s38 offen sc1\ns_mov_b32 s38, 4096\nxW lgkmcnt(1)\nxS v[132:135],v224,s[12:15],s38 offen sc1\n"
"s_mov_b32 s38, 8192\nxW lgkmcnt(0)\nxS v[136:139],v224,s[12:15],s38 offen sc1\nKEND_%=:\ns_endpgm\nREDO_%=:\n"
"xW vmcnt(0) lgkmcnt(0)\ns_barrier\ns_mov_b32 s41, 0x0\nv_lshrrev_b32 v237, 4, v255\nv_and_b32 v238, 15, v255\nv_bfe_u32 v239, v255, 2, 2\n"
"v_and_b32 v240, 3, v255\nv_bfe_u32 v241, v255, 2, 3\ns_lshl_b32 s36, %[wid], 12\ns_lshl_b32 s39, s71, 19\nxA s12,s74,s39\ns_addc_u32 s13, s75, 0\n"
"s_and_b32 s13, s13, 0xffff\ns_mov_b32 s14, 524288\ns_mov_b32 s15, 0x20000\nxA s16,s76,s39\ns_addc_u32 s17, s77, 0\ns_and_b32 s17, s17, 0xffff\n"
"s_mov_b32 s18, 524288\ns_mov_b32 s19, 0x20000\ns_mul_i32 s39, s71, 294912\nxA s24,s80,s39\ns_addc_u32 s25, s81, 0\ns_and_b32 s25, s25, 0xffff\n"
"s_mov_b32 s26, 294912\ns_mov_b32 s27, 0x20000\ns_lshl_b32 s46, s70, 2\nxA s46,s46,%[wid]\ns_mul_i32 s46, s46, 589824\ns_mul_i32 s38, s71, 9216\n"
"xA s46,s46,s38\nxA s46,s46,7864320\nv_add_u32 v242, 0, v237\nv_xor_b32 v242, v242, v238\nv_lshlrev_b32 v242, 4, v242\nv_lshl_add_u32 v224, v238, 8, v242\n"
"v_lshl_add_u32 v230, v237, 8, v242\nv_add_u32 v242, 4, v237\nv_xor_b32 v242, v242, v238\nv_lshlrev_b32 v242, 4, v242\nv_lshl_add_u32 v225, v238, 8, v242\nv_lshl_add_u32 v231, v237, 8, v242\n"
"v_add_u32 v242, 8, v237\nv_xor_b32 v242, v242, v238\nv_lshlrev_b32 v242, 4, v242\nv_lshl_add_u32 v226, v238, 8, v242\nv_lshl_add_u32 v232, v237, 8, v242\nv_add_u32 v242, 12, v237\n"
"v_xor_b32 v242, v242, v238\nv_lshlrev_b32 v242, 4, v242\nv_lshl_add_u32 v227, v238, 8, v242\nv_lshl_add_u32 v233, v237, 8, v242\nv_and_b32 v242, 1, v237\nv_lshrrev_b32 v243, 1, v237\n"
"v_lshl_add_u32 v244, v242, 2, v239\nv_lshlrev_b32 v244, 6, v244\nv_lshl_add_u32 v244, v240, 3, v244\nv_lshl_add_u32 v244, v242, 5, v244\nv_lshl_add_u32 v228, v243, 11, v244\nv_add_u32 v228, 32768, v228\n"
"v_xor_b32 v229, 32, v228\nv_lshl_add_u32 v246, v243, 2, v239\nv_lshlrev_b32 v246, 6, v246\nv_lshl_add_u32 v246, v240, 4, v246\nv_mul_u32_u24 v242, 49160, v242\ns_mov_b32 s39, 49152\n"
"v_add3_u32 v246, v246, v242, s39\nv_lshrrev_b32 v242, 2, v241\nv_lshlrev_b32 v242, 1, v242\nv_xor_b32 v242, v242, v240\nv_lshlrev_b32 v242, 4, v242\nv_lshrrev_b32 v243, 5, v255\n"
"v_lshl_add_u32 v242, v243, 6, v242\nv_lshl_add_u32 v234, v241, 8, v242\nv_mul_u32_u24 v248, 1152, v241\nv_add_u32 v248, v248, v242\nv_lshrrev_b32 v242, 3, v238\nv_mul_u32_u24 v242, 147456, v242\n"
"v_lshl_add_u32 v242, v237, 7, v242\nv_and_b32 v243, 7, v255\nv_lshl_add_u32 v249, v243, 4, v242\nv_add_u32 v250, 294912, v249\nv_lshlrev_b32 v245, 5, v237\nv_add_u32 v245, 148480, v245\n"
"v_mov_b32 v242, 0x3f803f80\nxP a192,v242\nxP a193,v242\nxP a194,v242\nxP a195,v242\nv_and_b32 v242, 1, v255\n"
"v_lshlrev_b32 v242, 4, v242\nv_mov_b32 v243, 0x3f80\nv_lshlrev_b32 v242, v242, v243\nv_lshrrev_b32 v243, 1, v238\nv_cmp_eq_u32 vcc, 0, v243\ns_nop 1\n"
"v_cndmask_b32 v244, 0, v242, vcc\nxP a196,v244\nv_cmp_eq_u32 vcc, 1, v243\ns_nop 1\nv_cndmask_b32 v244, 0, v242, vcc\nxP a197,v244\n"
"v_cmp_eq_u32 vcc, 2, v243\ns_nop 1\nv_cndmask_b32 v244, 0, v242, vcc\nxP a198,v244\nv_cmp_eq_u32 vcc, 3, v243\ns_nop 1\n"
"v_cndmask_b32 v244, 0, v242, vcc\nxP a199,v244\nv_cmp_eq_u32 vcc, 4, v243\ns_nop 1\nv_cndmask_b32 v244, 0, v242, vcc\nxP a200,v244\n"
"v_cmp_eq_u32 vcc, 5, v243\ns_nop 1\nv_cndmask_b32 v244, 0, v242, vcc\nxP a201,v244\nv_cmp_eq_u32 vcc, 6, v243\ns_nop 1\n"
"v_cndmask_b32 v244, 0, v242, vcc\nxP a202,v244\nv_cmp_eq_u32 vcc, 7, v243\ns_nop 1\nv_cndmask_b32 v244, 0, v242, vcc\nxP a203,v244\n"
"s_mov_b32 s32, s36\ns_mov_b32 s33, s36\nxA s34,s36,128\ns_lshl_b32 s44, %[wid], 13\ns_mul_i32 s45, %[wid], 73728\ns_mov_b32 s56, s80\n"
"s_mov_b32 s57, s81\ns_cmp_eq_u32 %[wid], 1\ns_cselect_b32 s56, s82, s56\ns_cselect_b32 s57, s83, s57\ns_cmp_eq_u32 %[wid], 2\ns_cselect_b32 s56, s84, s56\n"
"s_cselect_b32 s57, s85, s57\ns_cmp_eq_u32 %[wid], 3\ns_cselect_b32 s56, s86, s56\ns_cselect_b32 s57, s87, s57\ns_and_b32 s57, s57, 0xffff\ns_cmp_eq_u32 %[wid], 0\n"
"s_mov_b32 s58, 5308416\ns_cselect_b32 s58, 4718592, s58\ns_mov_b32 s59, 0x20000\ns_mul_i32 s48, %[bid], 21760\ns_mov_b32 s46, 2176\nv_lshlrev_b32 v253, 7, v255\n"
"s_lshl_b32 s39, s70, 1\ns_and_b32 s38, s71, 1\nxA s39,s39,s38\ns_lshr_b32 s38, s71, 1\ns_sub_u32 s37, %[wid], 1\ns_cmp_lt_u32 s37, 2\n"
"s_cbranch_scc1 LPA2_%=\ns_lshl_b32 s38, s38, 5\nxA s39,s39,s38\ns_cmp_eq_u32 %[wid], 0\ns_cselect_b32 s38, 16, 18\ns_mul_i32 s39, s39, s38\n"
"s_mul_i32 s48, s39, 1152\ns_mov_b32 s46, 2304\ns_branch LPB2_%=\nLPA2_%=:\ns_mul_i32 s39, s39, 165888\ns_mul_i32 s38, s38, 1152\n"
"xA s48,s39,s38\ns_mov_b32 s46, 18432\nv_cmp_lt_u32 vcc, 8, v255\nv_add_u32 v244, 8064, v253\ns_nop 0\nv_cndmask_b32 v253, v253, v244, vcc\n"
"LPB2_%=:\nv_mov_b32 v244, 0x40000000\nv_cmp_gt_u32 vcc, 18, v255\ns_nop 1\nv_cndmask_b32 v253, v244, v253, vcc\ns_lshr_b32 s39, s71, 1\n"
"s_mul_i32 s39, s39, 589824\nxA s24,s80,s39\ns_addc_u32 s25, s81, 0\ns_and_b32 s25, s25, 0xffff\ns_mov_b32 s26, 590720\ns_mov_b32 s27, 0x20000\n"
"s_sub_u32 s24, s24, 896\ns_subb_u32 s25, s25, 0\ns_lshl_b32 s44, %[wid], 12\ns_and_b32 s39, s71, 1\ns_mul_i32 s42, s39, 294912\ns_mul_i32 s39, %[wid], 18432\n"
"xA s42,s42,s39\nxA m0,s36,0\ns_nop 0\nxL v230,s[12:15],s32 offen offset:0 lds\nxL v231,s[12:15],s32 offen offset:1024 lds\nxL v232,s[12:15],s32 offen offset:2048 lds\n"
"xL v233,s[12:15],s32 offen offset:3072 lds\nxA s32,s32,16384\nxA m0,s36,16384\ns_nop 0\nxL v230,s[12:15],s32 offen offset:0 lds\nxL v231,s[12:15],s32 offen offset:1024 lds\n"
"xL v232,s[12:15],s32 offen offset:2048 lds\nxL v233,s[12:15],s32 offen offset:3072 lds\nxA s32,s32,16384\nxA m0,s36,32768\ns_nop 0\nxL v234,s[16:19],s33 offen offset:0 lds\n"
"xL v234,s[16:19],s33 offen offset:2048 lds\nxA s33,s33,16384\nxA m0,s36,33792\ns_nop 0\nxL v234,s[16:19],s34 offen offset:0 lds\nxL v234,s[16:19],s34 offen offset:2048 lds\n"
"xA s34,s34,16384\nxA m0,s36,65536\ns_nop 0\nxL v234,s[16:19],s33 offen offset:0 lds\nxL v234,s[16:19],s33 offen offset:2048 lds\nxA s33,s33,16384\n"
"xA m0,s36,66560\ns_nop 0\nxL v234,s[16:19],s34 offen offset:0 lds\nxL v234,s[16:19],s34 offen offset:2048 lds\nxA s34,s34,16384\nxA m0,s36,49152\n"
"s_nop 0\nxL v230,s[12:15],s32 offen offset:0 lds\nxL v231,s[12:15],s32 offen offset:1024 lds\nxL v232,s[12:15],s32 offen offset:2048 lds\nxL v233,s[12:15],s32 offen offset:3072 lds\nxA s32,s32,16384\n"
"xW vmcnt(16)\ns_barrier\nxR a[64:67],v224 offset:0\nxR a[68:71],v225 offset:0\nxR a[72:75],v226 offset:0\nxR a[76:79],v227 offset:0\n"
"xR a[80:83],v224 offset:4096\nxR a[84:87],v225 offset:4096\nxR a[88:91],v226 offset:4096\nxR a[92:95],v227 offset:4096\nxR a[96:99],v224 offset:8192\nxR a[100:103],v225 offset:8192\n"
"xR a[104:107],v226 offset:8192\nxR a[108:111],v227 offset:8192\nxR a[112:115],v224 offset:12288\nxR a[116:119],v225 offset:12288\nxR a[120:123],v226 offset:12288\nxR a[124:127],v227 offset:12288\n"
"v_mov_b32 v64, 0\nv_mov_b32 v65, 0\nv_mov_b32 v66, 0\nv_mov_b32 v67, 0\nv_mov_b32 v68, 0\nv_mov_b32 v69, 0\n"
"v_mov_b32 v70, 0\nv_mov_b32 v71, 0\nv_mov_b32 v72, 0\nv_mov_b32 v73, 0\nv_mov_b32 v74, 0\nv_mov_b32 v75, 0\n"
"v_mov_b32 v76, 0\nv_mov_b32 v77, 0\nv_mov_b32 v78, 0\nv_mov_b32 v79, 0\nv_mov_b32 v80, 0\nv_mov_b32 v81, 0\n"
"v_mov_b32 v82, 0\nv_mov_b32 v83, 0\nv_mov_b32 v84, 0\nv_mov_b32 v85, 0\nv_mov_b32 v86, 0\nv_mov_b32 v87, 0\n"
"v_mov_b32 v88, 0\nv_mov_b32 v89, 0\nv_mov_b32 v90, 0\nv_mov_b32 v91, 0\nv_mov_b32 v92, 0\nv_mov_b32 v93, 0\n"
"v_mov_b32 v94, 0\nv_mov_b32 v95, 0\nv_mov_b32 v96, 0\nv_mov_b32 v97, 0\nv_mov_b32 v98, 0\nv_mov_b32 v99, 0\n"
"v_mov_b32 v100, 0\nv_mov_b32 v101, 0\nv_mov_b32 v102, 0\nv_mov_b32 v103, 0\nv_mov_b32 v104, 0\nv_mov_b32 v105, 0\n"
"v_mov_b32 v106, 0\nv_mov_b32 v107, 0\nv_mov_b32 v108, 0\nv_mov_b32 v109, 0\nv_mov_b32 v110, 0\nv_mov_b32 v111, 0\n"
"v_mov_b32 v112, 0\nv_mov_b32 v113, 0\nv_mov_b32 v114, 0\nv_mov_b32 v115, 0\nv_mov_b32 v116, 0\nv_mov_b32 v117, 0\n"
"v_mov_b32 v118, 0\nv_mov_b32 v119, 0\nv_mov_b32 v120, 0\nv_mov_b32 v121, 0\nv_mov_b32 v122, 0\nv_mov_b32 v123, 0\n"
"v_mov_b32 v124, 0\nv_mov_b32 v125, 0\nv_mov_b32 v126, 0\nv_mov_b32 v127, 0\nv_mov_b32 v216, 0\nv_mov_b32 v217, 0\n"
"v_mov_b32 v218, 0\nv_mov_b32 v219, 0\nv_mov_b32 v220, 0\nv_mov_b32 v221, 0\nv_mov_b32 v222, 0\nv_mov_b32 v223, 0\n"
"xW lgkmcnt(15)\nxM v[128:131],a[64:67],a[0:3],0\nxM v[144:147],a[64:67],a[16:19],0\nxW lgkmcnt(14)\n"
"xM v[128:131],a[68:71],a[4:7],v[128:131]\nxM v[144:147],a[68:71],a[20:23],v[144:147]\nxW lgkmcnt(13)\n"
"xM v[128:131],a[72:75],a[8:11],v[128:131]\nxM v[144:147],a[72:75],a[24:27],v[144:147]\nxW lgkmcnt(12)\n"
"xM v[128:131],a[76:79],a[12:15],v[128:131]\nxM v[144:147],a[76:79],a[28:31],v[144:147]\nxW lgkmcnt(0)\n"
"xM v[132:135],a[80:83],a[0:3],0\nxM v[148:151],a[80:83],a[16:19],0\nxM v[132:135],a[84:87],a[4:7],v[132:135]\n"
"xM v[148:151],a[84:87],a[20:23],v[148:151]\nxM v[132:135],a[88:91],a[8:11],v[132:135]\nxM v[148:151],a[88:91],a[24:27],v[148:151]\n"
"xM v[132:135],a[92:95],a[12:15],v[132:135]\nxM v[148:151],a[92:95],a[28:31],v[148:151]\nxM v[136:139],a[96:99],a[0:3],0\n"
"xM v[152:155],a[96:99],a[16:19],0\nxM v[136:139],a[100:103],a[4:7],v[136:139]\nxM v[152:155],a[100:103],a[20:23],v[152:155]\n"
"xM v[136:139],a[104:107],a[8:11],v[136:139]\nxM v[152:155],a[104:107],a[24:27],v[152:155]\nxM v[136:139],a[108:111],a[12:15],v[136:139]\n"
"xM v[152:155],a[108:111],a[28:31],v[152:155]\nxM v[140:143],a[112:115],a[0:3],0\nxM v[156:159],a[112:115],a[16:19],0\n"
"xM v[140:143],a[116:119],a[4:7],v[140:143]\nxM v[156:159],a[116:119],a[20:23],v[156:159]\nxM v[140:143],a[120:123],a[8:11],v[140:143]\n"
"xM v[156:159],a[120:123],a[24:27],v[156:159]\nxM v[140:143],a[124:127],a[12:15],v[140:143]\nxM v[156:159],a[124:127],a[28:31],v[156:159]\nxW vmcnt(8)\ns_barrier\ns_nop 7\n"
"SFIRST0_%=:\nxX v237,v128,v129,v130\nxX v237,v131,v132,v237\nxX v237,v133,v134,v237\nxX v237,v135,v136,v237\nxX v237,v137,v138,v237\n"
"xX v237,v139,v140,v237\nxX v237,v141,v142,v237\nv_max_f32 v237, v143, v237\nv_mov_b32 v238, v237\ns_nop 1\nv_permlane32_swap_b32 v237, v238\n"
"v_max_f32 v237, v237, v238\nv_mov_b32 v238, v237\ns_nop 1\nv_permlane16_swap_b32 v237, v238\nv_max_f32 v237, v237, v238\nv_add_f32 v237, s41, v237\n"
"v_sub_f32 v128, v128, v237\nv_sub_f32 v129, v129, v237\nv_sub_f32 v130, v130, v237\nv_sub_f32 v131, v131, v237\nv_sub_f32 v132, v132, v237\nv_sub_f32 v133, v133, v237\n"
"v_sub_f32 v134, v134, v237\nv_sub_f32 v135, v135, v237\nv_sub_f32 v136, v136, v237\nv_sub_f32 v137, v137, v237\nv_sub_f32 v138, v138, v237\nv_sub_f32 v139, v139, v237\n"
"v_sub_f32 v140, v140, v237\nv_sub_f32 v141, v141, v237\nv_sub_f32 v142, v142, v237\nv_sub_f32 v143, v143, v237\nv_sub_f32 v192, 0, v237\nv_sub_f32 v193, 0, v237\n"
"v_sub_f32 v194, 0, v237\nv_sub_f32 v195, 0, v237\nxX v237,v144,v145,v146\nxX v237,v147,v148,v237\nxX v237,v149,v150,v237\nxX v237,v151,v152,v237\n"
"xX v237,v153,v154,v237\nxX v237,v155,v156,v237\nxX v237,v157,v158,v237\nv_max_f32 v237, v159, v237\nv_mov_b32 v238, v237\ns_nop 1\n"
"v_permlane32_swap_b32 v237, v238\nv_max_f32 v237, v237, v238\nv_mov_b32 v238, v237\ns_nop 1\nv_permlane16_swap_b32 v237, v238\nv_max_f32 v237, v237, v238\n"
"v_add_f32 v237, s41, v237\nv_sub_f32 v144, v144, v237\nv_sub_f32 v145, v145, v237\nv_sub_f32 v146, v146, v237\nv_sub_f32 v147, v147, v237\nv_sub_f32 v148, v148, v237\n"
"v_sub_f32 v149, v149, v237\nv_sub_f32 v150, v150, v237\nv_sub_f32 v151, v151, v237\nv_sub_f32 v152, v152, v237\nv_sub_f32 v153, v153, v237\nv_sub_f32 v154, v154, v237\n"
"v_sub_f32 v155, v155, v237\nv_sub_f32 v156, v156, v237\nv_sub_f32 v157, v157, v237\nv_sub_f32 v158, v158, v237\nv_sub_f32 v159, v159, v237\nv_sub_f32 v196, 0, v237\n"
"v_sub_f32 v197, 0, v237\nv_sub_f32 v198, 0, v237\nv_sub_f32 v199, 0, v237\nxA m0,s36,81920\ns_nop 0\nxL v234,s[16:19],s33 offen offset:0 lds\n"
"xE v128,v128\nxT a[128:129],v228 offset:0\nxT a[130:131],v228 offset:4096\nxE v129,v129\nxT a[132:133],v229 offset:0\nxC v128,v128,v129\n"
"xE v130,v130\nxT a[134:135],v229 offset:4096\nxT a[136:137],v228 offset:512\nxE v131,v131\nxT a[138:139],v228 offset:4608\nxC v129,v130,v131\n"
"xE v132,v132\nxT a[140:141],v229 offset:512\nxT a[142:143],v229 offset:4608\nxE v133,v133\nxT a[144:145],v228 offset:1024\nxC v130,v132,v133\n"
"xE v134,v134\nxL v234,s[16:19],s33 offen offset:2048 lds\nxA s33,s33,16384\nxE v135,v135\nxT a[146:147],v228 offset:5120\nxC v131,v134,v135\n"
"xE v144,v144\nxT a[148:149],v229 offset:1024\nxE v145,v145\nxT a[150:151],v229 offset:5120\nxC v144,v144,v145\nxE v146,v146\n"
"xT a[152:153],v228 offset:1536\nxE v147,v147\nxT a[154:155],v228 offset:5632\nxC v145,v146,v147\nxE v148,v148\nxT a[156:157],v229 offset:1536\n"
"xE v149,v149\nxM v[160:163],a[64:67],a[32:35],0\nxT a[158:159],v229 offset:5632\nxC v146,v148,v149\n"
"xM v[176:179],a[64:67],a[48:51],0\nxA m0,s36,82944\nxE v150,v150\n"
"xM v[160:163],a[68:71],a[36:39],v[160:163]\nxL v234,s[16:19],s34 offen offset:0 lds\nxM v[176:179],a[68:71],a[52:55],v[176:179]\nxE v151,v151\n"
"xM v[160:163],a[72:75],a[40:43],v[160:163]\nxT a[160:161],v228 offset:8192\nxC v147,v150,v151\n"
"xM v[176:179],a[72:75],a[56:59],v[176:179]\nxE v136,v136\nxM v[160:163],a[76:79],a[44:47],v[160:163]\nxT a[162:163],v228 offset:12288\n"
"xM v[176:179],a[76:79],a[60:63],v[176:179]\nxE v137,v137\nxM v[164:167],a[80:83],a[32:35],0\nxT a[164:165],v229 offset:8192\nxC v136,v136,v137\n"
"xM v[180:183],a[80:83],a[48:51],0\nxE v138,v138\nxM v[164:167],a[84:87],a[36:39],v[164:167]\nxT a[166:167],v229 offset:12288\n"
"xM v[180:183],a[84:87],a[52:55],v[180:183]\nxE v139,v139\nxM v[164:167],a[88:91],a[40:43],v[164:167]\n"
"xM v[180:183],a[88:91],a[56:59],v[180:183]\nxE v140,v140\nxM v[164:167],a[92:95],a[44:47],v[164:167]\n"
"xM v[180:183],a[92:95],a[60:63],v[180:183]\nxE v141,v141\nxM v[168:171],a[96:99],a[32:35],0\nxC v137,v138,v139\nxC v138,v140,v141\n"
"xM v[184:187],a[96:99],a[48:51],0\nxE v142,v142\nxM v[168:171],a[100:103],a[36:39],v[168:171]\nxL v234,s[16:19],s34 offen offset:2048 lds\n"
"xM v[184:187],a[100:103],a[52:55],v[184:187]\nxA s34,s34,16384\nxE v143,v143\n"
"xM v[168:171],a[104:107],a[40:43],v[168:171]\nxM v[184:187],a[104:107],a[56:59],v[184:187]\nxE v152,v152\n"
"xM v[168:171],a[108:111],a[44:47],v[168:171]\nxM v[184:187],a[108:111],a[60:63],v[184:187]\nxE v153,v153\n"
"xM v[172:175],a[112:115],a[32:35],0\nxC v139,v142,v143\nxC v152,v152,v153\n"
"xM v[188:191],a[112:115],a[48:51],0\nxE v154,v154\nxM v[172:175],a[116:119],a[36:39],v[172:175]\nxT a[168:169],v228 offset:8704\nxT a[170:171],v228 offset:12800\n"
"xM v[188:191],a[116:119],a[52:55],v[188:191]\nxE v155,v155\nxM v[172:175],a[120:123],a[40:43],v[172:175]\n"
"xM v[188:191],a[120:123],a[56:59],v[188:191]\nxE v156,v156\nxM v[172:175],a[124:127],a[44:47],v[172:175]\n"
"xM v[188:191],a[124:127],a[60:63],v[188:191]\nxW lgkmcnt(2)\nxE v157,v157\n"
"xM v[0:3],a[128:131],v[128:131],0\nxC v153,v154,v155\nxC v154,v156,v157\n"
"xM v[32:35],a[128:131],v[144:147],0\nxE v158,v158\nxM v[4:7],a[132:135],v[128:131],0\nxT a[172:173],v229 offset:8704\nxT a[174:175],v229 offset:12800\n"
"xM v[36:39],a[132:135],v[144:147],0\nxE v159,v159\ns_nop 7\nSFIRST1_%=:\nxX v237,v160,v161,v162\nxX v237,v163,v164,v237\n"
"xX v237,v165,v166,v237\nxX v237,v167,v168,v237\nxX v237,v169,v170,v237\nxX v237,v171,v172,v237\nxX v237,v173,v174,v237\nv_max_f32 v237, v175, v237\n"
"v_mov_b32 v238, v237\ns_nop 1\nv_permlane32_swap_b32 v237, v238\nv_max_f32 v237, v237, v238\nv_mov_b32 v238, v237\ns_nop 1\n"
"v_permlane16_swap_b32 v237, v238\nv_max_f32 v237, v237, v238\nv_add_f32 v237, s41, v237\nv_sub_f32 v160, v160, v237\nv_sub_f32 v161, v161, v237\nv_sub_f32 v162, v162, v237\n"
"v_sub_f32 v163, v163, v237\nv_sub_f32 v164, v164, v237\nv_sub_f32 v165, v165, v237\nv_sub_f32 v166, v166, v237\nv_sub_f32 v167, v167, v237\nv_sub_f32 v168, v168, v237\n"
"v_sub_f32 v169, v169, v237\nv_sub_f32 v170, v170, v237\nv_sub_f32 v171, v171, v237\nv_sub_f32 v172, v172, v237\nv_sub_f32 v173, v173, v237\nv_sub_f32 v174, v174, v237\n"
"v_sub_f32 v175, v175, v237\nv_sub_f32 v200, 0, v237\nv_sub_f32 v201, 0, v237\nv_sub_f32 v202, 0, v237\nv_sub_f32 v203, 0, v237\nxX v237,v176,v177,v178\n"
"xX v237,v179,v180,v237\nxX v237,v181,v182,v237\nxX v237,v183,v184,v237\nxX v237,v185,v186,v237\nxX v237,v187,v188,v237\nxX v237,v189,v190,v237\n"
"v_max_f32 v237, v191, v237\nv_mov_b32 v238, v237\ns_nop 1\nv_permlane32_swap_b32 v237, v238\nv_max_f32 v237, v237, v238\nv_mov_b32 v238, v237\n"
"s_nop 1\nv_permlane16_swap_b32 v237, v238\nv_max_f32 v237, v237, v238\nv_add_f32 v237, s41, v237\nv_sub_f32 v176, v176, v237\nv_sub_f32 v177, v177, v237\n"
"v_sub_f32 v178, v178, v237\nv_sub_f32 v179, v179, v237\nv_sub_f32 v180, v180, v237\nv_sub_f32 v181, v181, v237\nv_sub_f32 v182, v182, v237\nv_sub_f32 v183, v183, v237\n"
"v_sub_f32 v184, v184, v237\nv_sub_f32 v185, v185, v237\nv_sub_f32 v186, v186, v237\nv_sub_f32 v187, v187, v237\nv_sub_f32 v188, v188, v237\nv_sub_f32 v189, v189, v237\n"
"v_sub_f32 v190, v190, v237\nv_sub_f32 v191, v191, v237\nv_sub_f32 v204, 0, v237\nv_sub_f32 v205, 0, v237\nv_sub_f32 v206, 0, v237\nv_sub_f32 v207, 0, v237\n"
"xM v[8:11],a[136:139],v[128:131],0\nxM v[40:43],a[136:139],v[144:147],0\nxM v[12:15],a[140:143],v[128:131],0\nxA m0,s36,0\nxC v155,v158,v159\n"
"xM v[44:47],a[140:143],v[144:147],0\nxL v230,s[12:15],s32 offen offset:0 lds\nxM v[16:19],a[144:147],v[128:131],0\n"
"xM v[48:51],a[144:147],v[144:147],0\nxE v160,v160\nxM v[20:23],a[148:151],v[128:131],0\nxL v231,s[12:15],s32 offen offset:1024 lds\n"
"xM v[52:55],a[148:151],v[144:147],0\nxE v161,v161\nxM v[24:27],a[152:155],v[128:131],0\nxT a[176:177],v228 offset:9216\nxC v160,v160,v161\n"
"xM v[56:59],a[152:155],v[144:147],0\nxE v162,v162\nxM v[28:31],a[156:159],v[128:131],0\nxT a[178:179],v228 offset:13312\nxT a[180:181],v229 offset:9216\n"
"xM v[60:63],a[156:159],v[144:147],0\nxE v163,v163\nxM v[208:211],a[192:195],v[128:131],0\nxT a[182:183],v229 offset:13312\nxC v161,v162,v163\n"
"xM v[212:215],a[192:195],v[144:147],0\nxE v164,v164\nxM v[0:3],a[160:163],v[136:139],v[0:3]\nxT a[184:185],v228 offset:9728\nxT a[186:187],v228 offset:13824\n"
"xM v[32:35],a[160:163],v[152:155],v[32:35]\nxE v165,v165\nxM v[4:7],a[164:167],v[136:139],v[4:7]\nxT a[188:189],v229 offset:9728\nxC v162,v164,v165\n"
"xM v[36:39],a[164:167],v[152:155],v[36:39]\nxW lgkmcnt(6)\nxE v166,v166\n"
"xM v[8:11],a[168:171],v[136:139],v[8:11]\nxT a[190:191],v229 offset:13824\nxR a[64:67],v224 offset:16384\n"
"xM v[40:43],a[168:171],v[152:155],v[40:43]\nxE v167,v167\nxM v[12:15],a[172:175],v[136:139],v[12:15]\nxR a[68:71],v225 offset:16384\nxC v163,v166,v167\n"
"xM v[44:47],a[172:175],v[152:155],v[44:47]\nxW lgkmcnt(6)\nxE v176,v176\n"
"xM v[16:19],a[176:179],v[136:139],v[16:19]\nxL v232,s[12:15],s32 offen offset:2048 lds\nxM v[48:51],a[176:179],v[152:155],v[48:51]\nxE v177,v177\n"
"xM v[20:23],a[180:183],v[136:139],v[20:23]\nxR a[72:75],v226 offset:16384\nxC v176,v176,v177\n"
"xM v[52:55],a[180:183],v[152:155],v[52:55]\nxW lgkmcnt(4)\nxE v178,v178\n"
"xM v[24:27],a[184:187],v[136:139],v[24:27]\nxR a[76:79],v227 offset:16384\nxR a[80:83],v224 offset:20480\n"
"xM v[56:59],a[184:187],v[152:155],v[56:59]\nxW lgkmcnt(4)\nxE v179,v179\n"
"xM v[28:31],a[188:191],v[136:139],v[28:31]\nxR a[84:87],v225 offset:20480\nxC v177,v178,v179\n"
"xM v[60:63],a[188:191],v[152:155],v[60:63]\nxE v180,v180\nxM v[208:211],a[192:195],v[136:139],v[208:211]\nxR a[88:91],v226 offset:20480\nxR a[92:95],v227 offset:20480\n"
"xM v[212:215],a[192:195],v[152:155],v[212:215]\nxE v181,v181\ns_mov_b32 s35, 10\nSLOOP_%=:\n"
"xM v[128:131],a[64:67],a[0:3],v[192:195]\nxR a[96:99],v224 offset:24576\nxC v178,v180,v181\n"
"xM v[144:147],a[64:67],a[16:19],v[196:199]\nxW lgkmcnt(6)\nxE v182,v182\n"
"xM v[128:131],a[68:71],a[4:7],v[128:131]\nxR a[100:103],v225 offset:24576\nxR a[104:107],v226 offset:24576\n"
"xM v[144:147],a[68:71],a[20:23],v[144:147]\nxE v183,v183\nxM v[128:131],a[72:75],a[8:11],v[128:131]\nxR a[108:111],v227 offset:24576\nxC v179,v182,v183\n"
"xM v[144:147],a[72:75],a[24:27],v[144:147]\nxW lgkmcnt(6)\nxE v168,v168\n"
"xM v[128:131],a[76:79],a[12:15],v[128:131]\nxL v233,s[12:15],s32 offen offset:3072 lds\nxM v[144:147],a[76:79],a[28:31],v[144:147]\nxA s32,s32,16384\nxE v169,v169\n"
"xM v[132:135],a[80:83],a[0:3],v[192:195]\nxR a[112:115],v224 offset:28672\nxC v168,v168,v169\n"
"xM v[148:151],a[80:83],a[16:19],v[196:199]\nxE v170,v170\nxM v[132:135],a[84:87],a[4:7],v[132:135]\nxR a[116:119],v225 offset:28672\nxR a[120:123],v226 offset:28672\n"
"xM v[148:151],a[84:87],a[20:23],v[148:151]\nxW lgkmcnt(4)\nxE v171,v171\n"
"xM v[132:135],a[88:91],a[8:11],v[132:135]\nxX v235,v128,v129,v128\nxX v235,v130,v131,v235\n"
"xM v[148:151],a[88:91],a[24:27],v[148:151]\nxE v172,v172\nxM v[132:135],a[92:95],a[12:15],v[132:135]\nxX v235,v144,v145,v235\nxX v235,v146,v147,v235\n"
"xM v[148:151],a[92:95],a[28:31],v[148:151]\nxE v173,v173\nxM v[136:139],a[96:99],a[0:3],v[192:195]\nxC v169,v170,v171\nxC v170,v172,v173\n"
"xM v[152:155],a[96:99],a[16:19],v[196:199]\nxE v174,v174\nxM v[136:139],a[100:103],a[4:7],v[136:139]\nxR a[124:127],v227 offset:28672\n"
"xM v[152:155],a[100:103],a[20:23],v[152:155]\nxE v175,v175\nxM v[136:139],a[104:107],a[8:11],v[136:139]\nxX v235,v132,v133,v235\nxX v235,v134,v135,v235\n"
"xM v[152:155],a[104:107],a[24:27],v[152:155]\nxW lgkmcnt(1)\nxE v184,v184\n"
"xM v[136:139],a[108:111],a[12:15],v[136:139]\nxX v235,v148,v149,v235\nxX v235,v150,v151,v235\n"
"xM v[152:155],a[108:111],a[28:31],v[152:155]\nxE v185,v185\nxM v[140:143],a[112:115],a[0:3],v[192:195]\nxC v171,v174,v175\nxC v184,v184,v185\n"
"xM v[156:159],a[112:115],a[16:19],v[196:199]\nxE v186,v186\nxM v[140:143],a[116:119],a[4:7],v[140:143]\n"
"xM v[156:159],a[116:119],a[20:23],v[156:159]\nxE v187,v187\nxM v[140:143],a[120:123],a[8:11],v[140:143]\nxX v235,v136,v137,v235\nxX v235,v138,v139,v235\n"
"xM v[156:159],a[120:123],a[24:27],v[156:159]\nxW lgkmcnt(0)\nxE v188,v188\n"
"xM v[140:143],a[124:127],a[12:15],v[140:143]\nxX v235,v152,v153,v235\nxX v235,v154,v155,v235\n"
"xM v[156:159],a[124:127],a[28:31],v[156:159]\nxE v189,v189\nxM v[64:67],a[128:131],v[160:163],v[64:67]\nxW vmcnt(8)\ns_barrier\n"
"xM v[96:99],a[128:131],v[176:179],v[96:99]\nxE v190,v190\nxM v[68:71],a[132:135],v[160:163],v[68:71]\nxC v185,v186,v187\nxC v186,v188,v189\n"
"xM v[100:103],a[132:135],v[176:179],v[100:103]\nxE v191,v191\nxM v[72:75],a[136:139],v[160:163],v[72:75]\nxX v235,v140,v141,v235\nxX v235,v142,v143,v235\n"
"xM v[104:107],a[136:139],v[176:179],v[104:107]\nxX v235,v156,v157,v235\nxX v235,v158,v159,v235\n"
"xM v[76:79],a[140:143],v[160:163],v[76:79]\nxA m0,s36,32768\nv_cmp_lt_f32 vcc, s40, v235\nxC v187,v190,v191\n"
"xM v[108:111],a[140:143],v[176:179],v[108:111]\nxL v234,s[16:19],s33 offen offset:0 lds\nxM v[80:83],a[144:147],v[160:163],v[80:83]\ns_cbranch_vccnz SBR4_0_%=\nSRET4_0_%=:\n"
"xM v[112:115],a[144:147],v[176:179],v[112:115]\nxE v128,v128\nxM v[84:87],a[148:151],v[160:163],v[84:87]\nxT a[128:129],v228 offset:32768\nxT a[130:131],v228 offset:36864\n"
"xM v[116:119],a[148:151],v[176:179],v[116:119]\nxE v129,v129\nxM v[88:91],a[152:155],v[160:163],v[88:91]\nxT a[132:133],v229 offset:32768\nxC v128,v128,v129\n"
"xM v[120:123],a[152:155],v[176:179],v[120:123]\nxE v130,v130\nxM v[92:95],a[156:159],v[160:163],v[92:95]\nxT a[134:135],v229 offset:36864\nxT a[136:137],v228 offset:33280\n"
"xM v[124:127],a[156:159],v[176:179],v[124:127]\nxE v131,v131\nxM v[216:219],a[192:195],v[160:163],v[216:219]\nxT a[138:139],v228 offset:37376\nxC v129,v130,v131\n"
"xM v[220:223],a[192:195],v[176:179],v[220:223]\nxE v132,v132\nxM v[64:67],a[160:163],v[168:171],v[64:67]\nxT a[140:141],v229 offset:33280\nxT a[142:143],v229 offset:37376\n"
"xM v[96:99],a[160:163],v[184:187],v[96:99]\nxE v133,v133\nxM v[68:71],a[164:167],v[168:171],v[68:71]\nxT a[144:145],v228 offset:33792\nxC v130,v132,v133\n"
"xM v[100:103],a[164:167],v[184:187],v[100:103]\nxE v134,v134\nxM v[72:75],a[168:171],v[168:171],v[72:75]\nxL v234,s[16:19],s33 offen offset:2048 lds\n"
"xM v[104:107],a[168:171],v[184:187],v[104:107]\nxA s33,s33,16384\nxE v135,v135\n"
"xM v[76:79],a[172:175],v[168:171],v[76:79]\nxT a[146:147],v228 offset:37888\nxC v131,v134,v135\n"
"xM v[108:111],a[172:175],v[184:187],v[108:111]\nxE v144,v144\nxM v[80:83],a[176:179],v[168:171],v[80:83]\nxT a[148:149],v229 offset:33792\n"
"xM v[112:115],a[176:179],v[184:187],v[112:115]\nxE v145,v145\nxM v[84:87],a[180:183],v[168:171],v[84:87]\nxT a[150:151],v229 offset:37888\nxC v144,v144,v145\n"
"xM v[116:119],a[180:183],v[184:187],v[116:119]\nxE v146,v146\nxM v[88:91],a[184:187],v[168:171],v[88:91]\nxT a[152:153],v228 offset:34304\n"
"xM v[120:123],a[184:187],v[184:187],v[120:123]\nxE v147,v147\nxM v[92:95],a[188:191],v[168:171],v[92:95]\nxT a[154:155],v228 offset:38400\nxC v145,v146,v147\n"
"xM v[124:127],a[188:191],v[184:187],v[124:127]\nxE v148,v148\nxM v[216:219],a[192:195],v[168:171],v[216:219]\nxT a[156:157],v229 offset:34304\n"
"xM v[220:223],a[192:195],v[184:187],v[220:223]\nxE v149,v149\nxM v[160:163],a[64:67],a[32:35],v[200:203]\nxT a[158:159],v229 offset:38400\nxC v146,v148,v149\n"
"xM v[176:179],a[64:67],a[48:51],v[204:207]\nxA m0,s36,33792\nxE v150,v150\n"
"xM v[160:163],a[68:71],a[36:39],v[160:163]\nxL v234,s[16:19],s34 offen offset:0 lds\nxM v[176:179],a[68:71],a[52:55],v[176:179]\nxE v151,v151\n"
"xM v[160:163],a[72:75],a[40:43],v[160:163]\nxT a[160:161],v228 offset:40960\nxC v147,v150,v151\n"
"xM v[176:179],a[72:75],a[56:59],v[176:179]\nxE v136,v136\nxM v[160:163],a[76:79],a[44:47],v[160:163]\nxT a[162:163],v228 offset:45056\n"
"xM v[176:179],a[76:79],a[60:63],v[176:179]\nxE v137,v137\nxM v[164:167],a[80:83],a[32:35],v[200:203]\nxT a[164:165],v229 offset:40960\nxC v136,v136,v137\n"
"xM v[180:183],a[80:83],a[48:51],v[204:207]\nxE v138,v138\nxM v[164:167],a[84:87],a[36:39],v[164:167]\nxT a[166:167],v229 offset:45056\n"
"xM v[180:183],a[84:87],a[52:55],v[180:183]\nxE v139,v139\nxM v[164:167],a[88:91],a[40:43],v[164:167]\nxX v236,v160,v161,v160\nxX v236,v162,v163,v236\n"
"xM v[180:183],a[88:91],a[56:59],v[180:183]\nxE v140,v140\nxM v[164:167],a[92:95],a[44:47],v[164:167]\nxX v236,v176,v177,v236\nxX v236,v178,v179,v236\n"
"xM v[180:183],a[92:95],a[60:63],v[180:183]\nxE v141,v141\nxM v[168:171],a[96:99],a[32:35],v[200:203]\nxC v137,v138,v139\nxC v138,v140,v141\n"
"xM v[184:187],a[96:99],a[48:51],v[204:207]\nxE v142,v142\nxM v[168:171],a[100:103],a[36:39],v[168:171]\nxL v234,s[16:19],s34 offen offset:2048 lds\n"
"xM v[184:187],a[100:103],a[52:55],v[184:187]\nxA s34,s34,16384\nxE v143,v143\n"
"xM v[168:171],a[104:107],a[40:43],v[168:171]\nxX v236,v164,v165,v236\nxX v236,v166,v167,v236\n"
"xM v[184:187],a[104:107],a[56:59],v[184:187]\nxE v152,v152\nxM v[168:171],a[108:111],a[44:47],v[168:171]\nxX v236,v180,v181,v236\nxX v236,v182,v183,v236\n"
"xM v[184:187],a[108:111],a[60:63],v[184:187]\nxE v153,v153\nxM v[172:175],a[112:115],a[32:35],v[200:203]\nxC v139,v142,v143\nxC v152,v152,v153\n"
"xM v[188:191],a[112:115],a[48:51],v[204:207]\nxE v154,v154\nxM v[172:175],a[116:119],a[36:39],v[172:175]\nxT a[168:169],v228 offset:41472\nxT a[170:171],v228 offset:45568\n"
"xM v[188:191],a[116:119],a[52:55],v[188:191]\nxE v155,v155\nxM v[172:175],a[120:123],a[40:43],v[172:175]\nxX v236,v168,v169,v236\nxX v236,v170,v171,v236\n"
"xM v[188:191],a[120:123],a[56:59],v[188:191]\nxE v156,v156\nxM v[172:175],a[124:127],a[44:47],v[172:175]\nxX v236,v184,v185,v236\nxX v236,v186,v187,v236\n"
"xM v[188:191],a[124:127],a[60:63],v[188:191]\nxW lgkmcnt(2)\nxE v157,v157\n"
"xM v[0:3],a[128:131],v[128:131],v[0:3]\nxC v153,v154,v155\nxC v154,v156,v157\n"
"xM v[32:35],a[128:131],v[144:147],v[32:35]\nxE v158,v158\nxM v[4:7],a[132:135],v[128:131],v[4:7]\nxT a[172:173],v229 offset:41472\nxT a[174:175],v229 offset:45568\n"
"xM v[36:39],a[132:135],v[144:147],v[36:39]\nxE v159,v159\nxM v[8:11],a[136:139],v[128:131],v[8:11]\nxX v236,v172,v173,v236\nxX v236,v174,v175,v236\n"
"xM v[40:43],a[136:139],v[144:147],v[40:43]\nxX v236,v188,v189,v236\nxX v236,v190,v191,v236\n"
"xM v[12:15],a[140:143],v[128:131],v[12:15]\nxA m0,s36,16384\nv_cmp_lt_f32 vcc, s40, v236\nxC v155,v158,v159\n"
"xM v[44:47],a[140:143],v[144:147],v[44:47]\nxL v230,s[12:15],s32 offen offset:0 lds\nxM v[16:19],a[144:147],v[128:131],v[16:19]\ns_cbranch_vccnz SBR4_1_%=\nSRET4_1_%=:\n"
"xM v[48:51],a[144:147],v[144:147],v[48:51]\nxE v160,v160\nxM v[20:23],a[148:151],v[128:131],v[20:23]\nxL v231,s[12:15],s32 offen offset:1024 lds\n"
"xM v[52:55],a[148:151],v[144:147],v[52:55]\nxE v161,v161\nxM v[24:27],a[152:155],v[128:131],v[24:27]\nxT a[176:177],v228 offset:41984\nxC v160,v160,v161\n"
"xM v[56:59],a[152:155],v[144:147],v[56:59]\nxE v162,v162\nxM v[28:31],a[156:159],v[128:131],v[28:31]\nxT a[178:179],v228 offset:46080\nxT a[180:181],v229 offset:41984\n"
"xM v[60:63],a[156:159],v[144:147],v[60:63]\nxE v163,v163\nxM v[208:211],a[192:195],v[128:131],v[208:211]\nxT a[182:183],v229 offset:46080\nxC v161,v162,v163\n"
"xM v[212:215],a[192:195],v[144:147],v[212:215]\nxE v164,v164\nxM v[0:3],a[160:163],v[136:139],v[0:3]\nxT a[184:185],v228 offset:42496\nxT a[186:187],v228 offset:46592\n"
"xM v[32:35],a[160:163],v[152:155],v[32:35]\nxE v165,v165\nxM v[4:7],a[164:167],v[136:139],v[4:7]\nxT a[188:189],v229 offset:42496\nxC v162,v164,v165\n"
"xM v[36:39],a[164:167],v[152:155],v[36:39]\nxW lgkmcnt(6)\nxE v166,v166\n"
"xM v[8:11],a[168:171],v[136:139],v[8:11]\nxT a[190:191],v229 offset:46592\nxR a[64:67],v224 offset:49152\n"
"xM v[40:43],a[168:171],v[152:155],v[40:43]\nxE v167,v167\nxM v[12:15],a[172:175],v[136:139],v[12:15]\nxR a[68:71],v225 offset:49152\nxC v163,v166,v167\n"
"xM v[44:47],a[172:175],v[152:155],v[44:47]\nxW lgkmcnt(6)\nxE v176,v176\n"
"xM v[16:19],a[176:179],v[136:139],v[16:19]\nxL v232,s[12:15],s32 offen offset:2048 lds\nxM v[48:51],a[176:179],v[152:155],v[48:51]\nxE v177,v177\n"
"xM v[20:23],a[180:183],v[136:139],v[20:23]\nxR a[72:75],v226 offset:49152\nxC v176,v176,v177\n"
"xM v[52:55],a[180:183],v[152:155],v[52:55]\nxW lgkmcnt(4)\nxE v178,v178\n"
"xM v[24:27],a[184:187],v[136:139],v[24:27]\nxR a[76:79],v227 offset:49152\nxR a[80:83],v224 offset:53248\n"
"xM v[56:59],a[184:187],v[152:155],v[56:59]\nxW lgkmcnt(4)\nxE v179,v179\n"
"xM v[28:31],a[188:191],v[136:139],v[28:31]\nxR a[84:87],v225 offset:53248\nxC v177,v178,v179\n"
"xM v[60:63],a[188:191],v[152:155],v[60:63]\nxE v180,v180\nxM v[208:211],a[192:195],v[136:139],v[208:211]\nxR a[88:91],v226 offset:53248\nxR a[92:95],v227 offset:53248\n"
"xM v[212:215],a[192:195],v[152:155],v[212:215]\nxE v181,v181\nxM v[128:131],a[64:67],a[0:3],v[192:195]\nxR a[96:99],v224 offset:57344\nxC v178,v180,v181\n"
"xM v[144:147],a[64:67],a[16:19],v[196:199]\nxW lgkmcnt(6)\nxE v182,v182\n"
"xM v[128:131],a[68:71],a[4:7],v[128:131]\nxR a[100:103],v225 offset:57344\nxR a[104:107],v226 offset:57344\n"
"xM v[144:147],a[68:71],a[20:23],v[144:147]\nxE v183,v183\nxM v[128:131],a[72:75],a[8:11],v[128:131]\nxR a[108:111],v227 offset:57344\nxC v179,v182,v183\n"
"xM v[144:147],a[72:75],a[24:27],v[144:147]\nxW lgkmcnt(6)\nxE v168,v168\n"
"xM v[128:131],a[76:79],a[12:15],v[128:131]\nxL v233,s[12:15],s32 offen offset:3072 lds\nxM v[144:147],a[76:79],a[28:31],v[144:147]\nxA s32,s32,16384\nxE v169,v169\n"
"xM v[132:135],a[80:83],a[0:3],v[192:195]\nxR a[112:115],v224 offset:61440\nxC v168,v168,v169\n"
"xM v[148:151],a[80:83],a[16:19],v[196:199]\nxE v170,v170\nxM v[132:135],a[84:87],a[4:7],v[132:135]\nxR a[116:119],v225 offset:61440\nxR a[120:123],v226 offset:61440\n"
"xM v[148:151],a[84:87],a[20:23],v[148:151]\nxW lgkmcnt(4)\nxE v171,v171\n"
"xM v[132:135],a[88:91],a[8:11],v[132:135]\nxX v235,v128,v129,v128\nxX v235,v130,v131,v235\n"
"xM v[148:151],a[88:91],a[24:27],v[148:151]\nxE v172,v172\nxM v[132:135],a[92:95],a[12:15],v[132:135]\nxX v235,v144,v145,v235\nxX v235,v146,v147,v235\n"
"xM v[148:151],a[92:95],a[28:31],v[148:151]\nxE v173,v173\nxM v[136:139],a[96:99],a[0:3],v[192:195]\nxC v169,v170,v171\nxC v170,v172,v173\n"
"xM v[152:155],a[96:99],a[16:19],v[196:199]\nxE v174,v174\nxM v[136:139],a[100:103],a[4:7],v[136:139]\nxR a[124:127],v227 offset:61440\n"
"xM v[152:155],a[100:103],a[20:23],v[152:155]\nxE v175,v175\nxM v[136:139],a[104:107],a[8:11],v[136:139]\nxX v235,v132,v133,v235\nxX v235,v134,v135,v235\n"
"xM v[152:155],a[104:107],a[24:27],v[152:155]\nxW lgkmcnt(1)\nxE v184,v184\n"
"xM v[136:139],a[108:111],a[12:15],v[136:139]\nxX v235,v148,v149,v235\nxX v235,v150,v151,v235\n"
"xM v[152:155],a[108:111],a[28:31],v[152:155]\nxE v185,v185\nxM v[140:143],a[112:115],a[0:3],v[192:195]\nxC v171,v174,v175\nxC v184,v184,v185\n"
"xM v[156:159],a[112:115],a[16:19],v[196:199]\nxE v186,v186\nxM v[140:143],a[116:119],a[4:7],v[140:143]\n"
"xM v[156:159],a[116:119],a[20:23],v[156:159]\nxE v187,v187\nxM v[140:143],a[120:123],a[8:11],v[140:143]\nxX v235,v136,v137,v235\nxX v235,v138,v139,v235\n"
"xM v[156:159],a[120:123],a[24:27],v[156:159]\nxW lgkmcnt(0)\nxE v188,v188\n"
"xM v[140:143],a[124:127],a[12:15],v[140:143]\nxX v235,v152,v153,v235\nxX v235,v154,v155,v235\n"
"xM v[156:159],a[124:127],a[28:31],v[156:159]\nxE v189,v189\nxM v[64:67],a[128:131],v[160:163],v[64:67]\nxW vmcnt(8)\ns_barrier\n"
"xM v[96:99],a[128:131],v[176:179],v[96:99]\nxE v190,v190\nxM v[68:71],a[132:135],v[160:163],v[68:71]\nxC v185,v186,v187\nxC v186,v188,v189\n"
"xM v[100:103],a[132:135],v[176:179],v[100:103]\nxE v191,v191\nxM v[72:75],a[136:139],v[160:163],v[72:75]\nxX v235,v140,v141,v235\nxX v235,v142,v143,v235\n"
"xM v[104:107],a[136:139],v[176:179],v[104:107]\nxX v235,v156,v157,v235\nxX v235,v158,v159,v235\n"
"xM v[76:79],a[140:143],v[160:163],v[76:79]\nxA m0,s36,65536\nv_cmp_lt_f32 vcc, s40, v235\nxC v187,v190,v191\n"
"xM v[108:111],a[140:143],v[176:179],v[108:111]\nxL v234,s[16:19],s33 offen offset:0 lds\nxM v[80:83],a[144:147],v[160:163],v[80:83]\ns_cbranch_vccnz SBR5_0_%=\nSRET5_0_%=:\n"
"xM v[112:115],a[144:147],v[176:179],v[112:115]\nxE v128,v128\nxM v[84:87],a[148:151],v[160:163],v[84:87]\nxT a[128:129],v228 offset:49152\nxT a[130:131],v228 offset:53248\n"
"xM v[116:119],a[148:151],v[176:179],v[116:119]\nxE v129,v129\nxM v[88:91],a[152:155],v[160:163],v[88:91]\nxT a[132:133],v229 offset:49152\nxC v128,v128,v129\n"
"xM v[120:123],a[152:155],v[176:179],v[120:123]\nxE v130,v130\nxM v[92:95],a[156:159],v[160:163],v[92:95]\nxT a[134:135],v229 offset:53248\nxT a[136:137],v228 offset:49664\n"
"xM v[124:127],a[156:159],v[176:179],v[124:127]\nxE v131,v131\nxM v[216:219],a[192:195],v[160:163],v[216:219]\nxT a[138:139],v228 offset:53760\nxC v129,v130,v131\n"
"xM v[220:223],a[192:195],v[176:179],v[220:223]\nxE v132,v132\nxM v[64:67],a[160:163],v[168:171],v[64:67]\nxT a[140:141],v229 offset:49664\nxT a[142:143],v229 offset:53760\n"
"xM v[96:99],a[160:163],v[184:187],v[96:99]\nxE v133,v133\nxM v[68:71],a[164:167],v[168:171],v[68:71]\nxT a[144:145],v228 offset:50176\nxC v130,v132,v133\n"
"xM v[100:103],a[164:167],v[184:187],v[100:103]\nxE v134,v134\nxM v[72:75],a[168:171],v[168:171],v[72:75]\nxL v234,s[16:19],s33 offen offset:2048 lds\n"
"xM v[104:107],a[168:171],v[184:187],v[104:107]\nxA s33,s33,16384\nxE v135,v135\n"
"xM v[76:79],a[172:175],v[168:171],v[76:79]\nxT a[146:147],v228 offset:54272\nxC v131,v134,v135\n"
"xM v[108:111],a[172:175],v[184:187],v[108:111]\nxE v144,v144\nxM v[80:83],a[176:179],v[168:171],v[80:83]\nxT a[148:149],v229 offset:50176\n"
"xM v[112:115],a[176:179],v[184:187],v[112:115]\nxE v145,v145\nxM v[84:87],a[180:183],v[168:171],v[84:87]\nxT a[150:151],v229 offset:54272\nxC v144,v144,v145\n"
"xM v[116:119],a[180:183],v[184:187],v[116:119]\nxE v146,v146\nxM v[88:91],a[184:187],v[168:171],v[88:91]\nxT a[152:153],v228 offset:50688\n"
"xM v[120:123],a[184:187],v[184:187],v[120:123]\nxE v147,v147\nxM v[92:95],a[188:191],v[168:171],v[92:95]\nxT a[154:155],v228 offset:54784\nxC v145,v146,v147\n"
"xM v[124:127],a[188:191],v[184:187],v[124:127]\nxE v148,v148\nxM v[216:219],a[192:195],v[168:171],v[216:219]\nxT a[156:157],v229 offset:50688\n"
"xM v[220:223],a[192:195],v[184:187],v[220:223]\nxE v149,v149\nxM v[160:163],a[64:67],a[32:35],v[200:203]\nxT a[158:159],v229 offset:54784\nxC v146,v148,v149\n"
"xM v[176:179],a[64:67],a[48:51],v[204:207]\nxA m0,s36,66560\nxE v150,v150\n"
"xM v[160:163],a[68:71],a[36:39],v[160:163]\nxL v234,s[16:19],s34 offen offset:0 lds\nxM v[176:179],a[68:71],a[52:55],v[176:179]\nxE v151,v151\n"
"xM v[160:163],a[72:75],a[40:43],v[160:163]\nxT a[160:161],v228 offset:57344\nxC v147,v150,v151\n"
"xM v[176:179],a[72:75],a[56:59],v[176:179]\nxE v136,v136\nxM v[160:163],a[76:79],a[44:47],v[160:163]\nxT a[162:163],v228 offset:61440\n"
"xM v[176:179],a[76:79],a[60:63],v[176:179]\nxE v137,v137\nxM v[164:167],a[80:83],a[32:35],v[200:203]\nxT a[164:165],v229 offset:57344\nxC v136,v136,v137\n"
"xM v[180:183],a[80:83],a[48:51],v[204:207]\nxE v138,v138\nxM v[164:167],a[84:87],a[36:39],v[164:167]\nxT a[166:167],v229 offset:61440\n"
"xM v[180:183],a[84:87],a[52:55],v[180:183]\nxE v139,v139\nxM v[164:167],a[88:91],a[40:43],v[164:167]\nxX v236,v160,v161,v160\nxX v236,v162,v163,v236\n"
"xM v[180:183],a[88:91],a[56:59],v[180:183]\nxE v140,v140\nxM v[164:167],a[92:95],a[44:47],v[164:167]\nxX v236,v176,v177,v236\nxX v236,v178,v179,v236\n"
"xM v[180:183],a[92:95],a[60:63],v[180:183]\nxE v141,v141\nxM v[168:171],a[96:99],a[32:35],v[200:203]\nxC v137,v138,v139\nxC v138,v140,v141\n"
"xM v[184:187],a[96:99],a[48:51],v[204:207]\nxE v142,v142\nxM v[168:171],a[100:103],a[36:39],v[168:171]\nxL v234,s[16:19],s34 offen offset:2048 lds\n"
"xM v[184:187],a[100:103],a[52:55],v[184:187]\nxA s34,s34,16384\nxE v143,v143\n"
"xM v[168:171],a[104:107],a[40:43],v[168:171]\nxX v236,v164,v165,v236\nxX v236,v166,v167,v236\n"
"xM v[184:187],a[104:107],a[56:59],v[184:187]\nxE v152,v152\nxM v[168:171],a[108:111],a[44:47],v[168:171]\nxX v236,v180,v181,v236\nxX v236,v182,v183,v236\n"
"xM v[184:187],a[108:111],a[60:63],v[184:187]\nxE v153,v153\nxM v[172:175],a[112:115],a[32:35],v[200:203]\nxC v139,v142,v143\nxC v152,v152,v153\n"
"xM v[188:191],a[112:115],a[48:51],v[204:207]\nxE v154,v154\nxM v[172:175],a[116:119],a[36:39],v[172:175]\nxT a[168:169],v228 offset:57856\nxT a[170:171],v228 offset:61952\n"
"xM v[188:191],a[116:119],a[52:55],v[188:191]\nxE v155,v155\nxM v[172:175],a[120:123],a[40:43],v[172:175]\nxX v236,v168,v169,v236\nxX v236,v170,v171,v236\n"
"xM v[188:191],a[120:123],a[56:59],v[188:191]\nxE v156,v156\nxM v[172:175],a[124:127],a[44:47],v[172:175]\nxX v236,v184,v185,v236\nxX v236,v186,v187,v236\n"
"xM v[188:191],a[124:127],a[60:63],v[188:191]\nxW lgkmcnt(2)\nxE v157,v157\n"
"xM v[0:3],a[128:131],v[128:131],v[0:3]\nxC v153,v154,v155\nxC v154,v156,v157\n"
"xM v[32:35],a[128:131],v[144:147],v[32:35]\nxE v158,v158\nxM v[4:7],a[132:135],v[128:131],v[4:7]\nxT a[172:173],v229 offset:57856\nxT a[174:175],v229 offset:61952\n"
"xM v[36:39],a[132:135],v[144:147],v[36:39]\nxE v159,v159\nxM v[8:11],a[136:139],v[128:131],v[8:11]\nxX v236,v172,v173,v236\nxX v236,v174,v175,v236\n"
"xM v[40:43],a[136:139],v[144:147],v[40:43]\nxX v236,v188,v189,v236\nxX v236,v190,v191,v236\n"
"xM v[12:15],a[140:143],v[128:131],v[12:15]\nxA m0,s36,49152\nv_cmp_lt_f32 vcc, s40, v236\nxC v155,v158,v159\n"
"xM v[44:47],a[140:143],v[144:147],v[44:47]\nxL v230,s[12:15],s32 offen offset:0 lds\nxM v[16:19],a[144:147],v[128:131],v[16:19]\ns_cbranch_vccnz SBR5_1_%=\nSRET5_1_%=:\n"
"xM v[48:51],a[144:147],v[144:147],v[48:51]\nxE v160,v160\nxM v[20:23],a[148:151],v[128:131],v[20:23]\nxL v231,s[12:15],s32 offen offset:1024 lds\n"
"xM v[52:55],a[148:151],v[144:147],v[52:55]\nxE v161,v161\nxM v[24:27],a[152:155],v[128:131],v[24:27]\nxT a[176:177],v228 offset:58368\nxC v160,v160,v161\n"
"xM v[56:59],a[152:155],v[144:147],v[56:59]\nxE v162,v162\nxM v[28:31],a[156:159],v[128:131],v[28:31]\nxT a[178:179],v228 offset:62464\nxT a[180:181],v229 offset:58368\n"
"xM v[60:63],a[156:159],v[144:147],v[60:63]\nxE v163,v163\nxM v[208:211],a[192:195],v[128:131],v[208:211]\nxT a[182:183],v229 offset:62464\nxC v161,v162,v163\n"
"xM v[212:215],a[192:195],v[144:147],v[212:215]\nxE v164,v164\nxM v[0:3],a[160:163],v[136:139],v[0:3]\nxT a[184:185],v228 offset:58880\nxT a[186:187],v228 offset:62976\n"
"xM v[32:35],a[160:163],v[152:155],v[32:35]\nxE v165,v165\nxM v[4:7],a[164:167],v[136:139],v[4:7]\nxT a[188:189],v229 offset:58880\nxC v162,v164,v165\n"
"xM v[36:39],a[164:167],v[152:155],v[36:39]\nxW lgkmcnt(6)\nxE v166,v166\n"
"xM v[8:11],a[168:171],v[136:139],v[8:11]\nxT a[190:191],v229 offset:62976\nxR a[64:67],v224 offset:0\n"
"xM v[40:43],a[168:171],v[152:155],v[40:43]\nxE v167,v167\nxM v[12:15],a[172:175],v[136:139],v[12:15]\nxR a[68:71],v225 offset:0\nxC v163,v166,v167\n"
"xM v[44:47],a[172:175],v[152:155],v[44:47]\nxW lgkmcnt(6)\nxE v176,v176\n"
"xM v[16:19],a[176:179],v[136:139],v[16:19]\nxL v232,s[12:15],s32 offen offset:2048 lds\nxM v[48:51],a[176:179],v[152:155],v[48:51]\nxE v177,v177\n"
"xM v[20:23],a[180:183],v[136:139],v[20:23]\nxR a[72:75],v226 offset:0\nxC v176,v176,v177\n"
"xM v[52:55],a[180:183],v[152:155],v[52:55]\nxW lgkmcnt(4)\nxE v178,v178\n"
"xM v[24:27],a[184:187],v[136:139],v[24:27]\nxR a[76:79],v227 offset:0\nxR a[80:83],v224 offset:4096\n"
"xM v[56:59],a[184:187],v[152:155],v[56:59]\nxW lgkmcnt(4)\nxE v179,v179\n"
"xM v[28:31],a[188:191],v[136:139],v[28:31]\nxR a[84:87],v225 offset:4096\nxC v177,v178,v179\n"
"xM v[60:63],a[188:191],v[152:155],v[60:63]\nxE v180,v180\nxM v[208:211],a[192:195],v[136:139],v[208:211]\nxR a[88:91],v226 offset:4096\nxR a[92:95],v227 offset:4096\n"
"xM v[212:215],a[192:195],v[152:155],v[212:215]\nxE v181,v181\nxM v[128:131],a[64:67],a[0:3],v[192:195]\nxR a[96:99],v224 offset:8192\nxC v178,v180,v181\n"
"xM v[144:147],a[64:67],a[16:19],v[196:199]\nxW lgkmcnt(6)\nxE v182,v182\n"
"xM v[128:131],a[68:71],a[4:7],v[128:131]\nxR a[100:103],v225 offset:8192\nxR a[104:107],v226 offset:8192\n"
"xM v[144:147],a[68:71],a[20:23],v[144:147]\nxE v183,v183\nxM v[128:131],a[72:75],a[8:11],v[128:131]\nxR a[108:111],v227 offset:8192\nxC v179,v182,v183\n"
"xM v[144:147],a[72:75],a[24:27],v[144:147]\nxW lgkmcnt(6)\nxE v168,v168\n"
"xM v[128:131],a[76:79],a[12:15],v[128:131]\nxL v233,s[12:15],s32 offen offset:3072 lds\nxM v[144:147],a[76:79],a[28:31],v[144:147]\nxA s32,s32,16384\nxE v169,v169\n"
"xM v[132:135],a[80:83],a[0:3],v[192:195]\nxR a[112:115],v224 offset:12288\nxC v168,v168,v169\n"
"xM v[148:151],a[80:83],a[16:19],v[196:199]\nxE v170,v170\nxM v[132:135],a[84:87],a[4:7],v[132:135]\nxR a[116:119],v225 offset:12288\nxR a[120:123],v226 offset:12288\n"
"xM v[148:151],a[84:87],a[20:23],v[148:151]\nxW lgkmcnt(4)\nxE v171,v171\n"
"xM v[132:135],a[88:91],a[8:11],v[132:135]\nxX v235,v128,v129,v128\nxX v235,v130,v131,v235\n"
"xM v[148:151],a[88:91],a[24:27],v[148:151]\nxE v172,v172\nxM v[132:135],a[92:95],a[12:15],v[132:135]\nxX v235,v144,v145,v235\nxX v235,v146,v147,v235\n"
"xM v[148:151],a[92:95],a[28:31],v[148:151]\nxE v173,v173\nxM v[136:139],a[96:99],a[0:3],v[192:195]\nxC v169,v170,v171\nxC v170,v172,v173\n"
"xM v[152:155],a[96:99],a[16:19],v[196:199]\nxE v174,v174\nxM v[136:139],a[100:103],a[4:7],v[136:139]\nxR a[124:127],v227 offset:12288\n"
"xM v[152:155],a[100:103],a[20:23],v[152:155]\nxE v175,v175\nxM v[136:139],a[104:107],a[8:11],v[136:139]\nxX v235,v132,v133,v235\nxX v235,v134,v135,v235\n"
"xM v[152:155],a[104:107],a[24:27],v[152:155]\nxW lgkmcnt(1)\nxE v184,v184\n"
"xM v[136:139],a[108:111],a[12:15],v[136:139]\nxX v235,v148,v149,v235\nxX v235,v150,v151,v235\n"
"xM v[152:155],a[108:111],a[28:31],v[152:155]\nxE v185,v185\nxM v[140:143],a[112:115],a[0:3],v[192:195]\nxC v171,v174,v175\nxC v184,v184,v185\n"
"xM v[156:159],a[112:115],a[16:19],v[196:199]\nxE v186,v186\nxM v[140:143],a[116:119],a[4:7],v[140:143]\n"
"xM v[156:159],a[116:119],a[20:23],v[156:159]\nxE v187,v187\nxM v[140:143],a[120:123],a[8:11],v[140:143]\nxX v235,v136,v137,v235\nxX v235,v138,v139,v235\n"
"xM v[156:159],a[120:123],a[24:27],v[156:159]\nxW lgkmcnt(0)\nxE v188,v188\n"
"xM v[140:143],a[124:127],a[12:15],v[140:143]\nxX v235,v152,v153,v235\nxX v235,v154,v155,v235\n"
"xM v[156:159],a[124:127],a[28:31],v[156:159]\nxE v189,v189\nxM v[64:67],a[128:131],v[160:163],v[64:67]\nxW vmcnt(8)\ns_barrier\n"
"xM v[96:99],a[128:131],v[176:179],v[96:99]\nxE v190,v190\nxM v[68:71],a[132:135],v[160:163],v[68:71]\nxC v185,v186,v187\nxC v186,v188,v189\n"
"xM v[100:103],a[132:135],v[176:179],v[100:103]\nxE v191,v191\nxM v[72:75],a[136:139],v[160:163],v[72:75]\nxX v235,v140,v141,v235\nxX v235,v142,v143,v235\n"
"xM v[104:107],a[136:139],v[176:179],v[104:107]\nxX v235,v156,v157,v235\nxX v235,v158,v159,v235\n"
"xM v[76:79],a[140:143],v[160:163],v[76:79]\nxA m0,s36,81920\nv_cmp_lt_f32 vcc, s40, v235\nxC v187,v190,v191\n"
"xM v[108:111],a[140:143],v[176:179],v[108:111]\nxL v234,s[16:19],s33 offen offset:0 lds\nxM v[80:83],a[144:147],v[160:163],v[80:83]\ns_cbranch_vccnz SBR6_0_%=\nSRET6_0_%=:\n"
"xM v[112:115],a[144:147],v[176:179],v[112:115]\nxE v128,v128\nxM v[84:87],a[148:151],v[160:163],v[84:87]\nxT a[128:129],v228 offset:0\nxT a[130:131],v228 offset:4096\n"
"xM v[116:119],a[148:151],v[176:179],v[116:119]\nxE v129,v129\nxM v[88:91],a[152:155],v[160:163],v[88:91]\nxT a[132:133],v229 offset:0\nxC v128,v128,v129\n"
"xM v[120:123],a[152:155],v[176:179],v[120:123]\nxE v130,v130\nxM v[92:95],a[156:159],v[160:163],v[92:95]\nxT a[134:135],v229 offset:4096\nxT a[136:137],v228 offset:512\n"
"xM v[124:127],a[156:159],v[176:179],v[124:127]\nxE v131,v131\nxM v[216:219],a[192:195],v[160:163],v[216:219]\nxT a[138:139],v228 offset:4608\nxC v129,v130,v131\n"
"xM v[220:223],a[192:195],v[176:179],v[220:223]\nxE v132,v132\nxM v[64:67],a[160:163],v[168:171],v[64:67]\nxT a[140:141],v229 offset:512\nxT a[142:143],v229 offset:4608\n"
"xM v[96:99],a[160:163],v[184:187],v[96:99]\nxE v133,v133\nxM v[68:71],a[164:167],v[168:171],v[68:71]\nxT a[144:145],v228 offset:1024\nxC v130,v132,v133\n"
"xM v[100:103],a[164:167],v[184:187],v[100:103]\nxE v134,v134\nxM v[72:75],a[168:171],v[168:171],v[72:75]\nxL v234,s[16:19],s33 offen offset:2048 lds\n"
"xM v[104:107],a[168:171],v[184:187],v[104:107]\nxA s33,s33,16384\nxE v135,v135\n"
"xM v[76:79],a[172:175],v[168:171],v[76:79]\nxT a[146:147],v228 offset:5120\nxC v131,v134,v135\n"
"xM v[108:111],a[172:175],v[184:187],v[108:111]\nxE v144,v144\nxM v[80:83],a[176:179],v[168:171],v[80:83]\nxT a[148:149],v229 offset:1024\n"
"xM v[112:115],a[176:179],v[184:187],v[112:115]\nxE v145,v145\nxM v[84:87],a[180:183],v[168:171],v[84:87]\nxT a[150:151],v229 offset:5120\nxC v144,v144,v145\n"
"xM v[116:119],a[180:183],v[184:187],v[116:119]\nxE v146,v146\nxM v[88:91],a[184:187],v[168:171],v[88:91]\nxT a[152:153],v228 offset:1536\n"
"xM v[120:123],a[184:187],v[184:187],v[120:123]\nxE v147,v147\nxM v[92:95],a[188:191],v[168:171],v[92:95]\nxT a[154:155],v228 offset:5632\nxC v145,v146,v147\n"
"xM v[124:127],a[188:191],v[184:187],v[124:127]\nxE v148,v148\nxM v[216:219],a[192:195],v[168:171],v[216:219]\nxT a[156:157],v229 offset:1536\n"
"xM v[220:223],a[192:195],v[184:187],v[220:223]\nxE v149,v149\nxM v[160:163],a[64:67],a[32:35],v[200:203]\nxT a[158:159],v229 offset:5632\nxC v146,v148,v149\n"
"xM v[176:179],a[64:67],a[48:51],v[204:207]\nxA m0,s36,82944\nxE v150,v150\n"
"xM v[160:163],a[68:71],a[36:39],v[160:163]\nxL v234,s[16:19],s34 offen offset:0 lds\nxM v[176:179],a[68:71],a[52:55],v[176:179]\nxE v151,v151\n"
"xM v[160:163],a[72:75],a[40:43],v[160:163]\nxT a[160:161],v228 offset:8192\nxC v147,v150,v151\n"
"xM v[176:179],a[72:75],a[56:59],v[176:179]\nxE v136,v136\nxM v[160:163],a[76:79],a[44:47],v[160:163]\nxT a[162:163],v228 offset:12288\n"
"xM v[176:179],a[76:79],a[60:63],v[176:179]\nxE v137,v137\nxM v[164:167],a[80:83],a[32:35],v[200:203]\nxT a[164:165],v229 offset:8192\nxC v136,v136,v137\n"
"xM v[180:183],a[80:83],a[48:51],v[204:207]\nxE v138,v138\nxM v[164:167],a[84:87],a[36:39],v[164:167]\nxT a[166:167],v229 offset:12288\n"
"xM v[180:183],a[84:87],a[52:55],v[180:183]\nxE v139,v139\nxM v[164:167],a[88:91],a[40:43],v[164:167]\nxX v236,v160,v161,v160\nxX v236,v162,v163,v236\n"
"xM v[180:183],a[88:91],a[56:59],v[180:183]\nxE v140,v140\nxM v[164:167],a[92:95],a[44:47],v[164:167]\nxX v236,v176,v177,v236\nxX v236,v178,v179,v236\n"
"xM v[180:183],a[92:95],a[60:63],v[180:183]\nxE v141,v141\nxM v[168:171],a[96:99],a[32:35],v[200:203]\nxC v137,v138,v139\nxC v138,v140,v141\n"
"xM v[184:187],a[96:99],a[48:51],v[204:207]\nxE v142,v142\nxM v[168:171],a[100:103],a[36:39],v[168:171]\nxL v234,s[16:19],s34 offen offset:2048 lds\n"
"xM v[184:187],a[100:103],a[52:55],v[184:187]\nxA s34,s34,16384\nxE v143,v143\n"
"xM v[168:171],a[104:107],a[40:43],v[168:171]\nxX v236,v164,v165,v236\nxX v236,v166,v167,v236\n"
"xM v[184:187],a[104:107],a[56:59],v[184:187]\nxE v152,v152\nxM v[168:171],a[108:111],a[44:47],v[168:171]\nxX v236,v180,v181,v236\nxX v236,v182,v183,v236\n"
"xM v[184:187],a[108:111],a[60:63],v[184:187]\nxE v153,v153\nxM v[172:175],a[112:115],a[32:35],v[200:203]\nxC v139,v142,v143\nxC v152,v152,v153\n"
"xM v[188:191],a[112:115],a[48:51],v[204:207]\nxE v154,v154\nxM v[172:175],a[116:119],a[36:39],v[172:175]\nxT a[168:169],v228 offset:8704\nxT a[170:171],v228 offset:12800\n"
"xM v[188:191],a[116:119],a[52:55],v[188:191]\nxE v155,v155\nxM v[172:175],a[120:123],a[40:43],v[172:175]\nxX v236,v168,v169,v236\nxX v236,v170,v171,v236\n"
"xM v[188:191],a[120:123],a[56:59],v[188:191]\nxE v156,v156\nxM v[172:175],a[124:127],a[44:47],v[172:175]\nxX v236,v184,v185,v236\nxX v236,v186,v187,v236\n"
"xM v[188:191],a[124:127],a[60:63],v[188:191]\nxW lgkmcnt(2)\nxE v157,v157\n"
"xM v[0:3],a[128:131],v[128:131],v[0:3]\nxC v153,v154,v155\nxC v154,v156,v157\n"
"xM v[32:35],a[128:131],v[144:147],v[32:35]\nxE v158,v158\nxM v[4:7],a[132:135],v[128:131],v[4:7]\nxT a[172:173],v229 offset:8704\nxT a[174:175],v229 offset:12800\n"
"xM v[36:39],a[132:135],v[144:147],v[36:39]\nxE v159,v159\nxM v[8:11],a[136:139],v[128:131],v[8:11]\nxX v236,v172,v173,v236\nxX v236,v174,v175,v236\n"
"xM v[40:43],a[136:139],v[144:147],v[40:43]\nxX v236,v188,v189,v236\nxX v236,v190,v191,v236\n"
"xM v[12:15],a[140:143],v[128:131],v[12:15]\nxA m0,s36,0\nv_cmp_lt_f32 vcc, s40, v236\nxC v155,v158,v159\n"
"xM v[44:47],a[140:143],v[144:147],v[44:47]\nxL v230,s[12:15],s32 offen offset:0 lds\nxM v[16:19],a[144:147],v[128:131],v[16:19]\ns_cbranch_vccnz SBR6_1_%=\nSRET6_1_%=:\n"
"xM v[48:51],a[144:147],v[144:147],v[48:51]\nxE v160,v160\nxM v[20:23],a[148:151],v[128:131],v[20:23]\nxL v231,s[12:15],s32 offen offset:1024 lds\n"
"xM v[52:55],a[148:151],v[144:147],v[52:55]\nxE v161,v161\nxM v[24:27],a[152:155],v[128:131],v[24:27]\nxT a[176:177],v228 offset:9216\nxC v160,v160,v161\n"
"xM v[56:59],a[152:155],v[144:147],v[56:59]\nxE v162,v162\nxM v[28:31],a[156:159],v[128:131],v[28:31]\nxT a[178:179],v228 offset:13312\nxT a[180:181],v229 offset:9216\n"
"xM v[60:63],a[156:159],v[144:147],v[60:63]\nxE v163,v163\nxM v[208:211],a[192:195],v[128:131],v[208:211]\nxT a[182:183],v229 offset:13312\nxC v161,v162,v163\n"
"xM v[212:215],a[192:195],v[144:147],v[212:215]\nxE v164,v164\nxM v[0:3],a[160:163],v[136:139],v[0:3]\nxT a[184:185],v228 offset:9728\nxT a[186:187],v228 offset:13824\n"
"xM v[32:35],a[160:163],v[152:155],v[32:35]\nxE v165,v165\nxM v[4:7],a[164:167],v[136:139],v[4:7]\nxT a[188:189],v229 offset:9728\nxC v162,v164,v165\n"
"xM v[36:39],a[164:167],v[152:155],v[36:39]\nxW lgkmcnt(6)\nxE v166,v166\n"
"xM v[8:11],a[168:171],v[136:139],v[8:11]\nxT a[190:191],v229 offset:13824\nxR a[64:67],v224 offset:16384\n"
"xM v[40:43],a[168:171],v[152:155],v[40:43]\nxE v167,v167\nxM v[12:15],a[172:175],v[136:139],v[12:15]\nxR a[68:71],v225 offset:16384\nxC v163,v166,v167\n"
"xM v[44:47],a[172:175],v[152:155],v[44:47]\nxW lgkmcnt(6)\nxE v176,v176\n"
"xM v[16:19],a[176:179],v[136:139],v[16:19]\nxL v232,s[12:15],s32 offen offset:2048 lds\nxM v[48:51],a[176:179],v[152:155],v[48:51]\nxE v177,v177\n"
"xM v[20:23],a[180:183],v[136:139],v[20:23]\nxR a[72:75],v226 offset:16384\nxC v176,v176,v177\n"
"xM v[52:55],a[180:183],v[152:155],v[52:55]\nxW lgkmcnt(4)\nxE v178,v178\n"
"xM v[24:27],a[184:187],v[136:139],v[24:27]\nxR a[76:79],v227 offset:16384\nxR a[80:83],v224 offset:20480\n"
"xM v[56:59],a[184:187],v[152:155],v[56:59]\nxW lgkmcnt(4)\nxE v179,v179\n"
"xM v[28:31],a[188:191],v[136:139],v[28:31]\nxR a[84:87],v225 offset:20480\nxC v177,v178,v179\n"
"xM v[60:63],a[188:191],v[152:155],v[60:63]\nxE v180,v180\nxM v[208:211],a[192:195],v[136:139],v[208:211]\nxR a[88:91],v226 offset:20480\nxR a[92:95],v227 offset:20480\n"
"xM v[212:215],a[192:195],v[152:155],v[212:215]\nxE v181,v181\ns_sub_u32 s35, s35, 1\ns_cmp_lg_u32 s35, 0\ns_cbranch_scc1 SLOOP_%=\n"
"xM v[128:131],a[64:67],a[0:3],v[192:195]\nxR a[96:99],v224 offset:24576\nxC v178,v180,v181\n"
"xM v[144:147],a[64:67],a[16:19],v[196:199]\nxW lgkmcnt(6)\nxE v182,v182\n"
"xM v[128:131],a[68:71],a[4:7],v[128:131]\nxR a[100:103],v225 offset:24576\nxR a[104:107],v226 offset:24576\n"
"xM v[144:147],a[68:71],a[20:23],v[144:147]\nxE v183,v183\nxM v[128:131],a[72:75],a[8:11],v[128:131]\nxR a[108:111],v227 offset:24576\nxC v179,v182,v183\n"
"xM v[144:147],a[72:75],a[24:27],v[144:147]\nxW lgkmcnt(6)\nxE v168,v168\n"
"xM v[128:131],a[76:79],a[12:15],v[128:131]\nxM v[144:147],a[76:79],a[28:31],v[144:147]\nxE v169,v169\n"
"xM v[132:135],a[80:83],a[0:3],v[192:195]\nxR a[112:115],v224 offset:28672\nxC v168,v168,v169\n"
"xM v[148:151],a[80:83],a[16:19],v[196:199]\nxE v170,v170\nxM v[132:135],a[84:87],a[4:7],v[132:135]\nxR a[116:119],v225 offset:28672\nxR a[120:123],v226 offset:28672\n"
"xM v[148:151],a[84:87],a[20:23],v[148:151]\nxW lgkmcnt(4)\nxE v171,v171\n"
"xM v[132:135],a[88:91],a[8:11],v[132:135]\nxX v235,v128,v129,v128\nxX v235,v130,v131,v235\n"
"xM v[148:151],a[88:91],a[24:27],v[148:151]\nxE v172,v172\nxM v[132:135],a[92:95],a[12:15],v[132:135]\nxX v235,v144,v145,v235\nxX v235,v146,v147,v235\n"
"xM v[148:151],a[92:95],a[28:31],v[148:151]\nxE v173,v173\nxM v[136:139],a[96:99],a[0:3],v[192:195]\nxC v169,v170,v171\nxC v170,v172,v173\n"
"xM v[152:155],a[96:99],a[16:19],v[196:199]\nxE v174,v174\nxM v[136:139],a[100:103],a[4:7],v[136:139]\nxR a[124:127],v227 offset:28672\n"
"xM v[152:155],a[100:103],a[20:23],v[152:155]\nxE v175,v175\nxM v[136:139],a[104:107],a[8:11],v[136:139]\nxX v235,v132,v133,v235\nxX v235,v134,v135,v235\n"
"xM v[152:155],a[104:107],a[24:27],v[152:155]\nxW lgkmcnt(1)\nxE v184,v184\n"
"xM v[136:139],a[108:111],a[12:15],v[136:139]\nxX v235,v148,v149,v235\nxX v235,v150,v151,v235\n"
"xM v[152:155],a[108:111],a[28:31],v[152:155]\nxE v185,v185\nxM v[140:143],a[112:115],a[0:3],v[192:195]\nxC v171,v174,v175\nxC v184,v184,v185\n"
"xM v[156:159],a[112:115],a[16:19],v[196:199]\nxE v186,v186\nxM v[140:143],a[116:119],a[4:7],v[140:143]\n"
"xM v[156:159],a[116:119],a[20:23],v[156:159]\nxE v187,v187\nxM v[140:143],a[120:123],a[8:11],v[140:143]\nxX v235,v136,v137,v235\nxX v235,v138,v139,v235\n"
"xM v[156:159],a[120:123],a[24:27],v[156:159]\nxW lgkmcnt(0)\nxE v188,v188\n"
"xM v[140:143],a[124:127],a[12:15],v[140:143]\nxX v235,v152,v153,v235\nxX v235,v154,v155,v235\n"
"xM v[156:159],a[124:127],a[28:31],v[156:159]\nxE v189,v189\nxM v[64:67],a[128:131],v[160:163],v[64:67]\nxW vmcnt(0)\ns_barrier\n"
"xM v[96:99],a[128:131],v[176:179],v[96:99]\nxE v190,v190\nxM v[68:71],a[132:135],v[160:163],v[68:71]\nxC v185,v186,v187\nxC v186,v188,v189\n"
"xM v[100:103],a[132:135],v[176:179],v[100:103]\nxE v191,v191\nxM v[72:75],a[136:139],v[160:163],v[72:75]\nxX v235,v140,v141,v235\nxX v235,v142,v143,v235\n"
"xM v[104:107],a[136:139],v[176:179],v[104:107]\nxX v235,v156,v157,v235\nxX v235,v158,v159,v235\n"
"xM v[76:79],a[140:143],v[160:163],v[76:79]\nv_cmp_lt_f32 vcc, s40, v235\nxC v187,v190,v191\n"
"xM v[108:111],a[140:143],v[176:179],v[108:111]\nxM v[80:83],a[144:147],v[160:163],v[80:83]\ns_cbranch_vccnz SBR31_0_%=\nSRET31_0_%=:\n"
"xM v[112:115],a[144:147],v[176:179],v[112:115]\nxE v128,v128\nxM v[84:87],a[148:151],v[160:163],v[84:87]\nxT a[128:129],v228 offset:32768\nxT a[130:131],v228 offset:36864\n"
"xM v[116:119],a[148:151],v[176:179],v[116:119]\nxE v129,v129\nxM v[88:91],a[152:155],v[160:163],v[88:91]\nxT a[132:133],v229 offset:32768\nxC v128,v128,v129\n"
"xM v[120:123],a[152:155],v[176:179],v[120:123]\nxE v130,v130\nxM v[92:95],a[156:159],v[160:163],v[92:95]\nxT a[134:135],v229 offset:36864\nxT a[136:137],v228 offset:33280\n"
"xM v[124:127],a[156:159],v[176:179],v[124:127]\nxE v131,v131\nxM v[216:219],a[192:195],v[160:163],v[216:219]\nxT a[138:139],v228 offset:37376\nxC v129,v130,v131\n"
"xM v[220:223],a[192:195],v[176:179],v[220:223]\nxE v132,v132\nxM v[64:67],a[160:163],v[168:171],v[64:67]\nxT a[140:141],v229 offset:33280\nxT a[142:143],v229 offset:37376\n"
"xM v[96:99],a[160:163],v[184:187],v[96:99]\nxE v133,v133\nxM v[68:71],a[164:167],v[168:171],v[68:71]\nxT a[144:145],v228 offset:33792\nxC v130,v132,v133\n"
"xM v[100:103],a[164:167],v[184:187],v[100:103]\nxE v134,v134\nxM v[72:75],a[168:171],v[168:171],v[72:75]\n"
"xM v[104:107],a[168:171],v[184:187],v[104:107]\nxE v135,v135\nxM v[76:79],a[172:175],v[168:171],v[76:79]\nxT a[146:147],v228 offset:37888\nxC v131,v134,v135\n"
"xM v[108:111],a[172:175],v[184:187],v[108:111]\nxE v144,v144\nxM v[80:83],a[176:179],v[168:171],v[80:83]\nxT a[148:149],v229 offset:33792\n"
"xM v[112:115],a[176:179],v[184:187],v[112:115]\nxE v145,v145\nxM v[84:87],a[180:183],v[168:171],v[84:87]\nxT a[150:151],v229 offset:37888\nxC v144,v144,v145\n"
"xM v[116:119],a[180:183],v[184:187],v[116:119]\nxE v146,v146\nxM v[88:91],a[184:187],v[168:171],v[88:91]\nxT a[152:153],v228 offset:34304\n"
"xM v[120:123],a[184:187],v[184:187],v[120:123]\nxE v147,v147\nxM v[92:95],a[188:191],v[168:171],v[92:95]\nxT a[154:155],v228 offset:38400\nxC v145,v146,v147\n"
"xM v[124:127],a[188:191],v[184:187],v[124:127]\nxE v148,v148\nxM v[216:219],a[192:195],v[168:171],v[216:219]\nxT a[156:157],v229 offset:34304\n"
"xM v[220:223],a[192:195],v[184:187],v[220:223]\nxE v149,v149\nxM v[160:163],a[64:67],a[32:35],v[200:203]\nxT a[158:159],v229 offset:38400\nxC v146,v148,v149\n"
"xM v[176:179],a[64:67],a[48:51],v[204:207]\nxE v150,v150\nxM v[160:163],a[68:71],a[36:39],v[160:163]\n"
"xM v[176:179],a[68:71],a[52:55],v[176:179]\nxE v151,v151\nxM v[160:163],a[72:75],a[40:43],v[160:163]\nxT a[160:161],v228 offset:40960\nxC v147,v150,v151\n"
"xM v[176:179],a[72:75],a[56:59],v[176:179]\nxE v136,v136\nxM v[160:163],a[76:79],a[44:47],v[160:163]\nxT a[162:163],v228 offset:45056\n"
"xM v[176:179],a[76:79],a[60:63],v[176:179]\nxE v137,v137\nxM v[164:167],a[80:83],a[32:35],v[200:203]\nxT a[164:165],v229 offset:40960\nxC v136,v136,v137\n"
"xM v[180:183],a[80:83],a[48:51],v[204:207]\nxE v138,v138\nxM v[164:167],a[84:87],a[36:39],v[164:167]\nxT a[166:167],v229 offset:45056\n"
"xM v[180:183],a[84:87],a[52:55],v[180:183]\nxE v139,v139\nxM v[164:167],a[88:91],a[40:43],v[164:167]\nxX v236,v160,v161,v160\nxX v236,v162,v163,v236\n"
"xM v[180:183],a[88:91],a[56:59],v[180:183]\nxE v140,v140\nxM v[164:167],a[92:95],a[44:47],v[164:167]\nxX v236,v176,v177,v236\nxX v236,v178,v179,v236\n"
"xM v[180:183],a[92:95],a[60:63],v[180:183]\nxE v141,v141\nxM v[168:171],a[96:99],a[32:35],v[200:203]\nxC v137,v138,v139\nxC v138,v140,v141\n"
"xM v[184:187],a[96:99],a[48:51],v[204:207]\nxE v142,v142\nxM v[168:171],a[100:103],a[36:39],v[168:171]\n"
"xM v[184:187],a[100:103],a[52:55],v[184:187]\nxE v143,v143\nxM v[168:171],a[104:107],a[40:43],v[168:171]\nxX v236,v164,v165,v236\nxX v236,v166,v167,v236\n"
"xM v[184:187],a[104:107],a[56:59],v[184:187]\nxE v152,v152\nxM v[168:171],a[108:111],a[44:47],v[168:171]\nxX v236,v180,v181,v236\nxX v236,v182,v183,v236\n"
"xM v[184:187],a[108:111],a[60:63],v[184:187]\nxE v153,v153\nxM v[172:175],a[112:115],a[32:35],v[200:203]\nxC v139,v142,v143\nxC v152,v152,v153\n"
"xM v[188:191],a[112:115],a[48:51],v[204:207]\nxE v154,v154\nxM v[172:175],a[116:119],a[36:39],v[172:175]\nxT a[168:169],v228 offset:41472\nxT a[170:171],v228 offset:45568\n"
"xM v[188:191],a[116:119],a[52:55],v[188:191]\nxE v155,v155\nxM v[172:175],a[120:123],a[40:43],v[172:175]\nxX v236,v168,v169,v236\nxX v236,v170,v171,v236\n"
"xM v[188:191],a[120:123],a[56:59],v[188:191]\nxE v156,v156\nxM v[172:175],a[124:127],a[44:47],v[172:175]\nxX v236,v184,v185,v236\nxX v236,v186,v187,v236\n"
"xM v[188:191],a[124:127],a[60:63],v[188:191]\nxW lgkmcnt(2)\nxE v157,v157\n"
"xM v[0:3],a[128:131],v[128:131],v[0:3]\nxC v153,v154,v155\nxC v154,v156,v157\n"
"xM v[32:35],a[128:131],v[144:147],v[32:35]\nxE v158,v158\nxM v[4:7],a[132:135],v[128:131],v[4:7]\nxT a[172:173],v229 offset:41472\nxT a[174:175],v229 offset:45568\n"
"xM v[36:39],a[132:135],v[144:147],v[36:39]\nxE v159,v159\nxM v[8:11],a[136:139],v[128:131],v[8:11]\nxX v236,v172,v173,v236\nxX v236,v174,v175,v236\n"
"xM v[40:43],a[136:139],v[144:147],v[40:43]\nxX v236,v188,v189,v236\nxX v236,v190,v191,v236\n"
"xM v[12:15],a[140:143],v[128:131],v[12:15]\nv_cmp_lt_f32 vcc, s40, v236\nxC v155,v158,v159\n"
"xM v[44:47],a[140:143],v[144:147],v[44:47]\nxM v[16:19],a[144:147],v[128:131],v[16:19]\ns_cbranch_vccnz SBR31_1_%=\nSRET31_1_%=:\n"
"xM v[48:51],a[144:147],v[144:147],v[48:51]\nxE v160,v160\nxM v[20:23],a[148:151],v[128:131],v[20:23]\n"
"xM v[52:55],a[148:151],v[144:147],v[52:55]\nxE v161,v161\nxM v[24:27],a[152:155],v[128:131],v[24:27]\nxT a[176:177],v228 offset:41984\nxC v160,v160,v161\n"
"xM v[56:59],a[152:155],v[144:147],v[56:59]\nxE v162,v162\nxM v[28:31],a[156:159],v[128:131],v[28:31]\nxT a[178:179],v228 offset:46080\nxT a[180:181],v229 offset:41984\n"
"xM v[60:63],a[156:159],v[144:147],v[60:63]\nxE v163,v163\nxM v[208:211],a[192:195],v[128:131],v[208:211]\nxT a[182:183],v229 offset:46080\nxC v161,v162,v163\n"
"xM v[212:215],a[192:195],v[144:147],v[212:215]\nxE v164,v164\nxM v[0:3],a[160:163],v[136:139],v[0:3]\nxT a[184:185],v228 offset:42496\nxT a[186:187],v228 offset:46592\n"
"xM v[32:35],a[160:163],v[152:155],v[32:35]\nxE v165,v165\nxM v[4:7],a[164:167],v[136:139],v[4:7]\nxT a[188:189],v229 offset:42496\nxC v162,v164,v165\n"
"xM v[36:39],a[164:167],v[152:155],v[36:39]\nxW lgkmcnt(6)\nxE v166,v166\n"
"xM v[8:11],a[168:171],v[136:139],v[8:11]\nxT a[190:191],v229 offset:46592\nxM v[40:43],a[168:171],v[152:155],v[40:43]\nxE v167,v167\n"
"xM v[12:15],a[172:175],v[136:139],v[12:15]\nxC v163,v166,v167\nxM v[44:47],a[172:175],v[152:155],v[44:47]\nxW lgkmcnt(4)\nxE v176,v176\n"
"xM v[16:19],a[176:179],v[136:139],v[16:19]\nxM v[48:51],a[176:179],v[152:155],v[48:51]\nxE v177,v177\n"
"xM v[20:23],a[180:183],v[136:139],v[20:23]\nxC v176,v176,v177\nxM v[52:55],a[180:183],v[152:155],v[52:55]\nxW lgkmcnt(1)\nxE v178,v178\n"
"xM v[24:27],a[184:187],v[136:139],v[24:27]\nxM v[56:59],a[184:187],v[152:155],v[56:59]\nxW lgkmcnt(0)\nxE v179,v179\n"
"xM v[28:31],a[188:191],v[136:139],v[28:31]\nxC v177,v178,v179\nxM v[60:63],a[188:191],v[152:155],v[60:63]\nxE v180,v180\n"
"xM v[208:211],a[192:195],v[136:139],v[208:211]\nxM v[212:215],a[192:195],v[152:155],v[212:215]\nxE v181,v181\ns_nop 0\nxC v178,v180,v181\nxE v182,v182\n"
"xE v183,v183\ns_nop 0\nxC v179,v182,v183\nxE v168,v168\nxE v169,v169\ns_nop 0\n"
"xC v168,v168,v169\nxE v170,v170\nxE v171,v171\nxE v172,v172\nxE v173,v173\nxC v169,v170,v171\n"
"xC v170,v172,v173\nxE v174,v174\nxE v175,v175\nxE v184,v184\nxE v185,v185\nxC v171,v174,v175\n"
"xC v184,v184,v185\nxE v186,v186\nxE v187,v187\nxE v188,v188\nxE v189,v189\n"
"xM v[64:67],a[128:131],v[160:163],v[64:67]\nxM v[96:99],a[128:131],v[176:179],v[96:99]\nxE v190,v190\n"
"xM v[68:71],a[132:135],v[160:163],v[68:71]\nxC v185,v186,v187\nxC v186,v188,v189\n"
"xM v[100:103],a[132:135],v[176:179],v[100:103]\nxE v191,v191\nxM v[72:75],a[136:139],v[160:163],v[72:75]\n"
"xM v[104:107],a[136:139],v[176:179],v[104:107]\nxM v[76:79],a[140:143],v[160:163],v[76:79]\nxC v187,v190,v191\n"
"xM v[108:111],a[140:143],v[176:179],v[108:111]\nxM v[80:83],a[144:147],v[160:163],v[80:83]\nxM v[112:115],a[144:147],v[176:179],v[112:115]\n"
"xM v[84:87],a[148:151],v[160:163],v[84:87]\nxM v[116:119],a[148:151],v[176:179],v[116:119]\nxM v[88:91],a[152:155],v[160:163],v[88:91]\n"
"xM v[120:123],a[152:155],v[176:179],v[120:123]\nxM v[92:95],a[156:159],v[160:163],v[92:95]\nxM v[124:127],a[156:159],v[176:179],v[124:127]\n"
"xM v[216:219],a[192:195],v[160:163],v[216:219]\nxM v[220:223],a[192:195],v[176:179],v[220:223]\nxM v[64:67],a[160:163],v[168:171],v[64:67]\n"
"xM v[96:99],a[160:163],v[184:187],v[96:99]\nxM v[68:71],a[164:167],v[168:171],v[68:71]\nxM v[100:103],a[164:167],v[184:187],v[100:103]\n"
"xM v[72:75],a[168:171],v[168:171],v[72:75]\nxM v[104:107],a[168:171],v[184:187],v[104:107]\nxM v[76:79],a[172:175],v[168:171],v[76:79]\n"
"xM v[108:111],a[172:175],v[184:187],v[108:111]\nxM v[80:83],a[176:179],v[168:171],v[80:83]\nxM v[112:115],a[176:179],v[184:187],v[112:115]\n"
"xM v[84:87],a[180:183],v[168:171],v[84:87]\nxM v[116:119],a[180:183],v[184:187],v[116:119]\nxM v[88:91],a[184:187],v[168:171],v[88:91]\n"
"xM v[120:123],a[184:187],v[184:187],v[120:123]\nxM v[92:95],a[188:191],v[168:171],v[92:95]\nxM v[124:127],a[188:191],v[184:187],v[124:127]\n"
"xM v[216:219],a[192:195],v[168:171],v[216:219]\nxM v[220:223],a[192:195],v[184:187],v[220:223]\ns_nop 7\nxW vmcnt(0) lgkmcnt(0)\ns_barrier\nxA m0,s44,0\n"
"xA s39,s42,896\nxL v248,s[24:27],s39 offen lds\nxA m0,s44,1024\nxA s39,s42,1024\nxL v248,s[24:27],s39 offen lds\nxA m0,s44,2048\n"
"xA s39,s42,10112\nxL v248,s[24:27],s39 offen lds\nxA m0,s44,3072\nxA s39,s42,10240\nxL v248,s[24:27],s39 offen lds\nxA m0,s44,16384\n"
"xA s39,s42,74624\nxL v248,s[24:27],s39 offen lds\nxA m0,s44,17408\nxA s39,s42,74752\nxL v248,s[24:27],s39 offen lds\nxA m0,s44,18432\n"
"xA s39,s42,83840\nxL v248,s[24:27],s39 offen lds\nxA m0,s44,19456\nxA s39,s42,83968\nxL v248,s[24:27],s39 offen lds\nxA m0,s44,32768\n"
"xA s39,s42,148352\nxL v248,s[24:27],s39 offen lds\nxA m0,s44,33792\nxA s39,s42,148480\nxL v248,s[24:27],s39 offen lds\nxA m0,s44,34816\n"
"xA s39,s42,157568\nxL v248,s[24:27],s39 offen lds\nxA m0,s44,35840\nxA s39,s42,157696\nxL v248,s[24:27],s39 offen lds\nxA m0,s44,49152\n"
"xA s39,s42,222080\nxL v248,s[24:27],s39 offen lds\nxA m0,s44,50176\nxA s39,s42,222208\nxL v248,s[24:27],s39 offen lds\nxA m0,s44,51200\n"
"xA s39,s42,231296\nxL v248,s[24:27],s39 offen lds\nxA m0,s44,52224\nxA s39,s42,231424\nxL v248,s[24:27],s39 offen lds\nxA m0,s44,98304\n"
"xA s39,s42,1152\nxL v248,s[24:27],s39 offen lds\nxA m0,s44,99328\nxA s39,s42,1280\nxL v248,s[24:27],s39 offen lds\nxA m0,s44,100352\n"
"xA s39,s42,10368\nxL v248,s[24:27],s39 offen lds\nxA m0,s44,101376\nxA s39,s42,10496\nxL v248,s[24:27],s39 offen lds\nxA m0,s44,114688\n"
"xA s39,s42,74880\nxL v248,s[24:27],s39 offen lds\nxA m0,s44,115712\nxA s39,s42,75008\nxL v248,s[24:27],s39 offen lds\nxA m0,s44,116736\n"
"xA s39,s42,84096\nxL v248,s[24:27],s39 offen lds\nxA m0,s44,117760\nxA s39,s42,84224\nxL v248,s[24:27],s39 offen lds\nxA m0,s44,131072\n"
"xA s39,s42,148608\nxL v248,s[24:27],s39 offen lds\nxA m0,s44,132096\nxA s39,s42,148736\nxL v248,s[24:27],s39 offen lds\nxA m0,s44,133120\n"
"xA s39,s42,157824\nxL v248,s[24:27],s39 offen lds\nxA m0,s44,134144\nxA s39,s42,157952\nxL v248,s[24:27],s39 offen lds\nxA m0,s44,147456\n"
"xA s39,s42,222336\nxL v248,s[24:27],s39 offen lds\nxA m0,s44,148480\nxA s39,s42,222464\nxL v248,s[24:27],s39 offen lds\nxA m0,s44,149504\n"
"xA s39,s42,231552\nxL v248,s[24:27],s39 offen lds\nxA m0,s44,150528\nxA s39,s42,231680\nxL v248,s[24:27],s39 offen lds\nv_lshrrev_b32 v231, 4, v255\n"
"v_mul_u32_u24 v239, 9216, v231\nv_and_b32 v232, 15, v255\nv_lshl_add_u32 v239, v232, 4, v239\ns_lshl_b32 s39, %[wid], 8\nv_add_u32 v239, s39, v239\nv_lshrrev_b32 v232, 5, v255\n"
"v_lshlrev_b32 v232, 6, v232\nv_and_b32 v231, 1, v231\nv_lshl_add_u32 v232, v231, 4, v232\nv_bfe_u32 v231, v255, 3, 1\nv_lshl_add_u32 v232, v231, 2, v232\ns_lshr_b32 s39, %[wid], 1\n"
"s_lshl_b32 s39, s39, 5\ns_and_b32 s38, %[wid], 1\ns_lshl_b32 s38, s38, 3\nxA s39,s39,s38\nv_add_u32 v232, s39, v232\nv_lshlrev_b32 v241, 2, v232\n"
"v_and_b32 v231, 7, v255\nv_mul_u32_u24 v231, 1152, v231\nv_lshl_add_u32 v240, v232, 1, v231\nv_mov_b32 v242, s68\nv_mov_b32 v243, s69\nv_lshlrev_b32 v238, 3, v255\n"
"s_min_u32 s39, %[wid], 1\ns_lshl_b32 s39, s39, 30\nv_add_u32 v238, s39, v238\ns_lshl_b32 s49, s70, 4\nxA s49,s49,s71\ns_mul_i32 s39, s49, 9216\n"
"xA s12,s72,s39\ns_addc_u32 s13, s73, 0\ns_and_b32 s13, s13, 0xffff\ns_mov_b32 s14, 9216\ns_mov_b32 s15, 0x20000\ns_mul_i32 s49, s49, 73728\n"
"xA s49,s49,7864320\ns_lshl_b32 s51, s70, 11\nxA s51,s51,1130496\ns_and_b32 s39, s71, 1\ns_lshl_b32 s39, s39, 10\nxA s50,s51,s39\n"
"s_lshr_b32 s38, s71, 1\ns_lshl_b32 s39, s38, 2\ns_lshl_b32 s54, 15, s39\nxA s39,s39,%[wid]\ns_lshl_b32 s39, s39, 3\nxA s50,s50,s39\n"
"s_xor_b32 s39, s38, s71\ns_and_b32 s39, s39, 1\ns_sub_u32 s39, s39, 1\ns_and_b32 s54, s54, s39\ns_not_b32 s54, s54\ns_and_b32 s39, s38, 1\n"
"s_lshl_b32 s39, s39, 10\nxA s51,s51,s39\ns_lshl_b32 s34, %[wid], 10\ns_min_u32 s35, s34, 2048\ns_and_b32 s43, s71, 1\ns_mul_i32 s32, %[wid], 3072\n"
"s_mul_i32 s36, %[wid], 6144\ns_mul_i32 s33, %[wid], 221184\ns_lshl_b32 s39, s43, 6\nxA s33,s33,s39\ns_mul_i32 s39, s71, 576\nxA s16,s82,s39\n"
"s_addc_u32 s17, s83, 0\ns_and_b32 s17, s17, 0xffff\ns_mov_b32 s18, 5299776\ns_mov_b32 s19, 0x20000\nxA s20,s84,s39\ns_addc_u32 s21, s85, 0\n"
"s_and_b32 s21, s21, 0xffff\ns_mov_b32 s22, 5299776\ns_mov_b32 s23, 0x20000\ns_and_b32 s38, s71, 1\ns_xor_b32 s39, s38, 1\ns_mul_i32 s45, s39, 294912\n"
"s_mul_i32 s39, %[wid], 18432\nxA s45,s45,s39\ns_lshl_b32 s46, s70, 3\nxA s46,s46,s38\ns_mul_i32 s46, s46, 147456\ns_lshr_b32 s39, s71, 1\n"
"s_mul_i32 s39, s39, 9216\nxA s46,s46,s39\ns_lshl_b32 s39, %[wid], 9\nxA s46,s46,s39\nxA s46,s46,7864320\ns_lshl_b32 s39, s70, 4\n"
"xA s39,s39,s71\ns_lshl_b32 s47, s39, 15\nxA s47,s47,102170624\ns_lshl_b32 s60, s39, 7\ns_lshl_b32 s38, %[wid], 3\nxA s60,s60,s38\n"
"xA s60,s60,1245280\ns_xor_b32 s39, s39, 1\ns_lshl_b32 s48, s39, 15\nxA s48,s48,102170624\ns_lshl_b32 s61, s39, 7\nxA s61,s61,1245184\n"
"v_bfe_u32 v231, v255, 3, 1\nv_mul_u32_u24 v231, 73728, v231\nv_and_b32 v232, 7, v255\nv_lshl_add_u32 v231, v232, 4, v231\nv_lshrrev_b32 v232, 4, v255\nv_lshl_add_u32 v249, v232, 7, v231\n"
"v_add_u32 v250, 294912, v249\nv_add_u32 v251, 589824, v249\nv_add_u32 v252, 884736, v249\nv_lshrrev_b32 v233, 5, v255\nv_lshl_add_u32 v231, v233, 7, v231\nv_and_b32 v232, 1, v232\n"
"v_mul_u32_u24 v233, 294912, v232\nv_add_u32 v253, v231, v233\nv_bfe_u32 v231, v255, 4, 1\nv_and_b32 v233, 3, v255\nv_xor_b32 v231, v231, v233\nv_lshlrev_b32 v231, 4, v231\n"
"v_and_b32 v244, 0xffffffcf, v248\nv_add_u32 v244, v244, v231\ns_mul_i32 s39, %[wid], 18432\nv_add_u32 v244, s39, v244\ns_lshl_b32 s39, %[wid], 9\ns_sub_u32 s39, 32768, s39\n"
"v_subrev_u32 v192, s39, v228\nv_xor_b32 v193, 32, v192\nv_add_u32 v194, 65536, v192\nv_add_u32 v195, 65536, v193\nv_add_u32 v196, 131072, v192\nv_add_u32 v197, 131072, v193\n"
"v_lshrrev_b32 v230, 5, v255\nv_lshlrev_b32 v198, 10, v230\nv_bfe_u32 v230, v255, 2, 3\nv_lshl_add_u32 v198, v230, 6, v198\nv_and_b32 v230, 1, v255\nv_lshl_add_u32 v198, v230, 5, v198\n"
"v_bfe_u32 v230, v255, 1, 1\nv_lshl_add_u32 v198, v230, 3, v198\ns_and_b32 s39, %[wid], 1\nv_bfe_u32 v230, v255, 4, 1\nv_xor_b32 v230, s39, v230\nv_lshl_add_u32 v198, v230, 4, v198\n"
"s_lshr_b32 s39, %[wid], 1\ns_lshl_b32 s39, s39, 9\nv_add_u32 v198, s39, v198\nv_lshlrev_b32 v199, 4, v255\nv_add_u32 v199, 65536, v199\nv_add_u32 v234, s34, v199\n"
"s_lshl_b32 s37, %[wid], 8\nv_rcp_f32 v225, v212\ns_nop 0\nxU v32,v32,v225\nxU v33,v33,v225\nxU v34,v34,v225\n"
"xU v35,v35,v225\nxU v36,v36,v225\nxU v37,v37,v225\nxU v38,v38,v225\nxU v39,v39,v225\nxC v32,v32,v33\n"
"xC v33,v34,v35\nxC v34,v36,v37\nxC v35,v38,v39\nxA s63,s47,0\nxS v[32:35],v234,s[96:99],s63 offen sc1\nxU v40,v40,v225\n"
"xU v41,v41,v225\nxU v42,v42,v225\nxU v43,v43,v225\nxU v44,v44,v225\nxU v45,v45,v225\nxU v46,v46,v225\n"
"xU v47,v47,v225\nxC v40,v40,v41\nxC v41,v42,v43\nxC v42,v44,v45\nxC v43,v46,v47\nxA s63,s47,4096\n"
"xS v[40:43],v234,s[96:99],s63 offen sc1\nxU v48,v48,v225\nxU v49,v49,v225\nxU v50,v50,v225\nxU v51,v51,v225\nxU v52,v52,v225\n"
"xU v53,v53,v225\nxU v54,v54,v225\nxU v55,v55,v225\nxC v48,v48,v49\nxC v49,v50,v51\nxC v50,v52,v53\n"
"xC v51,v54,v55\nxA s63,s47,8192\nxS v[48:51],v234,s[96:99],s63 offen sc1\nxU v56,v56,v225\nxU v57,v57,v225\nxU v58,v58,v225\n"
"xU v59,v59,v225\nxU v60,v60,v225\nxU v61,v61,v225\nxU v62,v62,v225\nxU v63,v63,v225\nxC v56,v56,v57\n"
"xC v57,v58,v59\nxC v58,v60,v61\nxC v59,v62,v63\nxA s63,s47,12288\nxS v[56:59],v234,s[96:99],s63 offen sc1\nv_rcp_f32 v224, v208\n"
"s_nop 0\nxU v0,v0,v224\nxU v1,v1,v224\nxU v2,v2,v224\nxU v3,v3,v224\nxU v4,v4,v224\n"
"xU v5,v5,v224\nxU v6,v6,v224\nxU v7,v7,v224\nxC v0,v0,v1\nxC v1,v2,v3\nxC v2,v4,v5\n"
"xC v3,v6,v7\nds_write_b128 v234, v[0:3] offset:0\nxU v8,v8,v224\nxU v9,v9,v224\nxU v10,v10,v224\nxU v11,v11,v224\n"
"xU v12,v12,v224\nxU v13,v13,v224\nxU v14,v14,v224\nxU v15,v15,v224\nxC v8,v8,v9\nxC v9,v10,v11\n"
"xC v10,v12,v13\nxC v11,v14,v15\nds_write_b128 v234, v[8:11] offset:4096\nxU v16,v16,v224\nxU v17,v17,v224\nxU v18,v18,v224\n"
"xU v19,v19,v224\nxU v20,v20,v224\nxU v21,v21,v224\nxU v22,v22,v224\nxU v23,v23,v224\nxC v16,v16,v17\n"
"xC v17,v18,v19\nxC v18,v20,v21\nxC v19,v22,v23\nds_write_b128 v234, v[16:19] offset:8192\nxU v24,v24,v224\nxU v25,v25,v224\n"
"xU v26,v26,v224\nxU v27,v27,v224\nxU v28,v28,v224\nxU v29,v29,v224\nxU v30,v30,v224\nxU v31,v31,v224\n"
"xC v24,v24,v25\nxC v25,v26,v27\nxC v26,v28,v29\nxC v27,v30,v31\nds_write_b128 v234, v[24:27] offset:12288\nv_rcp_f32 v227, v220\n"
"s_nop 0\nv_pk_mul_f32 v[96:97], v[96:97], v[226:227] op_sel:[0,1] op_sel_hi:[1,1]\nv_pk_mul_f32 v[98:99], v[98:99], v[226:227] op_sel:[0,1] op_sel_hi:[1,1]\nv_pk_mul_f32 v[100:101], v[100:101], v[226:227] op_sel:[0,1] op_sel_hi:[1,1]\nv_pk_mul_f32 v[102:103], v[102:103], v[226:227] op_sel:[0,1] op_sel_hi:[1,1]\nxC v96,v96,v97\n"
"xC v97,v98,v99\nxC v98,v100,v101\nxC v99,v102,v103\nxA s63,s47,16384\nxS v[96:99],v234,s[96:99],s63 offen sc1\nv_pk_mul_f32 v[104:105], v[104:105], v[226:227] op_sel:[0,1] op_sel_hi:[1,1]\n"
"v_pk_mul_f32 v[106:107], v[106:107], v[226:227] op_sel:[0,1] op_sel_hi:[1,1]\nv_pk_mul_f32 v[108:109], v[108:109], v[226:227] op_sel:[0,1] op_sel_hi:[1,1]\nv_pk_mul_f32 v[110:111], v[110:111], v[226:227] op_sel:[0,1] op_sel_hi:[1,1]\nxC v104,v104,v105\nxC v105,v106,v107\nxC v106,v108,v109\n"
"xC v107,v110,v111\nxA s63,s47,20480\nxS v[104:107],v234,s[96:99],s63 offen sc1\nv_pk_mul_f32 v[112:113], v[112:113], v[226:227] op_sel:[0,1] op_sel_hi:[1,1]\nv_pk_mul_f32 v[114:115], v[114:115], v[226:227] op_sel:[0,1] op_sel_hi:[1,1]\nv_pk_mul_f32 v[116:117], v[116:117], v[226:227] op_sel:[0,1] op_sel_hi:[1,1]\n"
"v_pk_mul_f32 v[118:119], v[118:119], v[226:227] op_sel:[0,1] op_sel_hi:[1,1]\nxC v112,v112,v113\nxC v113,v114,v115\nxC v114,v116,v117\nxC v115,v118,v119\nxA s63,s47,24576\n"
"xS v[112:115],v234,s[96:99],s63 offen sc1\nv_pk_mul_f32 v[120:121], v[120:121], v[226:227] op_sel:[0,1] op_sel_hi:[1,1]\nv_pk_mul_f32 v[122:123], v[122:123], v[226:227] op_sel:[0,1] op_sel_hi:[1,1]\nv_pk_mul_f32 v[124:125], v[124:125], v[226:227] op_sel:[0,1] op_sel_hi:[1,1]\nv_pk_mul_f32 v[126:127], v[126:127], v[226:227] op_sel:[0,1] op_sel_hi:[1,1]\nxC v120,v120,v121\n"
"xC v121,v122,v123\nxC v122,v124,v125\nxC v123,v126,v127\nxA s63,s47,28672\nxS v[120:123],v234,s[96:99],s63 offen sc1\nv_rcp_f32 v226, v216\n"
"s_nop 0\nv_pk_mul_f32 v[64:65], v[64:65], v[226:227] op_sel:[0,0] op_sel_hi:[1,0]\nv_pk_mul_f32 v[66:67], v[66:67], v[226:227] op_sel:[0,0] op_sel_hi:[1,0]\nv_pk_mul_f32 v[68:69], v[68:69], v[226:227] op_sel:[0,0] op_sel_hi:[1,0]\nv_pk_mul_f32 v[70:71], v[70:71], v[226:227] op_sel:[0,0] op_sel_hi:[1,0]\nxC v64,v64,v65\n"
"xC v65,v66,v67\nxC v66,v68,v69\nxC v67,v70,v71\nv_pk_mul_f32 v[72:73], v[72:73], v[226:227] op_sel:[0,0] op_sel_hi:[1,0]\nv_pk_mul_f32 v[74:75], v[74:75], v[226:227] op_sel:[0,0] op_sel_hi:[1,0]\nv_pk_mul_f32 v[76:77], v[76:77], v[226:227] op_sel:[0,0] op_sel_hi:[1,0]\n"
"v_pk_mul_f32 v[78:79], v[78:79], v[226:227] op_sel:[0,0] op_sel_hi:[1,0]\nxC v72,v72,v73\nxC v73,v74,v75\nxC v74,v76,v77\nxC v75,v78,v79\nv_pk_mul_f32 v[80:81], v[80:81], v[226:227] op_sel:[0,0] op_sel_hi:[1,0]\n"
"v_pk_mul_f32 v[82:83], v[82:83], v[226:227] op_sel:[0,0] op_sel_hi:[1,0]\nv_pk_mul_f32 v[84:85], v[84:85], v[226:227] op_sel:[0,0] op_sel_hi:[1,0]\nv_pk_mul_f32 v[86:87], v[86:87], v[226:227] op_sel:[0,0] op_sel_hi:[1,0]\nxC v80,v80,v81\nxC v81,v82,v83\nxC v82,v84,v85\n"
"xC v83,v86,v87\nv_pk_mul_f32 v[88:89], v[88:89], v[226:227] op_sel:[0,0] op_sel_hi:[1,0]\nv_pk_mul_f32 v[90:91], v[90:91], v[226:227] op_sel:[0,0] op_sel_hi:[1,0]\nv_pk_mul_f32 v[92:93], v[92:93], v[226:227] op_sel:[0,0] op_sel_hi:[1,0]\nv_pk_mul_f32 v[94:95], v[94:95], v[226:227] op_sel:[0,0] op_sel_hi:[1,0]\nxC v88,v88,v89\n"
"xC v89,v90,v91\nxC v90,v92,v93\nxC v91,v94,v95\nxW vmcnt(0) lgkmcnt(0)\ns_barrier\nxR a[64:67],v199 offset:0\n"
"xR a[68:71],v199 offset:1024\nxR a[72:75],v199 offset:2048\nxR a[76:79],v199 offset:3072\nxR a[80:83],v199 offset:4096\nxR a[84:87],v199 offset:5120\nxR a[88:91],v199 offset:6144\n"
"xR a[92:95],v199 offset:7168\nxR a[96:99],v199 offset:8192\nxR a[100:103],v199 offset:9216\nxR a[104:107],v199 offset:10240\nxR a[108:111],v199 offset:11264\nxR a[112:115],v199 offset:12288\n"
"xR a[116:119],v199 offset:13312\nxR a[120:123],v199 offset:14336\nxR a[124:127],v199 offset:15360\nxT v[144:145],v192 offset:0\nxT v[146:147],v192 offset:4096\nxT v[148:149],v193 offset:0\n"
"xT v[150:151],v193 offset:4096\nxT v[152:153],v192 offset:8192\nxT v[154:155],v192 offset:12288\nxT v[156:157],v193 offset:8192\nxT v[158:159],v193 offset:12288\nxT v[160:161],v192 offset:16384\n"
"xT v[162:163],v192 offset:20480\nxT v[164:165],v193 offset:16384\nxT v[166:167],v193 offset:20480\nxT v[168:169],v192 offset:24576\nxT v[170:171],v192 offset:28672\nxW lgkmcnt(0)\n"
"s_branch REJOIN_%=\nSBR4_0_%=:\ns_call_b64 s[62:63], RAREF0_%=\ns_branch SRET4_0_%=\nSBR4_1_%=:\ns_call_b64 s[62:63], RAREF1_%=\n"
"s_branch SRET4_1_%=\nSBR5_0_%=:\ns_call_b64 s[62:63], RAREF0_%=\ns_branch SRET5_0_%=\nSBR5_1_%=:\ns_call_b64 s[62:63], RAREF1_%=\n"
"s_branch SRET5_1_%=\nSBR6_0_%=:\ns_call_b64 s[62:63], RAREF0_%=\ns_branch SRET6_0_%=\nSBR6_1_%=:\ns_call_b64 s[62:63], RAREF1_%=\n"
"s_branch SRET6_1_%=\nSBR31_0_%=:\ns_call_b64 s[62:63], RAREF0_%=\ns_branch SRET31_0_%=\nSBR31_1_%=:\ns_call_b64 s[62:63], RAREF1_%=\n"
"s_branch SRET31_1_%=\nRAREF0_%=:\nxX v237,v128,v129,v130\nxX v237,v131,v132,v237\nxX v237,v133,v134,v237\nxX v237,v135,v136,v237\n"
"xX v237,v137,v138,v237\nxX v237,v139,v140,v237\nxX v237,v141,v142,v237\nv_max_f32 v237, v143, v237\nv_mov_b32 v238, v237\ns_nop 1\n"
"v_permlane32_swap_b32 v237, v238\nv_max_f32 v237, v237, v238\nv_mov_b32 v238, v237\ns_nop 1\nv_permlane16_swap_b32 v237, v238\nv_max_f32 v237, v237, v238\n"
"v_add_f32 v237, s41, v237\nv_max_f32 v237, 0, v237\nxE v238,-v237\nv_sub_f32 v128, v128, v237\nv_sub_f32 v129, v129, v237\nv_sub_f32 v130, v130, v237\n"
"v_sub_f32 v131, v131, v237\nv_sub_f32 v132, v132, v237\nv_sub_f32 v133, v133, v237\nv_sub_f32 v134, v134, v237\nv_sub_f32 v135, v135, v237\nv_sub_f32 v136, v136, v237\n"
"v_sub_f32 v137, v137, v237\nv_sub_f32 v138, v138, v237\nv_sub_f32 v139, v139, v237\nv_sub_f32 v140, v140, v237\nv_sub_f32 v141, v141, v237\nv_sub_f32 v142, v142, v237\n"
"v_sub_f32 v143, v143, v237\nv_sub_f32 v192, v192, v237\nv_sub_f32 v193, v193, v237\nv_sub_f32 v194, v194, v237\nv_sub_f32 v195, v195, v237\nxU v0,v0,v238\n"
"xU v1,v1,v238\nxU v2,v2,v238\nxU v3,v3,v238\nxU v4,v4,v238\nxU v5,v5,v238\nxU v6,v6,v238\n"
"xU v7,v7,v238\nxU v8,v8,v238\nxU v9,v9,v238\nxU v10,v10,v238\nxU v11,v11,v238\nxU v12,v12,v238\n"
"xU v13,v13,v238\nxU v14,v14,v238\nxU v15,v15,v238\nxU v16,v16,v238\nxU v17,v17,v238\nxU v18,v18,v238\n"
"xU v19,v19,v238\nxU v20,v20,v238\nxU v21,v21,v238\nxU v22,v22,v238\nxU v23,v23,v238\nxU v24,v24,v238\n"
"xU v25,v25,v238\nxU v26,v26,v238\nxU v27,v27,v238\nxU v28,v28,v238\nxU v29,v29,v238\nxU v30,v30,v238\n"
"xU v31,v31,v238\nxU v208,v208,v238\nxU v209,v209,v238\nxU v210,v210,v238\nxU v211,v211,v238\nxX v237,v144,v145,v146\n"
"xX v237,v147,v148,v237\nxX v237,v149,v150,v237\nxX v237,v151,v152,v237\nxX v237,v153,v154,v237\nxX v237,v155,v156,v237\nxX v237,v157,v158,v237\n"
"v_max_f32 v237, v159, v237\nv_mov_b32 v238, v237\ns_nop 1\nv_permlane32_swap_b32 v237, v238\nv_max_f32 v237, v237, v238\nv_mov_b32 v238, v237\n"
"s_nop 1\nv_permlane16_swap_b32 v237, v238\nv_max_f32 v237, v237, v238\nv_add_f32 v237, s41, v237\nv_max_f32 v237, 0, v237\nxE v238,-v237\n"
"v_sub_f32 v144, v144, v237\nv_sub_f32 v145, v145, v237\nv_sub_f32 v146, v146, v237\nv_sub_f32 v147, v147, v237\nv_sub_f32 v148, v148, v237\nv_sub_f32 v149, v149, v237\n"
"v_sub_f32 v150, v150, v237\nv_sub_f32 v151, v151, v237\nv_sub_f32 v152, v152, v237\nv_sub_f32 v153, v153, v237\nv_sub_f32 v154, v154, v237\nv_sub_f32 v155, v155, v237\n"
"v_sub_f32 v156, v156, v237\nv_sub_f32 v157, v157, v237\nv_sub_f32 v158, v158, v237\nv_sub_f32 v159, v159, v237\nv_sub_f32 v196, v196, v237\nv_sub_f32 v197, v197, v237\n"
"v_sub_f32 v198, v198, v237\nv_sub_f32 v199, v199, v237\nxU v32,v32,v238\nxU v33,v33,v238\nxU v34,v34,v238\nxU v35,v35,v238\n"
"xU v36,v36,v238\nxU v37,v37,v238\nxU v38,v38,v238\nxU v39,v39,v238\nxU v40,v40,v238\nxU v41,v41,v238\n"
"xU v42,v42,v238\nxU v43,v43,v238\nxU v44,v44,v238\nxU v45,v45,v238\nxU v46,v46,v238\nxU v47,v47,v238\n"
"xU v48,v48,v238\nxU v49,v49,v238\nxU v50,v50,v238\nxU v51,v51,v238\nxU v52,v52,v238\nxU v53,v53,v238\n"
"xU v54,v54,v238\nxU v55,v55,v238\nxU v56,v56,v238\nxU v57,v57,v238\nxU v58,v58,v238\nxU v59,v59,v238\n"
"xU v60,v60,v238\nxU v61,v61,v238\nxU v62,v62,v238\nxU v63,v63,v238\nxU v212,v212,v238\nxU v213,v213,v238\n"
"xU v214,v214,v238\nxU v215,v215,v238\ns_setpc_b64 s[62:63]\nRAREF1_%=:\nxX v237,v160,v161,v162\nxX v237,v163,v164,v237\n"
"xX v237,v165,v166,v237\nxX v237,v167,v168,v237\nxX v237,v169,v170,v237\nxX v237,v171,v172,v237\nxX v237,v173,v174,v237\nv_max_f32 v237, v175, v237\n"
"v_mov_b32 v238, v237\ns_nop 1\nv_permlane32_swap_b32 v237, v238\nv_max_f32 v237, v237, v238\nv_mov_b32 v238, v237\ns_nop 1\n"
"v_permlane16_swap_b32 v237, v238\nv_max_f32 v237, v237, v238\nv_add_f32 v237, s41, v237\nv_max_f32 v237, 0, v237\nxE v238,-v237\nv_sub_f32 v160, v160, v237\n"
"v_sub_f32 v161, v161, v237\nv_sub_f32 v162, v162, v237\nv_sub_f32 v163, v163, v237\nv_sub_f32 v164, v164, v237\nv_sub_f32 v165, v165, v237\nv_sub_f32 v166, v166, v237\n"
"v_sub_f32 v167, v167, v237\nv_sub_f32 v168, v168, v237\nv_sub_f32 v169, v169, v237\nv_sub_f32 v170, v170, v237\nv_sub_f32 v171, v171, v237\nv_sub_f32 v172, v172, v237\n"
"v_sub_f32 v173, v173, v237\nv_sub_f32 v174, v174, v237\nv_sub_f32 v175, v175, v237\nv_sub_f32 v200, v200, v237\nv_sub_f32 v201, v201, v237\nv_sub_f32 v202, v202, v237\n"
"v_sub_f32 v203, v203, v237\nxU v64,v64,v238\nxU v65,v65,v238\nxU v66,v66,v238\nxU v67,v67,v238\nxU v68,v68,v238\n"
"xU v69,v69,v238\nxU v70,v70,v238\nxU v71,v71,v238\nxU v72,v72,v238\nxU v73,v73,v238\nxU v74,v74,v238\n"
"xU v75,v75,v238\nxU v76,v76,v238\nxU v77,v77,v238\nxU v78,v78,v238\nxU v79,v79,v238\nxU v80,v80,v238\n"
"xU v81,v81,v238\nxU v82,v82,v238\nxU v83,v83,v238\nxU v84,v84,v238\nxU v85,v85,v238\nxU v86,v86,v238\n"
"xU v87,v87,v238\nxU v88,v88,v238\nxU v89,v89,v238\nxU v90,v90,v238\nxU v91,v91,v238\nxU v92,v92,v238\n"
"xU v93,v93,v238\nxU v94,v94,v238\nxU v95,v95,v238\nxU v216,v216,v238\nxU v217,v217,v238\nxU v218,v218,v238\n"
"xU v219,v219,v238\nxX v237,v176,v177,v178\nxX v237,v179,v180,v237\nxX v237,v181,v182,v237\nxX v237,v183,v184,v237\nxX v237,v185,v186,v237\n"
"xX v237,v187,v188,v237\nxX v237,v189,v190,v237\nv_max_f32 v237, v191, v237\nv_mov_b32 v238, v237\ns_nop 1\nv_permlane32_swap_b32 v237, v238\n"
"v_max_f32 v237, v237, v238\nv_mov_b32 v238, v237\ns_nop 1\nv_permlane16_swap_b32 v237, v238\nv_max_f32 v237, v237, v238\nv_add_f32 v237, s41, v237\n"
"v_max_f32 v237, 0, v237\nxE v238,-v237\nv_sub_f32 v176, v176, v237\nv_sub_f32 v177, v177, v237\nv_sub_f32 v178, v178, v237\nv_sub_f32 v179, v179, v237\n"
"v_sub_f32 v180, v180, v237\nv_sub_f32 v181, v181, v237\nv_sub_f32 v182, v182, v237\nv_sub_f32 v183, v183, v237\nv_sub_f32 v184, v184, v237\nv_sub_f32 v185, v185, v237\n"
"v_sub_f32 v186, v186, v237\nv_sub_f32 v187, v187, v237\nv_sub_f32 v188, v188, v237\nv_sub_f32 v189, v189, v237\nv_sub_f32 v190, v190, v237\nv_sub_f32 v191, v191, v237\n"
"v_sub_f32 v204, v204, v237\nv_sub_f32 v205, v205, v237\nv_sub_f32 v206, v206, v237\nv_sub_f32 v207, v207, v237\nxU v96,v96,v238\nxU v97,v97,v238\n"
"xU v98,v98,v238\nxU v99,v99,v238\nxU v100,v100,v238\nxU v101,v101,v238\nxU v102,v102,v238\nxU v103,v103,v238\n"
"xU v104,v104,v238\nxU v105,v105,v238\nxU v106,v106,v238\nxU v107,v107,v238\nxU v108,v108,v238\nxU v109,v109,v238\n"
"xU v110,v110,v238\nxU v111,v111,v238\nxU v112,v112,v238\nxU v113,v113,v238\nxU v114,v114,v238\nxU v115,v115,v238\n"
"xU v116,v116,v238\nxU v117,v117,v238\nxU v118,v118,v238\nxU v119,v119,v238\nxU v120,v120,v238\nxU v121,v121,v238\n"
"xU v122,v122,v238\nxU v123,v123,v238\nxU v124,v124,v238\nxU v125,v125,v238\nxU v126,v126,v238\nxU v127,v127,v238\n"
"xU v220,v220,v238\nxU v221,v221,v238\nxU v222,v222,v238\nxU v223,v223,v238\ns_setpc_b64 s[62:63]\nFEND_%=:\n"
".purgem xM\n"
".purgem xT\n"
".purgem xR\n"
".purgem xL\n"
".purgem xC\n"
".purgem xF\n"
".purgem xW\n"
".purgem xE\n"
".purgem xA\n"
".purgem xP\n"
".purgem xS\n"
".purgem xU\n"
".purgem xX\n"
    :: [ka] "s"(__builtin_amdgcn_kernarg_segment_ptr()), [bid] "s"(bid), [wid] "s"(wid), "s"((unsigned)(size_t)lds)
    : "v0","v1","v2","v3","v4","v5","v6","v7","v8","v9","v10","v11","v12","v13","v14","v15","v16","v17","v18","v19","v20","v21","v22","v23","v24","v25","v26","v27","v28","v29","v30","v31","v32","v33","v34","v35","v36","v37","v38","v39","v40","v41","v42","v43","v44","v45","v46","v47","v48","v49","v50","v51","v52","v53","v54","v55","v56","v57","v58","v59","v60","v61","v62","v63","v64","v65","v66","v67","v68","v69","v70","v71","v72","v73","v74","v75","v76","v77","v78","v79","v80","v81","v82","v83","v84","v85","v86","v87","v88","v89","v90","v91","v92","v93","v94","v95","v96","v97","v98","v99","v100","v101","v102","v103","v104","v105","v106","v107","v108","v109","v110","v111","v112","v113","v114","v115","v116","v117","v118","v119","v120","v121","v122","v123","v124","v125","v126","v127","v128","v129","v130","v131","v132","v133","v134","v135","v136","v137","v138","v139","v140","v141","v142","v143","v144","v145","v146","v147","v148","v149","v150","v151","v152","v153","v154","v155","v156","v157","v158","v159","v160","v161","v162","v163","v164","v165","v166","v167","v168","v169","v170","v171","v172","v173","v174","v175","v176","v177","v178","v179","v180","v181","v182","v183","v184","v185","v186","v187","v188","v189","v190","v191","v192","v193","v194","v195","v196","v197","v198","v199","v200","v201","v202","v203","v204","v205","v206","v207","v208","v209","v210","v211","v212","v213","v214","v215","v216","v217","v218","v219","v220","v221","v222","v223","v224","v225","v226","v227","v228","v229","v230","v231","v232","v233","v234","v235","v236","v237","v238","v239","v240","v241","v242","v243","v244","v245","v246","v247","v248","v249","v250","v251","v252","v253","v254","v255","a0","a1","a2","a3","a4","a5","a6","a7","a8","a9","a10","a11","a12","a13","a14","a15","a16","a17","a18","a19","a20","a21","a22","a23","a24","a25","a26","a27","a28","a29","a30","a31","a32","a33","a34","a35","a36","a37","a38","a39","a40","a41","a42","a43","a44","a45","a46","a47","a48","a49","a50","a51","a52","a53","a54","a55","a56","a57","a58","a59","a60","a61","a62","a63","a64","a65","a66","a67","a68","a69","a70","a71","a72","a73","a74","a75","a76","a77","a78","a79","a80","a81","a82","a83","a84","a85","a86","a87","a88","a89","a90","a91","a92","a93","a94","a95","a96","a97","a98","a99","a100","a101","a102","a103","a104","a105","a106","a107","a108","a109","a110","a111","a112","a113","a114","a115","a116","a117","a118","a119","a120","a121","a122","a123","a124","a125","a126","a127","a128","a129","a130","a131","a132","a133","a134","a135","a136","a137","a138","a139","a140","a141","a142","a143","a144","a145","a146","a147","a148","a149","a150","a151","a152","a153","a154","a155","a156","a157","a158","a159","a160","a161","a162","a163","a164","a165","a166","a167","a168","a169","a170","a171","a172","a173","a174","a175","a176","a177","a178","a179","a180","a181","a182","a183","a184","a185","a186","a187","a188","a189","a190","a191","a192","a193","a194","a195","a196","a197","a198","a199","a200","a201","a202","a203","a204","a205","a206","a207","a208","a209","a210","a211","a212","a213","a214","a215","a216","a217","a218","a219","a220","a221","a222","a223","a224","a225","a226","a227","a228","a229","a230","a231","a232","a233","a234","a235","a236","a237","a238","a239","a240","a241","a242","a243","a244","a245","a246","a247","a248","a249","a250","a251","a252","a253","a254","a255","s12","s13","s14","s15","s16","s17","s18","s19","s20","s21","s22","s23","s24","s25","s26","s27","s28","s29","s30","s31","s32","s33","s34","s35","s36","s37","s38","s39","s40","s41","s42","s43","s44","s45","s46","s47","s48","s49","s50","s51","s52","s53","s54","s55","s56","s57","s58","s59","s60","s61","s62","s63","s64","s65","s66","s67","s68","s69","s70","s71","s72","s73","s74","s75","s76","s77","s78","s79","s80","s81","s82","s83","s84","s85","s86","s87","s88","s89","s90","s91","s92","s93","s94","s95","s96","s97","s98","s99","s100","s101","memory","scc","vcc");
}

extern "C" void kernel_launch(void* const* d_in, const int* in_sizes, int n_in, void* d_out, int out_size, void* d_ws, size_t ws_size, hipStream_t stream) {
  k_main<<<256, 256, 0, stream>>>(d_in[0], d_in[1], d_in[2], d_in[3], d_in[4], d_in[6], d_in[7], d_in[8], d_in[9], d_in[10], d_out, d_ws);
}
```
